```python
import torch

def _softmax(x, axis=-1):
    return torch.softmax(x, dim=axis)
def _gelu(x):
    return torch.nn.functional.gelu(x, approximate="tanh")
def _silu(x):
    return torch.nn.functional.silu(x)
def _sigmoid(x):
    return torch.sigmoid(x)
def _layernorm(x, axis=-1, eps=1e-5):
    m = x.mean(dim=axis, keepdim=True)
    v = x.var(dim=axis, keepdim=True, unbiased=False)
    return (x - m) / torch.sqrt(v + eps)
def _scatter_add(tgt, idx, src, axis=0):
    return tgt.clone().index_add_(axis, idx, src)
def _segment_sum(src, seg, n):
    out = torch.zeros((n,) + src.shape[1:], dtype=src.dtype)
    return out.index_add_(0, seg, src)
def _pad(x, pw):
    flat = tuple(v for pair in reversed(pw) for v in pair)
    return torch.nn.functional.pad(x, flat)

kernel_name = 'interleave_layers__sliding_local_dilated__dense_activation_gelu__prompt'

def setup_inputs():

    _w = (lambda a: (lambda t: t.to(torch.bfloat16) if t.is_floating_point() else t)(torch.as_tensor(a)))
    def _A_setup():
        B, S, D = 1, 2048, 576
        return {
            "x": _w((lambda *s: torch.randn(*s, dtype=torch.bfloat16) * 0.05)(B, S, D)),
            "wqkv": _w((lambda *s: torch.randn(*s, dtype=torch.bfloat16) * 0.05)(D, 3 * D)),
            "wo": _w((lambda *s: torch.randn(*s, dtype=torch.bfloat16) * 0.05)(D, D)),
        }
    _A_in = _A_setup()

    def _B_setup():
        B, S, D, F = 1, 2048, 576, 4608
        return {
            "x": _w((lambda *s: torch.randn(*s, dtype=torch.bfloat16) * 0.05)(B, S, D)),
            "w1": _w((lambda *s: torch.randn(*s, dtype=torch.bfloat16) * 0.8)(D, F) * (D ** -0.5)),
            "b1": _w((lambda *s: torch.randn(*s, dtype=torch.bfloat16) * 0.8)(F) * 0.01),
            "w2": _w((lambda *s: torch.randn(*s, dtype=torch.bfloat16) * 0.8)(F, D) * (F ** -0.5)),
            "b2": _w((lambda *s: torch.randn(*s, dtype=torch.bfloat16) * 0.8)(D) * 0.01),
        }
    _B_in = _B_setup()

    return {
        "A_x": _A_in["x"],
        "A_wqkv": _A_in["wqkv"],
        "A_wo": _A_in["wo"],
        "B_x": _B_in["x"],
        "B_w1": _B_in["w1"],
        "B_b1": _B_in["b1"],
        "B_w2": _B_in["w2"],
        "B_b2": _B_in["b2"],
    }

def reference(A_x, A_wqkv, A_wo, B_x, B_w1, B_b1, B_w2, B_b2):
    def _A_ref(x, wqkv, wo):
        B, S, D = x.shape
        H = 32; Dh = D // H
        W, dil = 8, 2
        qkv = (lambda x, *d: x.permute(*d))((x @ wqkv).reshape(B, S, 3, H, Dh), 2, 0, 3, 1, 4)
        q, k, v = qkv[0], qkv[1], qkv[2]
        scale = Dh ** -0.5
        pos = torch.arange(S)
        off = torch.arange(W) * dil
        idx = pos[:, None] - off[None, :]
        mask = idx >= 0
        idx_c = torch.where(mask, idx, 0).reshape(-1)
        kg = k[:, :, idx_c, :].reshape(B, H, S, W, Dh)
        vg = v[:, :, idx_c, :].reshape(B, H, S, W, Dh)
        logits = torch.einsum("bhsd,bhswd->bhsw", q, kg) * scale
        logits = torch.where(mask[None, None, :, :], logits, -1e9)
        attn = _softmax(logits, axis=-1)
        o = torch.einsum("bhsw,bhswd->bhsd", attn, vg)
        return (lambda x, *d: x.permute(*d))(o, 0, 2, 1, 3).reshape(B, S, D) @ wo

    def _B_ref(x, w1, b1, w2, b2):
        h = torch.einsum("bsd,df->bsf", x, w1) + b1
        h = _gelu(h)
        return torch.einsum("bsf,fd->bsd", h, w2) + b2

    x = A_x
    x = x + _A_ref(_layernorm(x, axis=-1), A_wqkv, A_wo)
    x = x + _B_ref(_layernorm(x, axis=-1), B_w1, B_b1, B_w2, B_b2)
    x = x + _A_ref(_layernorm(x, axis=-1), A_wqkv, A_wo)
    x = x + _B_ref(_layernorm(x, axis=-1), B_w1, B_b1, B_w2, B_b2)
    return x
```

```cpp
#include <hip/hip_runtime.h>
#include <hip/hip_bf16.h>
typedef unsigned short u16;
__device__ __forceinline__ float bf2f(u16 b){ return __builtin_bit_cast(float, (unsigned)b<<16); }
__device__ __forceinline__ u16 f2bf(float f){ unsigned u=__builtin_bit_cast(unsigned,f); u += 0x7FFFu + ((u>>16)&1u); return (u16)(u>>16); }
constexpr int S=2048,D=576,F=4608,H=32,DH=18;
__global__ void k_ln(const u16* x, u16* y){
  int r=blockIdx.x; int t=threadIdx.x;
  float v[9]; float s=0;
  for(int i=0;i<9;i++){ v[i]=bf2f(x[r*D+t+64*i]); s+=v[i]; }
  for(int o=32;o;o>>=1) s+=__shfl_xor(s,o);
  float m=s/D; float q=0;
  for(int i=0;i<9;i++){ float d=v[i]-m; q+=d*d; }
  for(int o=32;o;o>>=1) q+=__shfl_xor(q,o);
  float rs=rsqrtf(q/D+1e-5f);
  for(int i=0;i<9;i++) y[r*D+t+64*i]=f2bf((v[i]-m)*rs);
}
template<int MODE>
__global__ void k_gemm(const u16* A,const u16* B,u16* C,const u16* bias,const u16* res,int M,int N,int K){
  __shared__ float As[16][17], Bs[16][17];
  int tx=threadIdx.x, ty=threadIdx.y;
  int r=blockIdx.y*16+ty, c=blockIdx.x*16+tx;
  float acc=0;
  for(int k0=0;k0<K;k0+=16){
    As[ty][tx]=bf2f(A[r*K+k0+tx]);
    Bs[ty][tx]=bf2f(B[(k0+ty)*N+c]);
    __syncthreads();
    for(int k=0;k<16;k++) acc+=As[ty][k]*Bs[k][tx];
    __syncthreads();
  }
  if(MODE==1){ acc+=bf2f(bias[c]); float u=0.7978845608f*(acc+0.044715f*acc*acc*acc); acc=0.5f*acc*(1.f+tanhf(u)); }
  if(MODE==2){ acc+=bf2f(res[r*N+c]); }
  if(MODE==3){ acc+=bf2f(bias[c]); acc+=bf2f(res[r*N+c]); }
  C[r*N+c]=f2bf(acc);
}
__global__ void k_attn(const u16* qkv,u16* o){
  int idx=blockIdx.x*blockDim.x+threadIdx.x; int s=idx/H, h=idx%H;
  float l[8]; float mx=-1e30f;
  for(int w=0;w<8;w++){ int p=s-2*w; float a=0;
    if(p>=0){ for(int d=0;d<DH;d++) a+=bf2f(qkv[s*3*D+h*DH+d])*bf2f(qkv[p*3*D+D+h*DH+d]); a*=0.23570226f; } else a=-1e9f;
    l[w]=a; mx=fmaxf(mx,a);}
  float den=0; for(int w=0;w<8;w++){ l[w]=__expf(l[w]-mx); den+=l[w]; }
  for(int d=0;d<DH;d++){ float a=0; for(int w=0;w<8;w++){ int p=s-2*w; if(p<0)p=0; a+=l[w]*bf2f(qkv[p*3*D+2*D+h*DH+d]); } o[s*D+h*DH+d]=f2bf(a/den); }
}
extern "C" void kernel_launch(void* const* d_in, const int* in_sizes, int n_in, void* d_out, int out_size, void* d_ws, size_t ws_size, hipStream_t stream){
  const u16* x=(const u16*)d_in[0]; const u16* wqkv=(const u16*)d_in[1]; const u16* wo=(const u16*)d_in[2];
  const u16* w1=(const u16*)d_in[4]; const u16* b1=(const u16*)d_in[5]; const u16* w2=(const u16*)d_in[6]; const u16* b2=(const u16*)d_in[7];
  u16* ws=(u16*)d_ws; u16* xn=ws; u16* qkv=xn+S*D; u16* o=qkv+S*3*D; u16* x1=o+S*D; u16* h=x1+S*D; u16* x2=h+S*F;
  const u16* cur=x;
  for(int l=0;l<2;l++){
    k_ln<<<S,64,0,stream>>>(cur,xn);
    k_gemm<0><<<dim3(3*D/16,S/16),dim3(16,16),0,stream>>>(xn,wqkv,qkv,nullptr,nullptr,S,3*D,D);
    k_attn<<<S*H/256,256,0,stream>>>(qkv,o);
    k_gemm<2><<<dim3(D/16,S/16),dim3(16,16),0,stream>>>(o,wo,x1,nullptr,cur,S,D,D);
    k_ln<<<S,64,0,stream>>>(x1,xn);
    k_gemm<1><<<dim3(F/16,S/16),dim3(16,16),0,stream>>>(xn,w1,h,b1,nullptr,S,F,D);
    u16* dst = l==0? x2 : (u16*)d_out;
    k_gemm<3><<<dim3(D/16,S/16),dim3(16,16),0,stream>>>(h,w2,dst,b2,x1,S,D,F);
    cur=x2;
  }
}
```

```cpp
#include <hip/hip_runtime.h>
#include <hip/hip_bf16.h>

namespace {
typedef unsigned short u16;
typedef unsigned v4u __attribute__((ext_vector_type(4)));
typedef unsigned v2u __attribute__((ext_vector_type(2)));
typedef float f32x4 __attribute__((ext_vector_type(4)));
typedef float f32x2 __attribute__((ext_vector_type(2)));
typedef short s16x8 __attribute__((ext_vector_type(8)));
typedef __bf16 bf16x2_t __attribute__((ext_vector_type(2)));
typedef __bf16 bf16x8_t __attribute__((ext_vector_type(8)));
#define GAS __attribute__((address_space(1)))
#define LAS __attribute__((address_space(3)))
typedef __amdgpu_buffer_rsrc_t rsrc_t;

constexpr int S = 2048, D = 576, F = 4608;
constexpr int KS = 18;
constexpr int NTHR = 512;
constexpr int XBUF = 128 * KS * 1024;
constexpr int OFF_CTRL = 0;
constexpr int OFF_WQ = 65536;
constexpr int OFF_WO = OFF_WQ + 8 * 14 * KS * 1024;
constexpr int OFF_WC = OFF_WO + 8 * 90 * 1024;
constexpr int OFF_X0 = OFF_WC + 8 * 36 * 2 * KS * 1024;
constexpr int OFF_XN = OFF_X0 + XBUF;
constexpr int OFF_O = OFF_XN + 2 * XBUF;
constexpr int OFF_X1 = OFF_O + 2 * XBUF;
constexpr int OFF_X2 = OFF_X1 + 2 * XBUF;
constexpr int OFF_P = OFF_X2 + XBUF;
constexpr int PBUF = 8 * XBUF;
constexpr int WS_BYTES = OFF_P + 2 * PBUF;

__device__ __forceinline__ f32x4 mfma16(v4u a, v4u b, f32x4 c) {
  return __builtin_amdgcn_mfma_f32_16x16x32_bf16(__builtin_bit_cast(bf16x8_t, a), __builtin_bit_cast(bf16x8_t, b), c, 0, 0, 0);
}
__device__ __forceinline__ unsigned pk2(float lo, float hi) {
  f32x2 v = {lo, hi};
  return __builtin_bit_cast(unsigned, __builtin_convertvector(v, bf16x2_t));
}
__device__ __forceinline__ float bflo(unsigned u) { return __builtin_bit_cast(float, u << 16); }
__device__ __forceinline__ float bfhi(unsigned u) { return __builtin_bit_cast(float, u & 0xffff0000u); }
__device__ __forceinline__ v2u pk4(f32x4 v) { return v2u{pk2(v.x, v.y), pk2(v.z, v.w)}; }

template <int AUX> __device__ __forceinline__ v4u ld128(rsrc_t rs, int voff, int soff) { return __builtin_amdgcn_raw_buffer_load_b128(rs, voff, soff, AUX); }
template <int AUX> __device__ __forceinline__ v2u ld64(rsrc_t rs, int voff, int soff) { return __builtin_amdgcn_raw_buffer_load_b64(rs, voff, soff, AUX); }
template <int AUX> __device__ __forceinline__ void st128(v4u v, rsrc_t rs, int voff, int soff) { __builtin_amdgcn_raw_buffer_store_b128(v, rs, voff, soff, AUX); }
template <int AUX> __device__ __forceinline__ void st64(v2u v, rsrc_t rs, int voff, int soff) { __builtin_amdgcn_raw_buffer_store_b64(v, rs, voff, soff, AUX); }
template <int AUX> __device__ __forceinline__ void st32(unsigned v, rsrc_t rs, int voff, int soff) { __builtin_amdgcn_raw_buffer_store_b32(v, rs, voff, soff, AUX); }
#define SB() __builtin_amdgcn_sched_barrier(0)

__device__ __forceinline__ float xor16(float v) { return __shfl_xor(v, 16); }
__device__ __forceinline__ float xor32(float v) { return __shfl_xor(v, 32); }

__device__ __forceinline__ void tr8x8(const v4u (&r)[8], v4u (&o)[8]) {
#pragma unroll
  for (int c = 0; c < 4; ++c) {
#pragma unroll
    for (int p = 0; p < 4; ++p) {
      o[2 * c][p] = __builtin_amdgcn_perm(r[2 * p + 1][c], r[2 * p][c], 0x05040100u);
      o[2 * c + 1][p] = __builtin_amdgcn_perm(r[2 * p + 1][c], r[2 * p][c], 0x07060302u);
    }
  }
}
__device__ __forceinline__ int wc_frag(int j, int T, int gemm, int u) {
  const int w = T < 20 ? T / 5 : 4 + (T - 20) / 4;
  const int nt = T < 20 ? 5 : 4;
  const int t = T < 20 ? T - 5 * w : (T - 20) - 4 * (w - 4);
  const int run = T < 20 ? w * 180 : 720 + (w - 4) * 144;
  return j * 1296 + run + (gemm * KS + u) * nt + t;
}
constexpr int K0_W1 = 72 * 576, K0_W2 = 576 * 72, K0_WO = 72 * 80, K0_WQ = 72 * 896;
constexpr int K0_B1 = K0_W1 / 256, K0_B2 = K0_W2 / 256, K0_BO = (K0_WO + 255) / 256, K0_BQ = K0_WQ / 256, K0_BX = 128 / 4;
constexpr int K0_BLOCKS = K0_BQ + K0_BX + K0_BO + K0_B1 + K0_B2;

__global__ void __launch_bounds__(256, 4) k_prep(const u16* __restrict__ x, const u16* __restrict__ wqkv, const u16* __restrict__ wo,
                                              const u16* __restrict__ w1, const u16* __restrict__ w2, unsigned char* __restrict__ ws) {
  int b = blockIdx.x;
  const int tid = threadIdx.x;
  if (b < K0_BQ) {
    const int i = b * 256 + tid, kg = i / 896, cp = i % 896;
    const int u = kg >> 2, gq = kg & 3;
    if (cp < 864) {
      unsigned r[8];
#pragma unroll
      for (int q = 0; q < 8; ++q) r[q] = *(const unsigned*)(wqkv + (size_t)(kg * 8 + q) * 1728 + cp * 2);
      v4u o0, o1;
#pragma unroll
      for (int p = 0; p < 4; ++p) {
        o0[p] = __builtin_amdgcn_perm(r[2 * p + 1], r[2 * p], 0x05040100u);
        o1[p] = __builtin_amdgcn_perm(r[2 * p + 1], r[2 * p], 0x07060302u);
      }
      const int type = cp / 288, hp = cp % 288, head = hp / 9, dp = hp % 9, j = head >> 2, h = head & 3;
      int blk, m;
      if (dp < 8) { blk = type * 4 + h; m = dp * 2; }
      else if (type < 2) { blk = 12; m = 4 * h + 2 * type; }
      else { blk = 13; m = 4 * h; }
      v4u* dst = (v4u*)(ws + OFF_WQ + ((size_t)((j * 14 + blk) * KS + u) * 64 + gq * 16 + m) * 16);
      dst[0] = o0;
      dst[1] = o1;
    } else {
      const int z = cp - 864, j = z >> 2, h = z & 3;
      v4u* dst = (v4u*)(ws + OFF_WQ + ((size_t)((j * 14 + 13) * KS + u) * 64 + gq * 16 + 4 * h + 2) * 16);
      dst[0] = v4u{0, 0, 0, 0};
      dst[1] = v4u{0, 0, 0, 0};
    }
    return;
  }
  b -= K0_BQ;
  if (b < K0_BX) {
    const int RB = b * 4 + (tid >> 6), lane = tid & 63, c = lane & 15, gq = lane >> 4;
    v4u v[KS];
#pragma unroll
    for (int u = 0; u < KS; ++u) v[u] = *(const v4u*)(x + (size_t)(RB * 16 + c) * D + u * 32 + gq * 8);
    float s = 0.f;
#pragma unroll
    for (int u = 0; u < KS; ++u)
#pragma unroll
      for (int p = 0; p < 4; ++p) s += bflo(v[u][p]) + bfhi(v[u][p]);
    s += xor16(s); s += xor32(s);
    const float mean = s * (1.f / D);
    float q = 0.f;
#pragma unroll
    for (int u = 0; u < KS; ++u) asm volatile("" : "+v"(v[u]));
#pragma unroll
    for (int u = 0; u < KS; ++u)
#pragma unroll
      for (int p = 0; p < 4; ++p) { const float a = bflo(v[u][p]) - mean, bb = bfhi(v[u][p]) - mean; q += a * a + bb * bb; }
    q += xor16(q); q += xor32(q);
    const float rstd = rsqrtf(q * (1.f / D) + 1e-5f);
#pragma unroll
    for (int u = 0; u < KS; ++u) asm volatile("" : "+v"(v[u]));
#pragma unroll
    for (int u = 0; u < KS; ++u) {
      v4u o;
#pragma unroll
      for (int p = 0; p < 4; ++p) o[p] = pk2((bflo(v[u][p]) - mean) * rstd, (bfhi(v[u][p]) - mean) * rstd);
      const size_t off = ((size_t)(RB * KS + u) * 64 + lane) * 16;
      *(v4u*)(ws + OFF_X0 + off) = v[u];
      *(v4u*)(ws + OFF_XN + off) = o;
    }
    return;
  }
  b -= K0_BX;
  if (b < K0_BO) {
    const int i = b * 256 + tid;
    if (i >= K0_WO) return;
    const int kg = i / 80, nb = i % 80, j = nb / 10, q8 = nb % 10, u = kg >> 2, gq = kg & 3;
    v4u o[8];
    if (q8 < 9) {
      v4u r[8];
#pragma unroll
      for (int jj = 0; jj < 8; ++jj) {
        int row;
        if (u < 16) row = (2 * u + (gq >> 1)) * 18 + (gq & 1) * 8 + jj;
        else row = (4 * ((u - 16) * 4 + gq) + (jj >> 1)) * 18 + 16 + (jj & 1);
        r[jj] = *(const v4u*)(wo + (size_t)row * D + j * 72 + q8 * 8);
      }
      tr8x8(r, o);
    } else {
#pragma unroll
      for (int c = 0; c < 8; ++c) o[c] = v4u{0, 0, 0, 0};
    }
    v4u* dst = (v4u*)(ws + OFF_WO + ((size_t)((j * KS + u) * 5 + (q8 >> 1)) * 64 + gq * 16 + (q8 & 1) * 8) * 16);
#pragma unroll
    for (int c = 0; c < 8; ++c) dst[c] = o[c];
    return;
  }
  b -= K0_BO;
  if (b < K0_B1) {
    const int i = b * 256 + tid, kg = i / 576, nb = i % 576, u = kg >> 2, gq = kg & 3;
    v4u r[8], o[8];
#pragma unroll
    for (int jj = 0; jj < 8; ++jj) r[jj] = *(const v4u*)(w1 + (size_t)(kg * 8 + jj) * F + nb * 8);
    tr8x8(r, o);
    const int j = nb / 72, q = nb % 72, T = q >> 1;
    v4u* dst = (v4u*)(ws + OFF_WC + ((size_t)wc_frag(j, T, 0, u) * 64 + gq * 16 + (q & 1) * 8) * 16);
#pragma unroll
    for (int c = 0; c < 8; ++c) dst[c] = o[c];
    return;
  }
  b -= K0_B1;
  {
    const int i = b * 256 + tid, kg = i / 72, nb = i % 72, j = kg / 72, kl = kg % 72, u = kl >> 2, gq = kl & 3;
    v4u r[8], o[8];
#pragma unroll
    for (int jj = 0; jj < 8; ++jj) r[jj] = *(const v4u*)(w2 + (size_t)(kg * 8 + jj) * D + nb * 8);
    tr8x8(r, o);
    v4u* dst = (v4u*)(ws + OFF_WC + ((size_t)wc_frag(j, nb >> 1, 1, u) * 64 + gq * 16 + (nb & 1) * 8) * 16);
#pragma unroll
    for (int c = 0; c < 8; ++c) dst[c] = o[c];
  }
}

constexpr int A_XT = 0;
constexpr int A_Q = 5 * KS * 1024;
constexpr int A_K = A_Q + 16 * 512;
constexpr int A_V = A_K + 20 * 512;
constexpr int A_EQK = A_V + 20 * 512;
constexpr int A_EV = A_EQK + 5 * 512;
constexpr int A_END = A_EV + 5 * 512;

template <int MODE>
__device__ __forceinline__ void qkv_gemm(LAS unsigned char* L, const v4u (&wa)[KS], const v4u (&wb)[KS], int h, int lane) {
  constexpr int A0 = MODE == 0 ? 0 : 1, A1 = 5;
  constexpr int B0 = MODE == 0 ? 0 : ((MODE & 1) ? 0 : 3), B1 = MODE == 0 ? 5 : ((MODE & 1) ? 3 : 5);
  constexpr bool BT = (MODE == 1 || MODE == 2);
  constexpr int R0 = A0 < B0 ? A0 : B0;
  f32x4 ac[5], bc[5];
#pragma unroll
  for (int r = 0; r < 5; ++r) { ac[r] = f32x4{0, 0, 0, 0}; bc[r] = f32x4{0, 0, 0, 0}; }
  v4u xf[2][5];
#pragma unroll
  for (int r = R0; r < 5; ++r) xf[0][r] = *(LAS v4u*)(L + A_XT + (r * KS) * 1024 + lane * 16);
#pragma unroll
  for (int u = 0; u < KS; ++u) {
    if (u + 1 < KS) {
#pragma unroll
      for (int r = R0; r < 5; ++r) xf[(u + 1) & 1][r] = *(LAS v4u*)(L + A_XT + (r * KS + u + 1) * 1024 + lane * 16);
    }
#pragma unroll
    for (int r = A0; r < A1; ++r) ac[r] = mfma16(wa[u], xf[u & 1][r], ac[r]);
#pragma unroll
    for (int r = B0; r < B1; ++r) bc[r] = BT ? mfma16(wb[u], xf[u & 1][r], bc[r]) : mfma16(xf[u & 1][r], wb[u], bc[r]);
  }
#pragma unroll
  for (int r = A0; r < A1; ++r) {
    const int off = MODE == 0 ? A_K + (h * 5 + r) * 512 : A_Q + (h * 4 + r - 1) * 512;
    *(LAS v2u*)(L + off + lane * 8) = pk4(ac[r]);
  }
#pragma unroll
  for (int r = B0; r < B1; ++r) {
    const int off = MODE == 0 ? A_V + (h * 5 + r) * 512 : (BT ? A_EQK : A_EV) + r * 512;
    *(LAS v2u*)(L + off + lane * 8) = pk4(bc[r]);
  }
}

template <int AUX>
__device__ __forceinline__ void stage_a(LAS unsigned char* L, rsrc_t rs, int layer, int g, int j, int w, int lane) {
  const int tid = w * 64 + lane;
  v4u wa[KS], wb[KS];
  {
    const int ba = w < 4 ? w : w, bb = w < 4 ? 12 + (w >> 1) : w + 4;
    const int sa = OFF_WQ + (j * 14 + ba) * KS * 1024, sb = OFF_WQ + (j * 14 + bb) * KS * 1024;
#pragma unroll
    for (int u = 0; u < KS; ++u) { wa[u] = ld128<0>(rs, lane * 16 + u * 1024, sa); wb[u] = ld128<0>(rs, lane * 16 + u * 1024, sb); }
  }
  {
    const int base = OFF_XN + layer * XBUF + (4 * g - 1) * KS * 1024;
    v4u t[12];
#pragma unroll
    for (int i = 0; i < 12; ++i) {
      const int o = i * 8192 + tid * 16;
      if (i < 11 || tid < 128) {
        t[i] = v4u{0, 0, 0, 0};
        if (g > 0 || o >= KS * 1024) t[i] = ld128<AUX>(rs, o, base);
      }
    }
#pragma unroll
    for (int i = 0; i < 12; ++i)
      if (i < 11 || tid < 128) *(LAS v4u*)(L + A_XT + i * 8192 + tid * 16) = t[i];
  }
  __syncthreads();
  if (w >= 4) qkv_gemm<0>(L, wa, wb, w - 4, lane);
  else if (w == 0) qkv_gemm<1>(L, wa, wb, w, lane);
  else if (w == 1) qkv_gemm<2>(L, wa, wb, w, lane);
  else if (w == 2) qkv_gemm<3>(L, wa, wb, w, lane);
  else qkv_gemm<4>(L, wa, wb, w, lane);
  __syncthreads();
  const int c = lane & 15, gq = lane >> 4;
  const int h = w & 3;
#pragma unroll
  for (int i = 0; i < 2; ++i) {
    const int rb = (w >> 2) * 2 + i;
    const v2u qm = *(LAS v2u*)(L + A_Q + (h * 4 + rb) * 512 + lane * 8);
    const v2u e0 = *(LAS v2u*)(L + A_EQK + rb * 512 + lane * 8);
    const v2u e1 = *(LAS v2u*)(L + A_EQK + (rb + 1) * 512 + lane * 8);
    const v2u k0 = *(LAS v2u*)(L + A_K + (h * 5 + rb) * 512 + lane * 8);
    const v2u k1 = *(LAS v2u*)(L + A_K + (h * 5 + rb + 1) * 512 + lane * 8);
    const v2u v0 = *(LAS v2u*)(L + A_V + (h * 5 + rb) * 512 + lane * 8);
    const v2u v1 = *(LAS v2u*)(L + A_V + (h * 5 + rb + 1) * 512 + lane * 8);
    const v2u x0 = *(LAS v2u*)(L + A_EV + rb * 512 + lane * 8);
    const v2u x1 = *(LAS v2u*)(L + A_EV + (rb + 1) * 512 + lane * 8);
    const bool mine = gq == h;
    const v4u qf = {qm.x, qm.y, mine ? e1.x : 0u, 0u};
    const v4u kf0 = {k0.x, k0.y, mine ? e0.y : 0u, 0u};
    const v4u kf1 = {k1.x, k1.y, mine ? e1.y : 0u, 0u};
    const f32x4 z = {0, 0, 0, 0};
    f32x4 s0 = mfma16(kf0, qf, z), s1 = mfma16(kf1, qf, z);
    const bool first = (g == 0 && rb == 0);
    float sv[8];
    bool ok[8];
#pragma unroll
    for (int r = 0; r < 4; ++r) {
      const int key = 4 * gq + r;
      const bool par = ((key ^ c) & 1) == 0;
      ok[r] = par && key >= c + 2 && !first;
      ok[4 + r] = par && key <= c;
      sv[r] = s0[r];
      sv[4 + r] = s1[r];
    }
    float m = -3.0e38f;
#pragma unroll
    for (int r = 0; r < 8; ++r) m = ok[r] ? fmaxf(m, sv[r]) : m;
    m = fmaxf(m, xor16(m));
    m = fmaxf(m, xor32(m));
    constexpr float SC = 0.23570226039551584f * 1.4426950408889634f;
    float sum = 0.f;
#pragma unroll
    for (int r = 0; r < 8; ++r) {
      sv[r] = ok[r] ? __builtin_amdgcn_exp2f((sv[r] - m) * SC) : 0.f;
      sum += sv[r];
    }
    sum += xor16(sum);
    sum += xor32(sum);
    const float inv = __builtin_amdgcn_rcpf(sum);
    const v4u pf = {pk2(sv[0] * inv, sv[1] * inv), pk2(sv[2] * inv, sv[3] * inv), pk2(sv[4] * inv, sv[5] * inv), pk2(sv[6] * inv, sv[7] * inv)};
    const v4u vf = {v0.x, v0.y, v1.x, v1.y}, xf = {x0.x, x0.y, x1.x, x1.y};
    const f32x4 om = mfma16(vf, pf, z), ox = mfma16(xf, pf, z);
    const int ob = OFF_O + layer * XBUF + (4 * g + rb) * KS * 1024;
    st64<AUX>(pk4(om), rs, (2 * j + (h >> 1)) * 1024 + ((2 * (h & 1) + (gq >> 1)) * 16 + c) * 16 + (gq & 1) * 8, ob);
    if (mine) st32<AUX>(pk2(ox.x, ox.y), rs, (16 + (j >> 2)) * 1024 + ((j & 3) * 16 + c) * 16 + h * 4, ob);
  }
}

constexpr int B_W = 0, B_RED = 90 * 1024;
template <int AUX>
__device__ __forceinline__ void stage_b(LAS unsigned char* L, rsrc_t rs, int layer, int g, int j, int w, int lane) {
  const int tid = w * 64 + lane;
  const int rb = w & 3, kh = w >> 2, c = lane & 15, gq = lane >> 4;
  {
    const int base = OFF_WO + j * 90 * 1024;
    v4u t[12];
#pragma unroll
    for (int i = 0; i < 12; ++i)
      if (i < 11 || tid < 128) t[i] = ld128<0>(rs, i * 8192 + tid * 16, base);
#pragma unroll
    for (int i = 0; i < 12; ++i)
      if (i < 11 || tid < 128) *(LAS v4u*)(L + B_W + i * 8192 + tid * 16) = t[i];
  }
  v4u of[9];
  const int ob = OFF_O + layer * XBUF + ((4 * g + rb) * KS + kh * 9) * 1024;
#pragma unroll
  for (int u = 0; u < 9; ++u) of[u] = ld128<AUX>(rs, u * 1024 + lane * 16, ob);
  const int xb = (layer == 0 ? OFF_X0 : OFF_X2) + (4 * g + rb) * KS * 1024;
  int xo[3];
  v2u res[3];
#pragma unroll
  for (int i = 0; i < 3; ++i) {
    const int t = kh * 3 + i, d0 = 72 * j + 16 * t + 4 * gq;
    xo[i] = (d0 >> 5) * 1024 + (((d0 >> 3) & 3) * 16 + c) * 16 + ((d0 >> 2) & 1) * 8;
    const bool valid = t < 4 || (t == 4 && gq < 2);
    res[i] = v2u{0, 0};
    if (valid) res[i] = ld64<AUX>(rs, xo[i], xb);
  }
  __syncthreads();
  f32x4 acc[5];
#pragma unroll
  for (int t = 0; t < 5; ++t) acc[t] = f32x4{0, 0, 0, 0};
#pragma unroll
  for (int u = 0; u < 9; ++u)
#pragma unroll
    for (int t = 0; t < 5; ++t) {
      const v4u wf = *(LAS v4u*)(L + B_W + ((kh * 9 + u) * 5 + t) * 1024 + lane * 16);
      acc[t] = mfma16(wf, of[u], acc[t]);
    }
  if (kh == 0) {
#pragma unroll
    for (int t = 3; t < 5; ++t) *(LAS f32x4*)(L + B_RED + (rb * 5 + t) * 1024 + lane * 16) = acc[t];
  } else {
#pragma unroll
    for (int t = 0; t < 3; ++t) *(LAS f32x4*)(L + B_RED + (rb * 5 + t) * 1024 + lane * 16) = acc[t];
  }
  __syncthreads();
  const int x1b = OFF_X1 + layer * XBUF + (4 * g + rb) * KS * 1024;
#pragma unroll
  for (int i = 0; i < 3; ++i) {
    const int t = kh * 3 + i;
    const bool valid = t < 4 || (t == 4 && gq < 2);
    if (valid) {
      const f32x4 o = *(LAS f32x4*)(L + B_RED + (rb * 5 + t) * 1024 + lane * 16);
      const f32x4 mine = kh == 0 ? acc[i] : acc[i < 2 ? 3 + i : 4];
      f32x4 v = o + mine;
      v.x += bflo(res[i].x); v.y += bfhi(res[i].x); v.z += bflo(res[i].y); v.w += bfhi(res[i].y);
      st64<AUX>(pk4(v), rs, xo[i], x1b);
    }
  }
}

constexpr int C_X = 0, C_H = 4 * KS * 1024, C_ST = 8 * KS * 1024, C_END = C_ST + 1024;
#define WKV 3
constexpr int WK = WKV;

template <int NT, int KS0>
__device__ __forceinline__ void c_gemm(LAS unsigned char* X, rsrc_t rs, int run, v4u (&a)[WK][5], f32x4 (&acc)[5][4], int lane) {
#pragma unroll
  for (int t = 0; t < NT; ++t)
#pragma unroll
    for (int r = 0; r < 4; ++r) acc[t][r] = f32x4{0, 0, 0, 0};
  v4u xf[2][4];
#pragma unroll
  for (int r = 0; r < 4; ++r) xf[0][r] = *(LAS v4u*)(X + (r * KS) * 1024 + lane * 16);
#pragma unroll
  for (int u = 0; u < KS; ++u) {
    if (u + 1 < KS) {
#pragma unroll
      for (int r = 0; r < 4; ++r) xf[(u + 1) & 1][r] = *(LAS v4u*)(X + (r * KS + u + 1) * 1024 + lane * 16);
    }
#pragma unroll
    for (int t = 0; t < NT; ++t) {
#pragma unroll
      for (int r = 0; r < 4; ++r) acc[t][r] = mfma16(a[(KS0 + u) % WK][t], xf[u & 1][r], acc[t][r]);
      if (KS0 + u + WK < 2 * KS) a[(KS0 + u) % WK][t] = ld128<0>(rs, lane * 16 + t * 1024, run + (KS0 + u + WK) * NT * 1024);
    }
    SB();
  }
}

template <int NT, int AUX>
__device__ __forceinline__ void c_wave(LAS unsigned char* L, rsrc_t rs, rsrc_t rb1, int layer, int g, int j, int T0, int run, v4u (&a)[WK][5], int lane) {
  const int c = lane & 15, gq = lane >> 4;
  f32x4 acc[5][4];
  v2u bias[NT];
#pragma unroll
  for (int t = 0; t < NT; ++t) bias[t] = ld64<0>(rb1, (16 * t + 4 * gq) * 2, (j * 576 + T0 * 16) * 2);
  c_gemm<NT, 0>(L + C_X, rs, run, a, acc, lane);
#pragma unroll
  for (int t = 0; t < NT; ++t) {
    const int T = T0 + t;
    const float bb[4] = {bflo(bias[t].x), bfhi(bias[t].x), bflo(bias[t].y), bfhi(bias[t].y)};
#pragma unroll
    for (int r = 0; r < 4; ++r) {
      f32x4 o;
#pragma unroll
      for (int e = 0; e < 4; ++e) {
        const float v = acc[t][r][e] + bb[e];
        const float p = __builtin_fmaf(v * v, -0.10294324f, -2.3022082f);
        const float ex = __builtin_amdgcn_exp2f(v * p);
        o[e] = v * __builtin_amdgcn_rcpf(1.0f + ex);
      }
      *(LAS v2u*)(L + C_H + (r * KS + (T >> 1)) * 1024 + ((2 * (T & 1) + (gq >> 1)) * 16 + c) * 16 + (gq & 1) * 8) = pk4(o);
    }
  }
  __syncthreads();
  c_gemm<NT, KS>(L + C_H, rs, run, a, acc, lane);
  const int pb = OFF_P + layer * PBUF + g * 64 * 9216 + j * 9216;
#pragma unroll
  for (int t = 0; t < NT; ++t)
#pragma unroll
    for (int r = 0; r < 4; ++r)
      st64<AUX>(pk4(acc[t][r]), rs, (2 * r + (c >> 3)) * 8 * 9216 + t * 256 + (c & 7) * 32 + gq * 8, pb + T0 * 256);
}

template <int AUX>
__device__ __forceinline__ void stage_c(LAS unsigned char* L, rsrc_t rs, rsrc_t rb1, int layer, int g, int j, int w, int lane) {
  const int c = lane & 15;
  const int run = OFF_WC + (j * 1296 + (w < 4 ? w * 180 : 720 + (w - 4) * 144)) * 1024;
  v4u a[WK][5];
  if (w < 4) {
#pragma unroll
    for (int s = 0; s < WK; ++s)
#pragma unroll
      for (int t = 0; t < 5; ++t) a[s][t] = ld128<0>(rs, lane * 16 + t * 1024, run + s * 5 * 1024);
  } else {
#pragma unroll
    for (int s = 0; s < WK; ++s)
#pragma unroll
      for (int t = 0; t < 4; ++t) a[s][t] = ld128<0>(rs, lane * 16 + t * 1024, run + s * 4 * 1024);
  }
  SB();
  {
    const int rb = w >> 1, uh = w & 1;
    const int xb = OFF_X1 + layer * XBUF + ((4 * g + rb) * KS + uh * 9) * 1024;
    v4u v[9];
#pragma unroll
    for (int u = 0; u < 9; ++u) v[u] = ld128<AUX>(rs, u * 1024 + lane * 16, xb);
    float s = 0.f, q = 0.f;
#pragma unroll
    for (int u = 0; u < 9; ++u)
#pragma unroll
      for (int p = 0; p < 4; ++p) {
        const float lo = bflo(v[u][p]), hi = bfhi(v[u][p]);
        s += lo + hi;
        q = __builtin_fmaf(lo, lo, q);
        q = __builtin_fmaf(hi, hi, q);
      }
    s += xor16(s); s += xor32(s);
    q += xor16(q); q += xor32(q);
    if (lane < 16) *(LAS f32x2*)(L + C_ST + (w * 16 + c) * 8) = f32x2{s, q};
    __syncthreads();
    const f32x2 o = *(LAS f32x2*)(L + C_ST + ((w ^ 1) * 16 + c) * 8);
    const float mean = (s + o.x) * (1.f / D);
    const float var = fmaxf((q + o.y) * (1.f / D) - mean * mean, 0.f);
    const float rstd = rsqrtf(var + 1e-5f), nm = -mean * rstd;
#pragma unroll
    for (int u = 0; u < 9; ++u) asm volatile("" : "+v"(v[u]));
#pragma unroll
    for (int u = 0; u < 9; ++u) {
      v4u o4;
#pragma unroll
      for (int p = 0; p < 4; ++p) o4[p] = pk2(__builtin_fmaf(bflo(v[u][p]), rstd, nm), __builtin_fmaf(bfhi(v[u][p]), rstd, nm));
      *(LAS v4u*)(L + C_X + (rb * KS + uh * 9 + u) * 1024 + lane * 16) = o4;
    }
  }
  __syncthreads();
  if (w < 4) c_wave<5, AUX>(L, rs, rb1, layer, g, j, 5 * w, run, a, lane);
  else c_wave<4, AUX>(L, rs, rb1, layer, g, j, 20 + 4 * (w - 4), run, a, lane);
}

template <int AUX>
__device__ __forceinline__ void stage_d(LAS unsigned char* L, rsrc_t rs, rsrc_t rb2, rsrc_t rout, int layer, int g, int j, int w, int lane) {
  const int tid = w * 64 + lane;
  const int pb = OFF_P + layer * PBUF + (g * 8 + j) * 8 * 9216;
  const int RB = 4 * g + (j >> 1);
  const int np = w == 0 ? 2 : 1;
  float x[2][8];
  int fo[2];
  float s = 0.f, q = 0.f;
  const int tok = (lane >> 1) & 7;
#pragma unroll
  for (int i = 0; i < 2; ++i) {
    if (i < np) {
      const int p = i * 512 + tid, T = p >> 4, hf = p & 1;
      v4u pv[8];
#pragma unroll
      for (int k = 0; k < 8; ++k) pv[k] = ld128<AUX>(rs, p * 16, pb + k * 9216);
      fo[i] = (T >> 1) * 1024 + ((2 * (T & 1) + hf) * 16 + (j & 1) * 8 + tok) * 16;
      const v4u xr = ld128<AUX>(rs, fo[i], OFF_X1 + layer * XBUF + RB * KS * 1024);
      const v4u bv = ld128<0>(rb2, (T * 16 + hf * 8) * 2, 0);
#pragma unroll
      for (int e = 0; e < 4; ++e) {
        float lo = 0.f, hi = 0.f;
#pragma unroll
        for (int k = 0; k < 8; ++k) { lo += bflo(pv[k][e]); hi += bfhi(pv[k][e]); }
        lo += bflo(bv[e]); hi += bfhi(bv[e]);
        lo += bflo(xr[e]); hi += bfhi(xr[e]);
        const unsigned r = pk2(lo, hi);
        x[i][2 * e] = bflo(r);
        x[i][2 * e + 1] = bfhi(r);
      }
      v4u o;
#pragma unroll
      for (int e = 0; e < 4; ++e) o[e] = (__builtin_bit_cast(unsigned, x[i][2 * e]) >> 16) | __builtin_bit_cast(unsigned, x[i][2 * e + 1]);
      if (layer == 0) st128<AUX>(o, rs, fo[i], OFF_X2 + RB * KS * 1024);
      else st128<0>(o, rout, ((g * 64 + j * 8 + tok) * D + T * 16 + hf * 8) * 2, 0);
#pragma unroll
      for (int e = 0; e < 8; ++e) { s += x[i][e]; q = __builtin_fmaf(x[i][e], x[i][e], q); }
    }
  }
  if (layer == 0) {
    s += __shfl_xor(s, 1); q += __shfl_xor(q, 1);
    s += xor16(s); s += xor32(s);
    q += xor16(q); q += xor32(q);
    if (lane < 16 && (lane & 1) == 0) *(LAS f32x2*)(L + (tok * 8 + w) * 8) = f32x2{s, q};
    __syncthreads();
    float ts = 0.f, tq = 0.f;
#pragma unroll
    for (int k = 0; k < 4; ++k) {
      const f32x4 v = *(LAS f32x4*)(L + tok * 64 + k * 16);
      ts += v.x + v.z;
      tq += v.y + v.w;
    }
    const float mean = ts * (1.f / D);
    const float var = fmaxf(tq * (1.f / D) - mean * mean, 0.f);
    const float rstd = rsqrtf(var + 1e-5f), nm = -mean * rstd;
#pragma unroll
    for (int i = 0; i < 2; ++i)
      if (i < np) {
        v4u o;
#pragma unroll
        for (int e = 0; e < 4; ++e) o[e] = pk2(__builtin_fmaf(x[i][2 * e], rstd, nm), __builtin_fmaf(x[i][2 * e + 1], rstd, nm));
        st128<AUX>(o, rs, fo[i], OFF_XN + XBUF + RB * KS * 1024);
      }
  }
}

#define PROLOG() \
  extern __shared__ __attribute__((aligned(16))) unsigned char smem[]; \
  LAS unsigned char* L = (LAS unsigned char*)smem; \
  const rsrc_t rs = __builtin_amdgcn_make_buffer_rsrc((void*)ws, 0, WS_BYTES, 0x00020000); \
  const int lane = threadIdx.x & 63, w = __builtin_amdgcn_readfirstlane(threadIdx.x >> 6); \
  const int j = blockIdx.x & 7, g = blockIdx.x >> 3;

__global__ void __launch_bounds__(NTHR, 2) k_a(unsigned char* ws, int layer) { PROLOG(); stage_a<0>(L, rs, layer, g, j, w, lane); }
__global__ void __launch_bounds__(NTHR, 2) k_b(unsigned char* ws, int layer) { PROLOG(); stage_b<0>(L, rs, layer, g, j, w, lane); }
__global__ void __launch_bounds__(NTHR, 2) k_c(unsigned char* ws, const u16* b1, int layer) {
  PROLOG();
  const rsrc_t rb1 = __builtin_amdgcn_make_buffer_rsrc((void*)b1, 0, F * 2, 0x00020000);
  stage_c<0>(L, rs, rb1, layer, g, j, w, lane);
}
__global__ void __launch_bounds__(NTHR, 2) k_d(unsigned char* ws, const u16* b2, u16* out, int layer) {
  PROLOG();
  const rsrc_t rb2 = __builtin_amdgcn_make_buffer_rsrc((void*)b2, 0, D * 2, 0x00020000);
  const rsrc_t rout = __builtin_amdgcn_make_buffer_rsrc((void*)out, 0, S * D * 2, 0x00020000);
  stage_d<0>(L, rs, rb2, rout, layer, g, j, w, lane);
}
}

extern "C" void kernel_launch(void* const* d_in, const int* in_sizes, int n_in, void* d_out, int out_size, void* d_ws, size_t ws_size,
                              hipStream_t stream) {
  const u16* x = (const u16*)d_in[0];
  const u16* wqkv = (const u16*)d_in[1];
  const u16* wo = (const u16*)d_in[2];
  const u16* w1 = (const u16*)d_in[4];
  const u16* b1 = (const u16*)d_in[5];
  const u16* w2 = (const u16*)d_in[6];
  const u16* b2 = (const u16*)d_in[7];
  unsigned char* ws = (unsigned char*)d_ws;
  (void)hipFuncSetAttribute((const void*)k_a, hipFuncAttributeMaxDynamicSharedMemorySize, A_END);
  (void)hipFuncSetAttribute((const void*)k_b, hipFuncAttributeMaxDynamicSharedMemorySize, B_RED + 20 * 1024);
  (void)hipFuncSetAttribute((const void*)k_c, hipFuncAttributeMaxDynamicSharedMemorySize, C_END);
  k_prep<<<K0_BLOCKS, 256, 0, stream>>>(x, wqkv, wo, w1, w2, ws);
  for (int layer = 0; layer < 2; ++layer) {
    k_a<<<256, NTHR, A_END, stream>>>(ws, layer);
    k_b<<<256, NTHR, B_RED + 20 * 1024, stream>>>(ws, layer);
    k_c<<<256, NTHR, C_END, stream>>>(ws, b1, layer);
    k_d<<<256, NTHR, 1024, stream>>>(ws, b2, (u16*)d_out, layer);
  }
}
```

```cpp
#include <hip/hip_runtime.h>
#include <hip/hip_bf16.h>

namespace {
typedef unsigned short u16;
typedef unsigned v4u __attribute__((ext_vector_type(4)));
typedef unsigned v2u __attribute__((ext_vector_type(2)));
typedef float f32x4 __attribute__((ext_vector_type(4)));
typedef float f32x2 __attribute__((ext_vector_type(2)));
typedef __bf16 bf16x2_t __attribute__((ext_vector_type(2)));
typedef __bf16 bf16x8_t __attribute__((ext_vector_type(8)));
#define GAS __attribute__((address_space(1)))
#define LAS __attribute__((address_space(3)))
typedef __amdgpu_buffer_rsrc_t rsrc_t;

constexpr int S = 2048, D = 576, F = 4608;
constexpr int KS = 18;
constexpr int NTHR = 512;
constexpr int XBUF = 128 * KS * 1024;
constexpr int OFF_CTRL = 0;
constexpr int OFF_CS = 65536;
constexpr int OFF_WQ = OFF_CS + 65536;
constexpr int OFF_WO = OFF_WQ + 8 * 14 * KS * 1024;
constexpr int OFF_WC = OFF_WO + 8 * 90 * 1024;
constexpr int OFF_X0 = OFF_WC + 8 * 36 * 2 * KS * 1024;
constexpr int OFF_XN = OFF_X0 + XBUF;
constexpr int OFF_O = OFF_XN + 2 * XBUF;
constexpr int OFF_X1 = OFF_O + 2 * XBUF;
constexpr int OFF_X2 = OFF_X1 + 2 * XBUF;
constexpr int OFF_P = OFF_X2 + XBUF;
constexpr int PBUF = 8 * XBUF;
constexpr int WS_BYTES = OFF_P + 2 * PBUF;

__device__ __forceinline__ f32x4 mfma16(v4u a, v4u b, f32x4 c) {
  return __builtin_amdgcn_mfma_f32_16x16x32_bf16(__builtin_bit_cast(bf16x8_t, a), __builtin_bit_cast(bf16x8_t, b), c, 0, 0, 0);
}
__device__ __forceinline__ unsigned pk2(float lo, float hi) {
  f32x2 v = {lo, hi};
  return __builtin_bit_cast(unsigned, __builtin_convertvector(v, bf16x2_t));
}
__device__ __forceinline__ float asf(unsigned u) { return __builtin_bit_cast(float, u); }
__device__ __forceinline__ unsigned asu(float f) { return __builtin_bit_cast(unsigned, f); }
__device__ __forceinline__ float bflo(unsigned u) { return asf(u << 16); }
__device__ __forceinline__ float bfhi(unsigned u) { return asf(u & 0xffff0000u); }
__device__ __forceinline__ v2u pk4(f32x4 v) { return v2u{pk2(v.x, v.y), pk2(v.z, v.w)}; }
__device__ __forceinline__ float dot2(unsigned a, unsigned b, float c) {
  return __builtin_amdgcn_fdot2_f32_bf16(__builtin_bit_cast(bf16x2_t, a), __builtin_bit_cast(bf16x2_t, b), c, false);
}
constexpr unsigned ONES = 0x3f803f80u;

template <int AUX> __device__ __forceinline__ v4u ld128(rsrc_t rs, int voff, int soff) { return __builtin_amdgcn_raw_buffer_load_b128(rs, voff, soff, AUX); }
template <int AUX> __device__ __forceinline__ v2u ld64(rsrc_t rs, int voff, int soff) { return __builtin_amdgcn_raw_buffer_load_b64(rs, voff, soff, AUX); }
template <int AUX> __device__ __forceinline__ void st128(v4u v, rsrc_t rs, int voff, int soff) { __builtin_amdgcn_raw_buffer_store_b128(v, rs, voff, soff, AUX); }
template <int AUX> __device__ __forceinline__ void st64(v2u v, rsrc_t rs, int voff, int soff) { __builtin_amdgcn_raw_buffer_store_b64(v, rs, voff, soff, AUX); }
template <int AUX> __device__ __forceinline__ void st32(unsigned v, rsrc_t rs, int voff, int soff) { __builtin_amdgcn_raw_buffer_store_b32(v, rs, voff, soff, AUX); }
#define SB() __builtin_amdgcn_sched_barrier(0)

__device__ __forceinline__ float sum32(float v) {
  const auto b = __builtin_amdgcn_permlane32_swap(asu(v), asu(v), false, false);
  const unsigned b0 = b[0], b1 = b[1];
  return asf(b0) + asf(b1);
}
__device__ __forceinline__ float sum1632(float v) {
  const auto a = __builtin_amdgcn_permlane16_swap(asu(v), asu(v), false, false);
  const unsigned a0 = a[0], a1 = a[1];
  return sum32(asf(a0) + asf(a1));
}
__device__ __forceinline__ float max1632(float v) {
  const auto a = __builtin_amdgcn_permlane16_swap(asu(v), asu(v), false, false);
  const unsigned a0 = a[0], a1 = a[1];
  v = fmaxf(asf(a0), asf(a1));
  const auto b = __builtin_amdgcn_permlane32_swap(asu(v), asu(v), false, false);
  const unsigned b0 = b[0], b1 = b[1];
  return fmaxf(asf(b0), asf(b1));
}

__device__ __forceinline__ void tr8x8(const v4u (&r)[8], v4u (&o)[8]) {
#pragma unroll
  for (int c = 0; c < 4; ++c) {
#pragma unroll
    for (int p = 0; p < 4; ++p) {
      o[2 * c][p] = __builtin_amdgcn_perm(r[2 * p + 1][c], r[2 * p][c], 0x05040100u);
      o[2 * c + 1][p] = __builtin_amdgcn_perm(r[2 * p + 1][c], r[2 * p][c], 0x07060302u);
    }
  }
}
__device__ __forceinline__ int wc_run(int w) { return w < 4 ? w * 180 : 720 + (w - 4) * 144; }
__device__ __forceinline__ int wc_frag1(int j, int T, int u) {
  const int w = T < 20 ? T / 5 : 4 + (T - 20) / 4;
  const int nt = T < 20 ? 5 : 4;
  const int t = T < 20 ? T - 5 * w : (T - 20) - 4 * (w - 4);
  return j * 1296 + wc_run(w) + u * nt + t;
}
__device__ __forceinline__ int wc_frag2(int j, int T, int u) {
  int w, o;
  if (T < 12) { w = T / 3; o = 90 + u * 3 + (T - 3 * w); }
  else if (T < 20) { w = 4 + ((T - 12) >> 1); o = 72 + u * 2 + (T & 1); }
  else { w = (T - 20) >> 1; o = (w < 4 ? 144 : 108) + u * 2 + (T & 1); }
  return j * 1296 + wc_run(w) + o;
}
constexpr int K0_WO = 72 * 80, K0_WQ = 72 * 896;
constexpr int K0_B1 = 216, K0_B2 = 216, K0_BO = (K0_WO + 255) / 256, K0_BQ = K0_WQ / 256, K0_BX = 128 / 2;
constexpr int K0_BLOCKS = K0_BQ + K0_BX + K0_BO + K0_B1 + K0_B2;

__global__ void __launch_bounds__(256, 4) k_prep(const u16* __restrict__ x, const u16* __restrict__ wqkv, const u16* __restrict__ wo,
                                                 const u16* __restrict__ w1, const u16* __restrict__ w2, unsigned char* __restrict__ ws) {
  __shared__ __attribute__((aligned(16))) unsigned char sm[24 * 1024];
  int b = blockIdx.x;
  const int tid = threadIdx.x;
  if (b < 16) ((v4u*)(ws + OFF_CTRL))[b * 256 + tid] = v4u{0, 0, 0, 0};
  if (b < K0_B1 + K0_B2) {
    const bool is1 = b < K0_B1;
    const int bb = is1 ? b : b - K0_B1;
    const int strip = is1 ? bb / 3 : bb % 9, chunk = is1 ? bb % 3 : bb / 9;
    const u16* src = is1 ? w1 + (size_t)chunk * 192 * F + strip * 64 : w2 + (size_t)chunk * 192 * D + strip * 64;
    const int ld = is1 ? F : D;
    if (tid < 192) {
      const int kg = tid >> 3, nb = tid & 7;
      v4u r[8], o[8];
#pragma unroll
      for (int jj = 0; jj < 8; ++jj) r[jj] = *(const v4u*)(src + (size_t)(kg * 8 + jj) * ld + nb * 8);
      tr8x8(r, o);
      const int base = (((nb >> 1) * 6 + (kg >> 2)) * 64 + (kg & 3) * 16 + (nb & 1) * 8) * 16;
#pragma unroll
      for (int c = 0; c < 8; ++c) *(v4u*)(sm + base + ((c ^ nb) * 16)) = o[c];
    }
    __syncthreads();
    const int tl = tid >> 6, lane = tid & 63;
    const int nbr = tl * 2 + ((lane >> 3) & 1);
    const int j = is1 ? strip / 9 : chunk / 3, T = is1 ? (strip % 9) * 4 + tl : strip * 4 + tl, u0 = is1 ? chunk * 6 : (chunk % 3) * 6;
    float s = 0.f;
#pragma unroll
    for (int u = 0; u < 6; ++u) {
      const v4u v = *(const v4u*)(sm + ((tl * 6 + u) * 64 + (lane & ~7) + ((lane & 7) ^ nbr)) * 16);
      const int fr = is1 ? wc_frag1(j, T, u0 + u) : wc_frag2(j, T, u0 + u);
      *(v4u*)(ws + OFF_WC + (size_t)fr * 1024 + lane * 16) = v;
#pragma unroll
      for (int p = 0; p < 4; ++p) s = dot2(v[p], ONES, s);
    }
    if (is1) {
      s = sum1632(s);
      if (lane < 16) ((float*)(ws + OFF_CS))[chunk * F + strip * 64 + tl * 16 + lane] = s;
    }
    return;
  }
  b -= K0_B1 + K0_B2;
  if (b < K0_BQ) {
    const int i = b * 256 + tid, kg = i / 896, cp = i % 896;
    const int u = kg >> 2, gq = kg & 3;
    if (cp < 864) {
      unsigned r[8];
#pragma unroll
      for (int q = 0; q < 8; ++q) r[q] = *(const unsigned*)(wqkv + (size_t)(kg * 8 + q) * 1728 + cp * 2);
      v4u o0, o1;
#pragma unroll
      for (int p = 0; p < 4; ++p) {
        o0[p] = __builtin_amdgcn_perm(r[2 * p + 1], r[2 * p], 0x05040100u);
        o1[p] = __builtin_amdgcn_perm(r[2 * p + 1], r[2 * p], 0x07060302u);
      }
      const int type = cp / 288, hp = cp % 288, head = hp / 9, dp = hp % 9, j = head >> 2, h = head & 3;
      int blk, m;
      if (dp < 8) { blk = type * 4 + h; m = dp * 2; }
      else if (type < 2) { blk = 12; m = 4 * h + 2 * type; }
      else { blk = 13; m = 4 * h; }
      v4u* dst = (v4u*)(ws + OFF_WQ + ((size_t)((j * 14 + blk) * KS + u) * 64 + gq * 16 + m) * 16);
      dst[0] = o0;
      dst[1] = o1;
    } else {
      const int z = cp - 864, j = z >> 2, h = z & 3;
      v4u* dst = (v4u*)(ws + OFF_WQ + ((size_t)((j * 14 + 13) * KS + u) * 64 + gq * 16 + 4 * h + 2) * 16);
      dst[0] = v4u{0, 0, 0, 0};
      dst[1] = v4u{0, 0, 0, 0};
    }
    return;
  }
  b -= K0_BQ;
  if (b < K0_BX) {
    const int wv = tid >> 6, RB = b * 2 + (wv >> 1), uh = wv & 1, lane = tid & 63, c = lane & 15, gq = lane >> 4;
    v4u v[9];
#pragma unroll
    for (int u = 0; u < 9; ++u) v[u] = *(const v4u*)(x + (size_t)(RB * 16 + c) * D + (uh * 9 + u) * 32 + gq * 8);
    float s = 0.f, q = 0.f;
#pragma unroll
    for (int u = 0; u < 9; ++u) {
      *(v4u*)(ws + OFF_X0 + ((size_t)(RB * KS + uh * 9 + u) * 64 + lane) * 16) = v[u];
#pragma unroll
      for (int p = 0; p < 4; ++p) { s = dot2(v[u][p], ONES, s); q = dot2(v[u][p], v[u][p], q); }
    }
    s = sum1632(s);
    q = sum1632(q);
    if (lane < 16) *(f32x2*)(sm + (wv * 16 + c) * 8) = f32x2{s, q};
    __syncthreads();
    const f32x2 o2 = *(const f32x2*)(sm + ((wv ^ 1) * 16 + c) * 8);
    const float mean = (s + o2.x) * (1.f / D);
    const float var = fmaxf((q + o2.y) * (1.f / D) - mean * mean, 0.f);
    const float rstd = rsqrtf(var + 1e-5f), nm = -mean * rstd;
#pragma unroll
    for (int u = 0; u < 9; ++u) {
      v4u o;
#pragma unroll
      for (int p = 0; p < 4; ++p) o[p] = pk2(__builtin_fmaf(bflo(v[u][p]), rstd, nm), __builtin_fmaf(bfhi(v[u][p]), rstd, nm));
      *(v4u*)(ws + OFF_XN + ((size_t)(RB * KS + uh * 9 + u) * 64 + lane) * 16) = o;
    }
    return;
  }
  b -= K0_BX;
  {
    const int i = b * 256 + tid;
    if (i >= K0_WO) return;
    const int kg = i / 80, nb = i % 80, j = nb / 10, q8 = nb % 10, u = kg >> 2, gq = kg & 3;
    v4u o[8];
    if (q8 < 9) {
      v4u r[8];
#pragma unroll
      for (int jj = 0; jj < 8; ++jj) {
        int row;
        if (u < 16) row = (2 * u + (gq >> 1)) * 18 + (gq & 1) * 8 + jj;
        else row = (4 * ((u - 16) * 4 + gq) + (jj >> 1)) * 18 + 16 + (jj & 1);
        r[jj] = *(const v4u*)(wo + (size_t)row * D + j * 72 + q8 * 8);
      }
      tr8x8(r, o);
    } else {
#pragma unroll
      for (int c = 0; c < 8; ++c) o[c] = v4u{0, 0, 0, 0};
    }
    v4u* dst = (v4u*)(ws + OFF_WO + ((size_t)((j * KS + u) * 5 + (q8 >> 1)) * 64 + gq * 16 + (q8 & 1) * 8) * 16);
#pragma unroll
    for (int c = 0; c < 8; ++c) dst[c] = o[c];
  }
}

#define RA 4
#define RING 20
constexpr int R = RING;
constexpr int SC1 = 16;
constexpr int A_XT = 0;
constexpr int A_Q = 5 * KS * 1024;
constexpr int A_K = A_Q + 16 * 512;
constexpr int A_V = A_K + 20 * 512;
constexpr int A_EQK = A_V + 20 * 512;
constexpr int A_EV = A_EQK + 5 * 512;
constexpr int B_W = 0, B_RED = 124 * 1024;
constexpr int C_X = 0, C_H = 4 * KS * 1024, C_ST = 8 * KS * 1024;
constexpr int D_ST = C_ST + 1024;
constexpr int L_CNT = D_ST + 1024;
constexpr int L_STAMP = L_CNT + 64;
constexpr int LDS_BYTES = L_STAMP;
#define STAMP(X_, slot) do {} while (0)
enum { CNT_A = 0, CNT_B = 1, CNT_C1 = 2, CNT_C2 = 3, CNT_D = 4 };
typedef GAS unsigned gu32;
__device__ __forceinline__ gu32* cnt_ptr(unsigned char* ws, int kind, int layer, int g) { return (gu32*)(ws + OFF_CTRL) + ((kind * 2 + layer) * 32 + g) * 32; }
#define RLX_AGENT __ATOMIC_RELAXED, __HIP_MEMORY_SCOPE_AGENT
#define BAR() do { SB(); asm volatile("s_waitcnt lgkmcnt(0)\n\ts_barrier" ::: "memory"); SB(); } while (0)
template <int N> __device__ __forceinline__ void drain() { SB(); asm volatile("s_waitcnt vmcnt(%0)" ::"i"(N) : "memory"); SB(); }
__device__ __forceinline__ void poll_ge(gu32* p, unsigned want) {
#pragma unroll 1
  for (unsigned spins = 0; spins < (1u << 22); ++spins) {
    const unsigned v = __builtin_amdgcn_readfirstlane(__hip_atomic_load(p, RLX_AGENT));
    if (v >= want) break;
    __builtin_amdgcn_s_sleep(1);
  }
  __builtin_amdgcn_fence(__ATOMIC_ACQUIRE, "wavefront");
  asm volatile("" ::: "memory");
}
__device__ __forceinline__ void signal(gu32* p, int lane) {
  if (lane == 0) __hip_atomic_fetch_add(p, 1u, RLX_AGENT);
}

struct Ctx {
  unsigned char* ws;
  LAS unsigned char* L;
  rsrc_t rs, rb1, rb2, rout;
  int g, j, w, lane;
};
__device__ __forceinline__ Ctx fresh(const Ctx& X) {
  Ctx Y = X;
  asm volatile("" : "+v"(Y.lane));
  asm volatile("" : "+s"(Y.g), "+s"(Y.j), "+s"(Y.w));
  return Y;
}

template <int HI, int U0, int U1>
__device__ __forceinline__ void wq_prefetch(const Ctx& X, v4u (&wa)[KS], v4u (&wb)[KS]) {
  const int ba = HI ? X.w + 4 : X.w, bb = HI ? 12 + ((X.w - 4) >> 1) : X.w + 4;
  const int sa = OFF_WQ + (X.j * 14 + ba) * KS * 1024, sb = OFF_WQ + (X.j * 14 + bb) * KS * 1024;
#pragma unroll
  for (int u = U0; u < U1; ++u) {
    wa[u] = ld128<0>(X.rs, X.lane * 16 + (u & 3) * 1024, sa + (u >> 2) * 4096);
    wb[u] = ld128<0>(X.rs, X.lane * 16 + (u & 3) * 1024, sb + (u >> 2) * 4096);
  }
  SB();
}

template <int HI>
__device__ __forceinline__ void qkv_gemm(LAS unsigned char* L, const v4u (&wa)[KS], const v4u (&wb)[KS], int w, int lane) {
  constexpr int A0 = HI ? 0 : 1, B1 = HI ? 3 : 5;
  const int h = w & 3;
  f32x4 ac[5], bc[5];
#pragma unroll
  for (int r = 0; r < 5; ++r) { ac[r] = f32x4{0, 0, 0, 0}; bc[r] = f32x4{0, 0, 0, 0}; }
  v4u xf[2][5];
  const int rot = HI ? (w & 1) * 2 : 0;
  LAS unsigned char* xa = L + A_XT + lane * 16 + rot * KS * 1024;
  LAS unsigned char* xb = L + A_XT + lane * 16 + (rot ? 0 : 3) * KS * 1024;
#define XF(r, u) (*(LAS v4u*)(((r) < 3 ? xa : xb) + (((r) < 3 ? (r) : (r) - 3) * KS + (u)) * 1024))
#pragma unroll
  for (int r = 0; r < 5; ++r) xf[0][r] = XF(r, 0);
#pragma unroll
  for (int r = 0; r < 5; ++r) xf[1][r] = XF(r, 1);
  SB();
#pragma unroll
  for (int u = 0; u < KS; ++u) {
#pragma unroll
    for (int r = 0; r < 5; ++r) {
      if (r >= A0) ac[r] = HI ? mfma16(xf[u & 1][r], wa[u], ac[r]) : mfma16(wa[u], xf[u & 1][r], ac[r]);
      if (r < B1) bc[r] = mfma16(wb[u], xf[u & 1][r], bc[r]);
      if (u + 2 < KS) xf[u & 1][r] = XF(r, u + 2);
      SB();
    }
  }
#undef XF
  if (!HI) {
#pragma unroll
    for (int r = 1; r < 5; ++r) *(LAS v2u*)(L + A_Q + (h * 4 + r - 1) * 512 + lane * 8) = pk4(ac[r]);
#pragma unroll
    for (int r = 0; r < 5; ++r) *(LAS v2u*)(L + A_K + (h * 5 + r) * 512 + lane * 8) = pk4(bc[r]);
  } else {
#pragma unroll
    for (int r = 0; r < 5; ++r) *(LAS v2u*)(L + A_V + (h * 5 + (r < 3 ? rot + r : (rot ? 0 : 3) + r - 3)) * 512 + lane * 8) = pk4(ac[r]);
    if (w < 6) {
#pragma unroll
      for (int r = 0; r < 3; ++r) *(LAS v2u*)(L + A_EQK + (rot + r) * 512 + lane * 8) = pk4(bc[r]);
    } else {
      const int c = lane & 15, gq = lane >> 4;
#pragma unroll
      for (int r = 0; r < 3; ++r) {
        const v2u v = pk4(bc[r]);
        LAS u16* p = (LAS u16*)(L + A_EV + (rot + r) * 512 + ((c >> 2) * 16 + 4 * gq) * 8 + (c & 3) * 2);
        p[0] = (u16)v.x;
        p[4] = (u16)(v.x >> 16);
        p[8] = (u16)v.y;
        p[12] = (u16)(v.y >> 16);
      }
    }
  }
}

template <int HI>
__device__ __forceinline__ void xn_load(const Ctx& X, int layer, v4u (&t)[12]) {
  const int tid = X.w * 64 + X.lane;
  const int base = OFF_XN + layer * XBUF + (4 * X.g - 1) * KS * 1024;
#pragma unroll
  for (int i = 0; i < 12; ++i) {
    t[i] = v4u{0, 0, 0, 0};
    if (i < 11 || (HI == 0 && X.w < 2)) {
      if (X.g > 0 || i > 2 || i * 8192 + X.w * 1024 >= KS * 1024) t[i] = ld128<SC1>(X.rs, tid * 16, base + i * 8192);
    }
  }
  SB();
}

template <int LO, int HIX>
__device__ __forceinline__ void ring_fill(const Ctx& X, int run, v4u (&a)[R]) {
#pragma unroll
  for (int i = LO; i < HIX; ++i) a[i] = ld128<0>(X.rs, X.lane * 16 + (i & 3) * 1024, run + (i >> 2) * 4096);
  SB();
}

template <int HI>
__device__ __forceinline__ void stage_a(const Ctx& X, int layer, const v4u (&t)[12], const v4u (&wa)[KS], const v4u (&wb)[KS]) {
  LAS unsigned char* L = X.L;
  const rsrc_t rs = X.rs;
  const int g = X.g, j = X.j, w = X.w, lane = X.lane, tid = w * 64 + lane;
#pragma unroll
  for (int i = 0; i < 12; ++i)
    if (i < 11 || (HI == 0 && w < 2)) *(LAS v4u*)(L + A_XT + i * 8192 + tid * 16) = t[i];
  STAMP(X, layer * 32 + 1);
  BAR();
  STAMP(X, layer * 32 + 2);
  qkv_gemm<HI>(L, wa, wb, w, lane);
  STAMP(X, layer * 32 + 3);
  BAR();
  STAMP(X, layer * 32 + 4);
  const int c = lane & 15, gq = lane >> 4;
  const int h = w & 3;
#pragma unroll
  for (int i = 0; i < 2; ++i) {
    const int rb = HI * 2 + i;
    const v2u qm = *(LAS v2u*)(L + A_Q + (h * 4 + rb) * 512 + lane * 8);
    const v2u e0 = *(LAS v2u*)(L + A_EQK + rb * 512 + lane * 8);
    const v2u e1 = *(LAS v2u*)(L + A_EQK + (rb + 1) * 512 + lane * 8);
    const v2u k0 = *(LAS v2u*)(L + A_K + (h * 5 + rb) * 512 + lane * 8);
    const v2u k1 = *(LAS v2u*)(L + A_K + (h * 5 + rb + 1) * 512 + lane * 8);
    const v2u v0 = *(LAS v2u*)(L + A_V + (h * 5 + rb) * 512 + lane * 8);
    const v2u v1 = *(LAS v2u*)(L + A_V + (h * 5 + rb + 1) * 512 + lane * 8);
    const v2u x0 = *(LAS v2u*)(L + A_EV + rb * 512 + lane * 8);
    const v2u x1 = *(LAS v2u*)(L + A_EV + (rb + 1) * 512 + lane * 8);
    const bool mine = gq == h;
    const v4u qf = {qm.x, qm.y, mine ? e1.x : 0u, 0u};
    const v4u kf0 = {k0.x, k0.y, mine ? e0.y : 0u, 0u};
    const v4u kf1 = {k1.x, k1.y, mine ? e1.y : 0u, 0u};
    const f32x4 z = {0, 0, 0, 0};
    const f32x4 s0 = mfma16(kf0, qf, z), s1 = mfma16(kf1, qf, z);
    const bool first = (g == 0 && rb == 0);
    const int par = c & 1;
    float sv[2];
    bool own[2], ok[2];
#pragma unroll
    for (int e = 0; e < 2; ++e) {
      const int key = 4 * gq + par + 2 * e;
      own[e] = key <= c;
      ok[e] = own[e] || !first;
      const float a0 = par ? s0[2 * e + 1] : s0[2 * e], a1 = par ? s1[2 * e + 1] : s1[2 * e];
      sv[e] = own[e] ? a1 : a0;
    }
    float m = fmaxf(ok[0] ? sv[0] : -3.0e38f, ok[1] ? sv[1] : -3.0e38f);
    m = max1632(m);
    constexpr float SC = 0.23570226039551584f * 1.4426950408889634f;
    float p0 = ok[0] ? __builtin_amdgcn_exp2f((sv[0] - m) * SC) : 0.f;
    float p1 = ok[1] ? __builtin_amdgcn_exp2f((sv[1] - m) * SC) : 0.f;
    const float inv = __builtin_amdgcn_rcpf(sum1632(p0 + p1));
    const unsigned pp = pk2(p0 * inv, p1 * inv);
    const unsigned d0 = par ? pp << 16 : pp & 0xffffu, d1 = par ? pp & 0xffff0000u : pp >> 16;
    const v4u pf = {own[0] ? 0u : d0, own[1] ? 0u : d1, own[0] ? d0 : 0u, own[1] ? d1 : 0u};
    const v4u vf = {v0.x, v0.y, v1.x, v1.y}, xf = {x0.x, x0.y, x1.x, x1.y};
    const f32x4 om = mfma16(vf, pf, z), ox = mfma16(xf, pf, z);
    const int ob = OFF_O + layer * XBUF + (4 * g + rb) * KS * 1024;
    st64<SC1>(pk4(om), rs, (2 * j + (h >> 1)) * 1024 + ((2 * (h & 1) + (gq >> 1)) * 16 + c) * 16 + (gq & 1) * 8, ob);
    if (mine) st32<SC1>(pk2(ox.x, ox.y), rs, (16 + (j >> 2)) * 1024 + ((j & 3) * 16 + c) * 16 + h * 4, ob);
  }
  STAMP(X, layer * 32 + 5);
  drain<0>();
  BAR();
  if (HI && w == 7) signal(cnt_ptr(X.ws, CNT_A, layer, g), lane);
  STAMP(X, layer * 32 + 6);
}

template <int HI>
__device__ __forceinline__ void stage_b(const Ctx& X, int layer, int run, v4u (&a)[R]) {
  LAS unsigned char* L = X.L;
  const rsrc_t rs = X.rs;
  const int g = X.g, j = X.j, w = X.w, lane = X.lane;
  constexpr int kh = HI, NK = HI ? 2 : 3;
  const int rb = w & 3, c = lane & 15, gq = lane >> 4;
  const bool poller = HI && w == 7;
  if (!poller) {
    v4u wt[13];
#pragma unroll
    for (int i = 0; i < 13; ++i) wt[i] = ld128<0>(rs, lane * 16, OFF_WO + (j * 90 + i * 7 + w) * 1024);
#pragma unroll
    for (int i = 0; i < 13; ++i) *(LAS v4u*)(L + B_W + (i * 7 + w) * 1024 + lane * 16) = wt[i];
  }
  const int xb = (layer == 0 ? OFF_X0 : OFF_X2) + (4 * g + rb) * KS * 1024;
  int xo[NK];
  v2u res[NK];
#pragma unroll
  for (int i = 0; i < NK; ++i) {
    const int t = kh * 3 + i, d0 = 72 * j + 16 * t + 4 * gq;
    xo[i] = (d0 >> 5) * 1024 + (((d0 >> 3) & 3) * 16 + c) * 16 + ((d0 >> 2) & 1) * 8;
    res[i] = v2u{0, 0};
    if (t < 4 || gq < 2) res[i] = ld64<SC1>(rs, xo[i], xb);
  }
  SB();
  if (poller) poll_ge(cnt_ptr(X.ws, CNT_A, layer, g), 8);
  STAMP(X, layer * 32 + 7);
  BAR();
  STAMP(X, layer * 32 + 8);
  v4u of[9];
  const int ob = OFF_O + layer * XBUF + ((4 * g + rb) * KS + kh * 9) * 1024;
#pragma unroll
  for (int u = 0; u < 9; ++u) of[u] = ld128<SC1>(rs, lane * 16 + (u & 3) * 1024, ob + (u >> 2) * 4096);
  SB();
  v4u wf[6][5];
#pragma unroll
  for (int u = 0; u < 6; ++u)
#pragma unroll
    for (int t = 0; t < 5; ++t) wf[u][t] = *(LAS v4u*)(L + B_W + ((kh * 9 + u) * 5 + t) * 1024 + lane * 16);
  SB();
  f32x4 acc[5];
#pragma unroll
  for (int t = 0; t < 5; ++t) acc[t] = f32x4{0, 0, 0, 0};
#pragma unroll
  for (int u = 0; u < 9; ++u) {
#pragma unroll
    for (int t = 0; t < 5; ++t) {
      acc[t] = mfma16(wf[u % 6][t], of[u], acc[t]);
      if (u < 3) wf[u][t] = *(LAS v4u*)(L + B_W + ((kh * 9 + u + 6) * 5 + t) * 1024 + lane * 16);
    }
    SB();
  }
#pragma unroll
  for (int t = (kh ? 0 : 3); t < (kh ? 3 : 5); ++t) *(LAS f32x4*)(L + B_RED + (rb * 5 + t) * 1024 + lane * 16) = acc[t];
  STAMP(X, layer * 32 + 9);
  BAR();
  const int x1b = OFF_X1 + layer * XBUF + (4 * g + rb) * KS * 1024;
#pragma unroll
  for (int i = 0; i < NK; ++i) {
    const int t = kh * 3 + i;
    const f32x4 o = *(LAS f32x4*)(L + B_RED + (rb * 5 + t) * 1024 + lane * 16);
    f32x4 v = o + acc[t];
    v.x += bflo(res[i].x); v.y += bfhi(res[i].x); v.z += bflo(res[i].y); v.w += bfhi(res[i].y);
    if (t < 4 || gq < 2) st64<SC1>(pk4(v), rs, xo[i], x1b);
  }
  STAMP(X, layer * 32 + 10);
  drain<0>();
  BAR();
  STAMP(X, layer * 32 + 11);
  if (poller) {
    gu32* p = cnt_ptr(X.ws, CNT_B, layer, g);
    signal(p, lane);
#pragma unroll
    for (int i = 0; i < R / 2; ++i) a[i] = v4u{0, 0, 0, 0};
    poll_ge(p, 8);
  } else {
    ring_fill<0, R / 2>(X, run, a);
  }
  BAR();
  STAMP(X, layer * 32 + 12);
}

template <int NP, int BASE, int TOTAL, int PUBK, typename PUB>
__device__ __forceinline__ void c_pass(LAS unsigned char* Xl, rsrc_t rs, int run, v4u (&a)[R], f32x4 (&acc)[NP][4], int lane, PUB pub) {
  v4u xf[2][4];
  LAS unsigned char* xp = Xl + lane * 16;
  LAS unsigned char* xq = xp + 2 * KS * 1024;
#define XF(r, u) (*(LAS v4u*)(((r) < 2 ? xp : xq) + (((r) & 1) * KS + (u)) * 1024))
#pragma unroll
  for (int r = 0; r < 4; ++r) xf[0][r] = XF(r, 0);
#pragma unroll
  for (int t = 0; t < NP; ++t)
#pragma unroll
    for (int r = 0; r < 4; ++r) acc[t][r] = f32x4{0, 0, 0, 0};
  SB();
#pragma unroll
  for (int u = 0; u < KS; ++u) {
    if (u == PUBK) pub();
#pragma unroll
    for (int t = 0; t < NP; ++t) {
      const int idx = BASE + u * NP + t;
      if (u + 1 < KS) {
#pragma unroll
        for (int r = 0; r < 4; ++r)
          if ((NP >= 4 ? r : r >> 1) == t) xf[(u + 1) & 1][r] = XF(r, u + 1);
      }
      SB();
#pragma unroll
      for (int r = 0; r < 4; ++r) acc[t][r] = mfma16(a[idx % R], xf[u & 1][r], acc[t][r]);
      if (idx + R < TOTAL) a[idx % R] = ld128<0>(rs, lane * 16 + ((idx + R) & 3) * 1024, run + ((idx + R) >> 2) * 4096);
      SB();
    }
  }
#undef XF
}

__device__ __forceinline__ void wg_publish(const Ctx& X, int slot, gu32* cnt) {
  if (X.lane == 0) {
    const unsigned old = __hip_atomic_fetch_add((LAS unsigned*)(X.L + L_CNT + slot * 4), 1u, __ATOMIC_RELAXED, __HIP_MEMORY_SCOPE_WORKGROUP);
    if ((old & 7u) == 7u) __hip_atomic_fetch_add(cnt, 1u, RLX_AGENT);
  }
}

template <int HI>
__device__ __forceinline__ void stage_c(const Ctx& X, int layer, int run, v4u (&a)[R]) {
  constexpr int NT = 5 - HI, NA = NT - 2, TOTAL = 2 * KS * NT;
  LAS unsigned char* L = X.L;
  const rsrc_t rs = X.rs;
  const int g = X.g, j = X.j, w = X.w, lane = X.lane;
  const int c = lane & 15, gq = lane >> 4;
  const int T0 = HI ? 20 + 4 * (w - 4) : 5 * w;
  {
    const int rb = w >> 1, uh = w & 1;
    const int xb = OFF_X1 + layer * XBUF + ((4 * g + rb) * KS + uh * 9) * 1024;
    v4u v[9];
#pragma unroll
    for (int u = 0; u < 9; ++u) v[u] = ld128<SC1>(rs, lane * 16 + (u & 3) * 1024, xb + (u >> 2) * 4096);
    SB();
    if (HI && w == 7) ring_fill<0, R / 2>(X, run, a);
    ring_fill<R / 2, R>(X, run, a);
#pragma unroll
    for (int u = 0; u < 9; ++u) *(LAS v4u*)(L + C_X + (rb * KS + uh * 9 + u) * 1024 + lane * 16) = v[u];
    STAMP(X, layer * 32 + 16);
    BAR();
    STAMP(X, layer * 32 + 17);
    float s = 0.f, q = 0.f;
#pragma unroll
    for (int u = 0; u < 9; ++u)
#pragma unroll
      for (int p = 0; p < 4; ++p) { s = dot2(v[u][p], ONES, s); q = dot2(v[u][p], v[u][p], q); }
    s = sum1632(s);
    q = sum1632(q);
    if (lane < 16) *(LAS f32x2*)(L + C_ST + ((rb * 16 + c) * 2 + uh) * 8) = f32x2{s, q};
  }
  STAMP(X, layer * 32 + 18);
  {
    f32x4 acc[NT][4];
    c_pass<NT, 0, TOTAL, -1>(L + C_X, rs, run, a, acc, lane, [] {});
    STAMP(X, layer * 32 + 19);
    v2u bias[NT];
    f32x4 cs[NT];
#pragma unroll
    for (int t = 0; t < NT; ++t) {
      bias[t] = ld64<0>(X.rb1, (16 * t + 4 * gq) * 2, (j * 576 + T0 * 16) * 2);
      const int so = OFF_CS + (j * 576 + T0 * 16) * 4;
      const v4u c0 = ld128<0>(rs, (16 * t + 4 * gq) * 4, so), c1 = ld128<0>(rs, (16 * t + 4 * gq) * 4, so + F * 4), c2 = ld128<0>(rs, (16 * t + 4 * gq) * 4, so + 2 * F * 4);
      cs[t] = __builtin_bit_cast(f32x4, c0) + __builtin_bit_cast(f32x4, c1) + __builtin_bit_cast(f32x4, c2);
    }
    BAR();
    float rstd[4], nm[4];
#pragma unroll
    for (int r = 0; r < 4; ++r) {
      const f32x4 st = *(LAS f32x4*)(L + C_ST + (r * 16 + c) * 16);
      const float mean = (st.x + st.z) * (1.f / D);
      const float var = fmaxf((st.y + st.w) * (1.f / D) - mean * mean, 0.f);
      rstd[r] = rsqrtf(var + 1e-5f);
      nm[r] = -mean * rstd[r];
    }
#pragma unroll
    for (int t = 0; t < NT; ++t) {
      const int T = T0 + t;
      const f32x2 b01 = {bflo(bias[t].x), bfhi(bias[t].x)}, b23 = {bflo(bias[t].y), bfhi(bias[t].y)};
#pragma unroll
      for (int r = 0; r < 4; ++r) {
        v2u o;
#pragma unroll
        for (int e = 0; e < 2; ++e) {
          const f32x2 ac = e ? f32x2{acc[t][r].z, acc[t][r].w} : f32x2{acc[t][r].x, acc[t][r].y};
          const f32x2 cc = e ? f32x2{cs[t].z, cs[t].w} : f32x2{cs[t].x, cs[t].y};
          const f32x2 tt = __builtin_elementwise_fma(cc, f32x2{nm[r], nm[r]}, e ? b23 : b01);
          const f32x2 v = __builtin_elementwise_fma(ac, f32x2{rstd[r], rstd[r]}, tt);
          const f32x2 p = __builtin_elementwise_fma(v * v, f32x2{-0.10294324f, -0.10294324f}, f32x2{-2.3022082f, -2.3022082f});
          const f32x2 z = v * p;
          const f32x2 d = f32x2{__builtin_amdgcn_exp2f(z.x), __builtin_amdgcn_exp2f(z.y)} + f32x2{1.f, 1.f};
          const f32x2 y = v * f32x2{__builtin_amdgcn_rcpf(d.x), __builtin_amdgcn_rcpf(d.y)};
          o[e] = pk2(y.x, y.y);
        }
        *(LAS v2u*)(L + C_H + (r * KS + (T >> 1)) * 1024 + ((2 * (T & 1) + (gq >> 1)) * 16 + c) * 16 + (gq & 1) * 8) = o;
      }
    }
  }
  STAMP(X, layer * 32 + 20);
  BAR();
  STAMP(X, layer * 32 + 21);
  const int pb = OFF_P + layer * PBUF + g * 64 * 9216 + j * 9216;
  const int pv = (c >> 3) * 8 * 9216 + (c & 7) * 32 + gq * 8;
  {
    f32x4 acc[NA][4];
    c_pass<NA, KS * NT, TOTAL, -1>(L + C_H, rs, run, a, acc, lane, [] {});
    const int Ta = HI ? 12 + 2 * (w - 4) : 3 * w;
#pragma unroll
    for (int t = 0; t < NA; ++t)
#pragma unroll
      for (int r = 0; r < 4; ++r) st64<SC1>(pk4(acc[t][r]), rs, pv + t * 256, pb + Ta * 256 + 2 * r * 8 * 9216);
    SB();
  }
  STAMP(X, layer * 32 + 22);
  {
    f32x4 acc[2][4];
    constexpr int PK = 8, NREF = (2 * PK < 36 - R) ? 2 * PK : (36 - R > 0 ? 36 - R : 0);
    c_pass<2, KS*(NT + NA), TOTAL, PK>(L + C_H, rs, run, a, acc, lane, [&] {
      drain<NREF>();
      wg_publish(X, 0, cnt_ptr(X.ws, CNT_C1, layer, g));
      SB();
    });
#pragma unroll
    for (int t = 0; t < 2; ++t)
#pragma unroll
      for (int r = 0; r < 4; ++r) st64<SC1>(pk4(acc[t][r]), rs, pv + t * 256, pb + (20 + 2 * w) * 256 + 2 * r * 8 * 9216);
    SB();
  }
  STAMP(X, layer * 32 + 23);
  drain<0>();
  wg_publish(X, 1, cnt_ptr(X.ws, CNT_C2, layer, g));
  STAMP(X, layer * 32 + 24);
}

template <int HI, int LAYER, typename PRE>
__device__ __forceinline__ void stage_d(const Ctx& X, PRE pre) {
  constexpr int layer = LAYER;
  LAS unsigned char* L = X.L;
  const rsrc_t rs = X.rs;
  const int g = X.g, j = X.j, w = X.w, lane = X.lane;
  const int pb = OFF_P + layer * PBUF + (g * 8 + j) * 8 * 9216;
  const int RB = 4 * g + (j >> 1);
  const int tok = (lane >> 1) & 7, hf = lane & 1;
  const bool has1 = !HI || w == 4;
  float x[2][8];
  int fo[2], pp[2];
  v4u xr[2], bv[2];
  pp[0] = w * 64 + lane;
  pp[1] = 320 + w * 32 + (lane & 31);
#pragma unroll
  for (int i = 0; i < 2; ++i) {
    const int T = pp[i] >> 4;
    fo[i] = (T >> 1) * 1024 + ((2 * (T & 1) + hf) * 16 + (j & 1) * 8 + tok) * 16;
    xr[i] = ld128<SC1>(rs, fo[i], OFF_X1 + layer * XBUF + RB * KS * 1024);
    bv[i] = ld128<0>(X.rb2, (T * 16 + hf * 8) * 2, 0);
  }
  float s = 0.f, q = 0.f;
#pragma unroll
  for (int e = 0; e < 8; ++e) x[0][e] = 0.f;
  if (has1) {
    poll_ge(cnt_ptr(X.ws, CNT_C1, layer, g), 8);
    v4u pv[8];
#pragma unroll
    for (int k = 0; k < 8; ++k) pv[k] = ld128<SC1>(rs, pp[0] * 16, pb + k * 9216);
    SB();
    v4u o;
#pragma unroll
    for (int e = 0; e < 4; ++e) {
      float lo = bflo(bv[0][e]) + bflo(xr[0][e]), hi = bfhi(bv[0][e]) + bfhi(xr[0][e]);
      float l2 = 0.f, h2 = 0.f;
#pragma unroll
      for (int k = 0; k < 8; ++k) { l2 += bflo(pv[k][e]); h2 += bfhi(pv[k][e]); }
      o[e] = pk2(lo + l2, hi + h2);
      x[0][2 * e] = bflo(o[e]);
      x[0][2 * e + 1] = bfhi(o[e]);
      s = dot2(o[e], ONES, s);
      q = dot2(o[e], o[e], q);
    }
    if (layer == 0) st128<SC1>(o, rs, fo[0], OFF_X2 + RB * KS * 1024);
    else st128<0>(o, X.rout, ((g * 64 + j * 8 + tok) * D + (pp[0] >> 4) * 16 + hf * 8) * 2, 0);
  }
  STAMP(X, layer * 32 + 25);
  if (HI && w == 7) poll_ge(cnt_ptr(X.ws, CNT_C2, layer, g), 8);
  else pre();
  BAR();
  STAMP(X, layer * 32 + 26);
  {
    v4u pv[4];
#pragma unroll
    for (int k = 0; k < 4; ++k) pv[k] = ld128<SC1>(rs, pp[1] * 16 + (lane >> 5) * 4 * 9216, pb + k * 9216);
    SB();
    v4u o;
#pragma unroll
    for (int e = 0; e < 4; ++e) {
      float l2 = 0.f, h2 = 0.f;
#pragma unroll
      for (int k = 0; k < 4; ++k) { l2 += bflo(pv[k][e]); h2 += bfhi(pv[k][e]); }
      l2 = sum32(l2);
      h2 = sum32(h2);
      o[e] = pk2(bflo(bv[1][e]) + bflo(xr[1][e]) + l2, bfhi(bv[1][e]) + bfhi(xr[1][e]) + h2);
      x[1][2 * e] = bflo(o[e]);
      x[1][2 * e + 1] = bfhi(o[e]);
      if (lane < 32) { s = dot2(o[e], ONES, s); q = dot2(o[e], o[e], q); }
    }
    if (lane < 32) {
      if (layer == 0) st128<SC1>(o, rs, fo[1], OFF_X2 + RB * KS * 1024);
      else st128<0>(o, X.rout, ((g * 64 + j * 8 + tok) * D + (pp[1] >> 4) * 16 + hf * 8) * 2, 0);
    }
  }
  STAMP(X, layer * 32 + 27);
  if (layer == 0) {
    s += asf(__builtin_amdgcn_mov_dpp(asu(s), 0xB1, 0xF, 0xF, true));
    q += asf(__builtin_amdgcn_mov_dpp(asu(q), 0xB1, 0xF, 0xF, true));
    s = sum1632(s);
    q = sum1632(q);
    if (lane < 16 && hf == 0) *(LAS f32x2*)(L + D_ST + (tok * 8 + w) * 8) = f32x2{s, q};
    BAR();
    float ts = 0.f, tq = 0.f;
#pragma unroll
    for (int k = 0; k < 4; ++k) {
      const f32x4 v = *(LAS f32x4*)(L + D_ST + tok * 64 + k * 16);
      ts += v.x + v.z;
      tq += v.y + v.w;
    }
    const float mean = ts * (1.f / D);
    const float var = fmaxf(tq * (1.f / D) - mean * mean, 0.f);
    const float rstd = rsqrtf(var + 1e-5f), nm = -mean * rstd;
#pragma unroll
    for (int i = 0; i < 2; ++i)
      if (i ? lane < 32 : has1) {
        v4u o;
#pragma unroll
        for (int e = 0; e < 4; ++e) o[e] = pk2(__builtin_fmaf(x[i][2 * e], rstd, nm), __builtin_fmaf(x[i][2 * e + 1], rstd, nm));
        st128<SC1>(o, rs, fo[i], OFF_XN + XBUF + RB * KS * 1024);
      }
    SB();
  }
}

template <int HI>
__device__ __forceinline__ void body(const Ctx& X) {
  const int run0 = OFF_WC + (X.j * 1296 + (HI ? 720 + (X.w - 4) * 144 : X.w * 180)) * 1024;
  v4u wa[KS], wb[KS], xt[12];
  STAMP(X, 0);
  asm volatile("; role %0" ::"i"(HI) : "memory");
  xn_load<HI>(fresh(X), 0, xt);
  wq_prefetch<HI, 0, KS>(fresh(X), wa, wb);
#pragma unroll 1
  for (int layer = 0;; ++layer) {
    stage_a<HI>(fresh(X), layer, xt, wa, wb);
    v4u a[R];
    int run = run0;
    asm volatile("" : "+s"(run));
    stage_b<HI>(fresh(X), layer, run, a);
    asm volatile("" : "+s"(run));
    stage_c<HI>(fresh(X), layer, run, a);
    if (layer == 1) break;
    const bool poller = HI && X.w == 7;
#pragma unroll
    for (int u = 0; u < KS; ++u) { wa[u] = v4u{0, 0, 0, 0}; wb[u] = v4u{0, 0, 0, 0}; }
    stage_d<HI, 0>(fresh(X), [&] { wq_prefetch<HI, 0, 9>(fresh(X), wa, wb); });
    drain<0>();
    STAMP(X, 28);
    BAR();
    if (poller) {
      signal(cnt_ptr(X.ws, CNT_D, 0, X.g), X.lane);
      poll_ge(cnt_ptr(X.ws, CNT_D, 0, X.g), 8);
      if (X.g > 0) poll_ge(cnt_ptr(X.ws, CNT_D, 0, X.g - 1), 8);
    } else {
      wq_prefetch<HI, 9, 15>(fresh(X), wa, wb);
    }
    BAR();
    STAMP(X, 32);
    xn_load<HI>(fresh(X), 1, xt);
    if (poller) wq_prefetch<HI, 0, 15>(fresh(X), wa, wb);
    wq_prefetch<HI, 15, KS>(fresh(X), wa, wb);
  }
  stage_d<HI, 1>(fresh(X), [] {});
  STAMP(X, 32 + 29);
}

__global__ void __launch_bounds__(NTHR, 2) k_main(unsigned char* ws, const u16* b1, const u16* b2, u16* out) {
  extern __shared__ __attribute__((aligned(16))) unsigned char smem[];
  Ctx X;
  X.ws = ws;
  X.L = (LAS unsigned char*)smem;
  X.rs = __builtin_amdgcn_make_buffer_rsrc((void*)ws, 0, WS_BYTES, 0x00020000);
  X.rb1 = __builtin_amdgcn_make_buffer_rsrc((void*)b1, 0, F * 2, 0x00020000);
  X.rb2 = __builtin_amdgcn_make_buffer_rsrc((void*)b2, 0, D * 2, 0x00020000);
  X.rout = __builtin_amdgcn_make_buffer_rsrc((void*)out, 0, S * D * 2, 0x00020000);
  X.lane = threadIdx.x & 63;
  X.w = __builtin_amdgcn_readfirstlane(threadIdx.x >> 6);
  const int x = blockIdx.x & 7, i = blockIdx.x >> 3;
  X.g = (x % RA) * (32 / RA) + i / RA;
  X.j = (x / RA) * RA + i % RA;
  if (threadIdx.x < 2) *(LAS unsigned*)(X.L + L_CNT + threadIdx.x * 4) = 0u;
  if (X.w < 4) body<0>(X);
  else body<1>(X);
}
}

extern "C" void kernel_launch(void* const* d_in, const int* in_sizes, int n_in, void* d_out, int out_size, void* d_ws, size_t ws_size,
                              hipStream_t stream) {
  const u16* x = (const u16*)d_in[0];
  const u16* wqkv = (const u16*)d_in[1];
  const u16* wo = (const u16*)d_in[2];
  const u16* w1 = (const u16*)d_in[4];
  const u16* b1 = (const u16*)d_in[5];
  const u16* w2 = (const u16*)d_in[6];
  const u16* b2 = (const u16*)d_in[7];
  unsigned char* ws = (unsigned char*)d_ws;
  (void)hipFuncSetAttribute((const void*)k_main, hipFuncAttributeMaxDynamicSharedMemorySize, LDS_BYTES);
  k_prep<<<K0_BLOCKS, 256, 0, stream>>>(x, wqkv, wo, w1, w2, ws);
  k_main<<<256, NTHR, LDS_BYTES, stream>>>(ws, b1, b2, (u16*)d_out);
}
```

```cpp
#include <hip/hip_runtime.h>
#include <hip/hip_bf16.h>

#if defined(__HIP_DEVICE_COMPILE__)
#pragma clang attribute push(__attribute__((target("no-packed-fp32-ops"))), apply_to = function)
#endif
namespace {
typedef unsigned short u16;
typedef unsigned v4u __attribute__((ext_vector_type(4)));
typedef unsigned v2u __attribute__((ext_vector_type(2)));
typedef float f32x4 __attribute__((ext_vector_type(4)));
typedef float f32x2 __attribute__((ext_vector_type(2)));
typedef __bf16 bf16x2_t __attribute__((ext_vector_type(2)));
typedef __bf16 bf16x8_t __attribute__((ext_vector_type(8)));
#define GAS __attribute__((address_space(1)))
#define LAS __attribute__((address_space(3)))
typedef __amdgpu_buffer_rsrc_t rsrc_t;

constexpr int S = 2048, D = 576, F = 4608;
constexpr int KS = 18;
constexpr int NTHR = 512;
constexpr int XBUF = 128 * KS * 1024;
constexpr int OFF_CTRL = 0;
constexpr int OFF_CS = 65536;
constexpr int OFF_RS = OFF_CS + 3 * 65536;
constexpr int OFF_WQ = OFF_RS + 2 * 128 * 2048;
constexpr int OFF_WO = OFF_WQ + 8 * 14 * KS * 1024;
constexpr int OFF_WC = OFF_WO + 8 * 90 * 1024;
constexpr int OFF_X0 = OFF_WC + 8 * 1296 * 1024;
constexpr int OFF_XN = OFF_X0 + XBUF;
constexpr int OFF_O = OFF_XN + 2 * XBUF;
constexpr int OFF_X1 = OFF_O + 2 * XBUF;
constexpr int OFF_X2 = OFF_X1 + 2 * XBUF;
constexpr int OFF_P = OFF_X2 + XBUF;
constexpr int PBUF = 8 * XBUF;
constexpr int WS_BYTES = OFF_P + 2 * PBUF;

__device__ __forceinline__ f32x4 mfma16(v4u a, v4u b, f32x4 c) {
  return __builtin_amdgcn_mfma_f32_16x16x32_bf16(__builtin_bit_cast(bf16x8_t, a), __builtin_bit_cast(bf16x8_t, b), c, 0, 0, 0);
}
__device__ __forceinline__ void m16(f32x4& c, v4u a, v4u b) { asm volatile("v_mfma_f32_16x16x32_bf16 %0, %1, %2, %0" : "+v"(c) : "v"(a), "v"(b)); }
__device__ __forceinline__ void m16z(f32x4& c, v4u a, v4u b) { asm volatile("v_mfma_f32_16x16x32_bf16 %0, %1, %2, 0" : "=&v"(c) : "v"(a), "v"(b)); }
template <bool Z> __device__ __forceinline__ void m16t(f32x4& c, v4u a, v4u b) { if (Z) m16z(c, a, b); else m16(c, a, b); }
__device__ __forceinline__ unsigned pk2(float lo, float hi) {
  f32x2 v = {lo, hi};
  return __builtin_bit_cast(unsigned, __builtin_convertvector(v, bf16x2_t));
}
__device__ __forceinline__ float asf(unsigned u) { return __builtin_bit_cast(float, u); }
__device__ __forceinline__ unsigned asu(float f) { return __builtin_bit_cast(unsigned, f); }
__device__ __forceinline__ float bflo(unsigned u) { return asf(u << 16); }
__device__ __forceinline__ float bfhi(unsigned u) { return asf(u & 0xffff0000u); }
__device__ __forceinline__ v2u pk4(f32x4 v) { return v2u{pk2(v.x, v.y), pk2(v.z, v.w)}; }
__device__ __forceinline__ float dot2(unsigned a, unsigned b, float c) {
  return __builtin_amdgcn_fdot2_f32_bf16(__builtin_bit_cast(bf16x2_t, a), __builtin_bit_cast(bf16x2_t, b), c, false);
}
constexpr unsigned ONES = 0x3f803f80u;

template <int AUX> __device__ __forceinline__ v4u ld128(rsrc_t rs, int voff, int soff) { return __builtin_amdgcn_raw_buffer_load_b128(rs, voff, soff, AUX); }
template <int AUX> __device__ __forceinline__ v2u ld64(rsrc_t rs, int voff, int soff) { return __builtin_amdgcn_raw_buffer_load_b64(rs, voff, soff, AUX); }
template <int AUX> __device__ __forceinline__ void st128(v4u v, rsrc_t rs, int voff, int soff) { __builtin_amdgcn_raw_buffer_store_b128(v, rs, voff + soff, 0, AUX); }
template <int AUX> __device__ __forceinline__ void st64(v2u v, rsrc_t rs, int voff, int soff) { __builtin_amdgcn_raw_buffer_store_b64(v, rs, voff, soff, AUX); }
template <int AUX> __device__ __forceinline__ void st32(unsigned v, rsrc_t rs, int voff, int soff) { __builtin_amdgcn_raw_buffer_store_b32(v, rs, voff, soff, AUX); }
#define SB() __builtin_amdgcn_sched_barrier(0)
#define MFMA_DONE() do { SB(); asm volatile("s_nop 7\n\ts_nop 1" ::: "memory"); SB(); } while (0)
#define TIDX ((int)__builtin_amdgcn_workitem_id_x())
#define BIDX ((int)__builtin_amdgcn_workgroup_id_x())
#define SYNC() do { __builtin_amdgcn_fence(__ATOMIC_RELEASE, "workgroup"); __builtin_amdgcn_s_barrier(); __builtin_amdgcn_fence(__ATOMIC_ACQUIRE, "workgroup"); } while (0)
__device__ __forceinline__ float fmx(float a, float b) { return __builtin_fmaxf(a, b); }
__device__ __forceinline__ float rsq(float a) { return __builtin_amdgcn_rsqf(a); }

__device__ __forceinline__ float sum32(float v) {
  const auto b = __builtin_amdgcn_permlane32_swap(asu(v), asu(v), false, false);
  const unsigned b0 = b[0], b1 = b[1];
  return asf(b0) + asf(b1);
}
__device__ __forceinline__ float sum1632(float v) {
  const auto a = __builtin_amdgcn_permlane16_swap(asu(v), asu(v), false, false);
  const unsigned a0 = a[0], a1 = a[1];
  return sum32(asf(a0) + asf(a1));
}
__device__ __forceinline__ float max1632(float v) {
  const auto a = __builtin_amdgcn_permlane16_swap(asu(v), asu(v), false, false);
  const unsigned a0 = a[0], a1 = a[1];
  v = fmx(asf(a0), asf(a1));
  const auto b = __builtin_amdgcn_permlane32_swap(asu(v), asu(v), false, false);
  const unsigned b0 = b[0], b1 = b[1];
  return fmx(asf(b0), asf(b1));
}

template <typename OP> __device__ __forceinline__ void red1632x2(float& a, float& b, OP op) {
  const auto s = __builtin_amdgcn_permlane16_swap(asu(a), asu(b), false, false);
  const unsigned s0 = s[0], s1 = s[1];
  const float t = op(asf(s0), asf(s1));
  const auto u = __builtin_amdgcn_permlane32_swap(asu(t), asu(t), false, false);
  const unsigned u0 = u[0], u1 = u[1];
  const float v = op(asf(u0), asf(u1));
  const auto x = __builtin_amdgcn_permlane16_swap(asu(v), asu(v), false, false);
  const unsigned x0 = x[0], x1 = x[1];
  a = asf(x0);
  b = asf(x1);
}
__device__ __forceinline__ void max1632x2(float& a, float& b) { red1632x2(a, b, [](float x, float y) { return fmx(x, y); }); }
__device__ __forceinline__ void sum1632x2(float& a, float& b) { red1632x2(a, b, [](float x, float y) { return x + y; }); }

__device__ __forceinline__ void tr8x8(const v4u (&r)[8], v4u (&o)[8]) {
#pragma unroll
  for (int c = 0; c < 4; ++c) {
#pragma unroll
    for (int p = 0; p < 4; ++p) {
      o[2 * c][p] = __builtin_amdgcn_perm(r[2 * p + 1][c], r[2 * p][c], 0x05040100u);
      o[2 * c + 1][p] = __builtin_amdgcn_perm(r[2 * p + 1][c], r[2 * p][c], 0x07060302u);
    }
  }
}
constexpr int K0_WO = 72 * 80, K0_WQ = 72 * 896;
constexpr int K0_BO = (K0_WO + 255) / 256, K0_BQ = K0_WQ / 256, K0_BX = 128;
constexpr int K0_BLOCKS = K0_BQ + K0_BX + K0_BO;

__global__ void __launch_bounds__(256, 4) k_prep(const u16* __restrict__ x, const u16* __restrict__ wqkv, const u16* __restrict__ wo,
                                                 unsigned char* __restrict__ ws) {
  __shared__ __attribute__((aligned(16))) unsigned char sm[512];
  int b = BIDX;
  const int tid = TIDX;
  if (b < 16) ((v4u*)(ws + OFF_CTRL))[b * 256 + tid] = v4u{0, 0, 0, 0};
  if (b < K0_BX) {
    const int wv = tid >> 6, RB = b, lane = tid & 63, c = lane & 15, gq = lane >> 4;
    v4u v[5];
#pragma unroll
    for (int i = 0; i < 5; ++i) {
      v[i] = v4u{0, 0, 0, 0};
      if (i < 4 || wv < 2) v[i] = *(const v4u*)(x + (size_t)(RB * 16 + c) * D + (i * 4 + wv) * 32 + gq * 8);
    }
    float s = 0.f, q = 0.f;
#pragma unroll
    for (int i = 0; i < 5; ++i) {
      if (i < 4 || wv < 2) *(v4u*)(ws + OFF_X0 + ((size_t)(RB * KS + i * 4 + wv) * 64 + lane) * 16) = v[i];
#pragma unroll
      for (int p = 0; p < 4; ++p) { s = dot2(v[i][p], ONES, s); q = dot2(v[i][p], v[i][p], q); }
    }
    s = sum1632(s);
    q = sum1632(q);
    if (lane < 16) *(f32x2*)(sm + (c * 4 + wv) * 8) = f32x2{s, q};
    SYNC();
    const f32x4 a0 = *(const f32x4*)(sm + c * 32), a1 = *(const f32x4*)(sm + c * 32 + 16);
    const float mean = (a0.x + a0.z + a1.x + a1.z) * (1.f / D);
    const float var = fmx((a0.y + a0.w + a1.y + a1.w) * (1.f / D) - mean * mean, 0.f);
    const float rstd = rsq(var + 1e-5f), nm = -mean * rstd;
#pragma unroll
    for (int i = 0; i < 5; ++i) {
      v4u o;
#pragma unroll
      for (int p = 0; p < 4; ++p) o[p] = pk2(__builtin_fmaf(bflo(v[i][p]), rstd, nm), __builtin_fmaf(bfhi(v[i][p]), rstd, nm));
      if (i < 4 || wv < 2) *(v4u*)(ws + OFF_XN + ((size_t)(RB * KS + i * 4 + wv) * 64 + lane) * 16) = o;
    }
    return;
  }
  b -= K0_BX;
  if (b < K0_BQ) {
    const int i = b * 256 + tid, kg = i / 896, cp = i % 896;
    const int u = kg >> 2, gq = kg & 3;
    if (cp < 864) {
      unsigned r[8];
#pragma unroll
      for (int q = 0; q < 8; ++q) r[q] = *(const unsigned*)(wqkv + (size_t)(kg * 8 + q) * 1728 + cp * 2);
      v4u o0, o1;
#pragma unroll
      for (int p = 0; p < 4; ++p) {
        o0[p] = __builtin_amdgcn_perm(r[2 * p + 1], r[2 * p], 0x05040100u);
        o1[p] = __builtin_amdgcn_perm(r[2 * p + 1], r[2 * p], 0x07060302u);
      }
      const int type = cp / 288, hp = cp % 288, head = hp / 9, dp = hp % 9, j = head >> 2, h = head & 3;
      int blk, m;
      if (dp < 8) { blk = type * 4 + h; m = dp * 2; }
      else if (type < 2) { blk = 12; m = 4 * h + 2 * type; }
      else { blk = 13; m = 2 * h; }
      v4u* dst = (v4u*)(ws + OFF_WQ + ((size_t)((j * 14 + blk) * KS + u) * 64 + gq * 16 + m) * 16);
      dst[0] = o0;
      dst[1] = o1;
    } else {
      const int z = cp - 864, j = z >> 2, h = z & 3;
      v4u* dst = (v4u*)(ws + OFF_WQ + ((size_t)((j * 14 + 13) * KS + u) * 64 + gq * 16 + 8 + 2 * h) * 16);
      dst[0] = v4u{0, 0, 0, 0};
      dst[1] = v4u{0, 0, 0, 0};
    }
    return;
  }
  b -= K0_BQ;
  {
    const int i = b * 256 + tid;
    if (i >= K0_WO) return;
    const int kg = i / 80, nb = i % 80, j = nb / 10, q8 = nb % 10, u = kg >> 2, gq = kg & 3;
    v4u o[8];
    if (q8 < 9) {
      v4u r[8];
#pragma unroll
      for (int jj = 0; jj < 8; ++jj) {
        int row;
        if (u < 16) row = (2 * u + (jj >> 2)) * 18 + 4 * gq + (jj & 3);
        else row = (2 * (8 * (u - 16) + 2 * gq + (jj >> 2)) + ((jj >> 1) & 1)) * 18 + 16 + (jj & 1);
        r[jj] = *(const v4u*)(wo + (size_t)row * D + j * 72 + q8 * 8);
      }
      tr8x8(r, o);
    } else {
#pragma unroll
      for (int c = 0; c < 8; ++c) o[c] = v4u{0, 0, 0, 0};
    }
    v4u* dst = (v4u*)(ws + OFF_WO + ((size_t)((j * KS + u) * 5 + (q8 >> 1)) * 64 + gq * 16 + (q8 & 1) * 8) * 16);
#pragma unroll
    for (int c = 0; c < 8; ++c) dst[c] = o[c];
  }
}

#define RA 2
#define RING_LO 24
#define RING_HI 30
#define QRING_LO 14
#define QRING 8
#define HPRE 12
#define SIGK 12
template <int HI> constexpr int ring() { return HI ? RING_HI : RING_LO; }
template <int HI> constexpr int rq() { return HI ? QRING : QRING_LO; }
constexpr int QPRE = 3;
constexpr int CPF = 4;
constexpr int CPRE = 12;
constexpr int cmin(int a, int b) { return a < b ? a : b; }
constexpr int SC1 = 16;
constexpr int A_XT = 0;
constexpr int A_Q = 5 * KS * 1024;
constexpr int A_K = A_Q + 16 * 512;
constexpr int A_V = A_K + 20 * 512;
constexpr int A_EQK = A_V + 20 * 512;
constexpr int A_EV = A_EQK + 5 * 512;
constexpr int B_W = 0, B_SCR = 96 * 1024, B_RED = 124 * 1024;
constexpr int C_X = 0, C_H = 4 * KS * 1024, C_ST = 8 * KS * 1024;
constexpr int L_CB = C_ST + 1024;
constexpr int D_ST = L_CB + 576 * 8;
constexpr int L_CNT = D_ST + 512;
constexpr int L_STAMP = L_CNT + 64;
#define STAMPCLK() __builtin_amdgcn_s_memrealtime()
constexpr int LDS_BYTES = L_STAMP;
#define STAMP(X_, slot) do {} while (0)
enum { CNT_A = 0, CNT_B = 1, CNT_C1 = 2, CNT_C2 = 3, CNT_D = 4, CNT_W = 5 };
typedef GAS unsigned gu32;
__device__ __forceinline__ gu32* cnt_ptr(unsigned char* ws, int kind, int layer, int g) { return (gu32*)(ws + OFF_CTRL) + ((kind * 2 + layer) * 32 + g) * 32; }
#define RLX_AGENT __ATOMIC_RELAXED, __HIP_MEMORY_SCOPE_AGENT
#define RLX_WG __ATOMIC_RELAXED, __HIP_MEMORY_SCOPE_WORKGROUP
#define BAR() do { SB(); asm volatile("s_waitcnt lgkmcnt(0)\n\ts_barrier" ::: "memory"); SB(); } while (0)
#define DRAIN() do { SB(); asm volatile("s_waitcnt vmcnt(0)" ::: "memory"); SB(); } while (0)
__device__ __forceinline__ void poll_ge(gu32* p, unsigned want) {
#pragma unroll 1
  for (unsigned spins = 0; spins < (1u << 22); ++spins) {
    const unsigned v = __builtin_amdgcn_readfirstlane(__hip_atomic_load(p, RLX_AGENT));
    if (v >= want) break;
    __builtin_amdgcn_s_sleep(1);
  }
  __builtin_amdgcn_fence(__ATOMIC_ACQUIRE, "wavefront");
  asm volatile("" ::: "memory");
}
__device__ __forceinline__ void signal(gu32* p, int lane) {
  if (lane == 0) __hip_atomic_fetch_add(p, 1u, RLX_AGENT);
}
__device__ __forceinline__ void lds_wait(LAS unsigned* p, unsigned want) {
#pragma unroll 1
  for (unsigned spins = 0; spins < (1u << 24); ++spins) {
    const unsigned v = __builtin_amdgcn_readfirstlane(__hip_atomic_load(p, RLX_WG));
    if (v >= want) break;
    __builtin_amdgcn_s_sleep(1);
  }
  asm volatile("" ::: "memory");
  SB();
}

struct Ctx {
  unsigned char* ws;
  const u16* w1;
  const u16* w2;
  const u16* b1;
  LAS unsigned char* L;
  rsrc_t rs, rb2, rout;
  int g, j, w, lane;
};
__device__ __forceinline__ Ctx fresh(const Ctx& X) {
  Ctx Y = X;
  asm volatile("" : "+v"(Y.lane));
  asm volatile("" : "+s"(Y.g), "+s"(Y.j), "+s"(Y.w));
  return Y;
}

__device__ __forceinline__ int rt_task(const Ctx& X) { return X.w < 5 ? X.g * 5 + X.w : (X.w == 5 && X.g < 2 ? 160 + X.g : -1); }
#define RTU 16
__device__ __forceinline__ void rt_load(const Ctx& X, int layer, v4u (&r)[8]) {
  const int task = layer == 0 ? rt_task(X) : -1;
  const bool is1 = task < 81;
  const int tt = is1 ? task : task - 81, chunk = tt / 9, strip = tt - 9 * chunk;
  const u16* src = is1 ? X.w1 + (size_t)chunk * 64 * F + X.j * 576 + strip * 64 : X.w2 + (size_t)(X.j * 576 + chunk * 64) * D + strip * 64;
  const int ld = is1 ? F : D;
  const rsrc_t rw = __builtin_amdgcn_make_buffer_rsrc((void*)src, 0, task < 0 ? 0 : (63 * ld + 64) * 2, 0x00020000);
  const int vo = ((X.lane >> 3) * 8 * ld + (X.lane & 7) * 8) * 2;
#pragma unroll
  for (int jj = 0; jj < 8; ++jj) r[jj] = v4u{0, 0, 0, 0};
  if (task >= 0) {
#pragma unroll
    for (int jj = 0; jj < 8; ++jj) r[jj] = ld128<0>(rw, vo, jj * ld * 2);
  }
  SB();
}
__device__ __forceinline__ void rt_store(const Ctx& X, const v4u (&r)[8]) {
  const int task = rt_task(X);
  if (task < 0) return;
  const bool is1 = task < 81;
  const int tt = is1 ? task : task - 81, chunk = tt / 9, strip = tt - 9 * chunk;
  const int lane = X.lane, kg = lane >> 3, nb = lane & 7;
  LAS unsigned char* sm = X.L + B_SCR + X.w * 8192;
  v4u o[8];
  tr8x8(r, o);
  const int base = (((nb >> 1) * 2 + (kg >> 2)) * 64 + (kg & 3) * 16 + (nb & 1) * 8) * 16;
#pragma unroll
  for (int c = 0; c < 8; ++c) *(LAS v4u*)(sm + base + ((c ^ nb) * 16)) = o[c];
  asm volatile("s_waitcnt lgkmcnt(0)" ::: "memory");
  float keep = 0.f;
#pragma unroll
  for (int tl = 0; tl < 4; ++tl) {
    const int T = strip * 4 + tl;
    const int wv = T < 20 ? T / 5 : 4 + ((T - 20) >> 2), nt = T < 20 ? 5 : 4, t = T < 20 ? T - 5 * wv : (T & 3);
    const int fr0 = X.j * 1296 + (T < 20 ? wv * 180 : 720 + (wv - 4) * 144) + ((is1 ? 0 : KS) + chunk * 2) * nt + t;
    float s = 0.f;
#pragma unroll
    for (int u = 0; u < 2; ++u) {
      const v4u v = *(LAS v4u*)(sm + ((tl * 2 + u) * 64 + (lane & ~7) + ((lane & 7) ^ (tl * 2 + ((lane >> 3) & 1)))) * 16);
      st128<SC1>(v, X.rs, lane * 16, OFF_WC + (fr0 + u * nt) * 1024);
#pragma unroll
      for (int p = 0; p < 4; ++p) s = dot2(v[p], ONES, s);
    }
    s = sum1632(s);
    if ((lane >> 4) == tl) keep = s;
  }
  if (is1) st32<SC1>(asu(keep), X.rs, lane * 4, OFF_CS + (chunk * F + X.j * 576 + strip * 64) * 4);
  SB();
}

template <int HI> __device__ __forceinline__ int wq_a(const Ctx& X) { return OFF_WQ + (X.j * 14 + (HI ? X.w + 4 : X.w)) * KS * 1024; }
template <int HI> __device__ __forceinline__ int wq_b(const Ctx& X) { return OFF_WQ + (X.j * 14 + (HI ? 12 + ((X.w - 4) >> 1) : X.w + 4)) * KS * 1024; }
template <int HI, int U0, int U1>
__device__ __forceinline__ void wq_prefetch(const Ctx& X, v4u (&wa)[rq<HI>()], v4u (&wb)[rq<HI>()]) {
  const int sa = wq_a<HI>(X), sb = wq_b<HI>(X);
#pragma unroll
  for (int u = U0; u < U1; ++u) {
    wa[u] = ld128<0>(X.rs, X.lane * 16 + (u & 3) * 1024, sa + (u >> 2) * 4096);
    wb[u] = ld128<0>(X.rs, X.lane * 16 + (u & 3) * 1024, sb + (u >> 2) * 4096);
  }
  SB();
}

template <int I0, int I1>
__device__ __forceinline__ void xn_load(const Ctx& X, int layer, v4u (&t)[12]) {
  const int base = OFF_XN + layer * XBUF + (4 * X.g - 1) * KS * 1024;
#pragma unroll
  for (int i = I0; i < I1; ++i) {
    const int f = i < 11 ? 8 * i + X.w : (X.w < 2 ? 88 + X.w : 89), u = f / 5, rb5 = f - 5 * u;
    t[i] = ld128<SC1>(X.rs, X.lane * 16, base + ((X.g == 0 && rb5 == 0 ? 1 : rb5) * KS + u) * 1024);
  }
  SB();
}

template <int LO, int HIX, int R>
__device__ __forceinline__ void ring_fill(const Ctx& X, int run, v4u (&a)[R]) {
#pragma unroll
  for (int i = LO; i < HIX; ++i) a[i] = ld128<SC1>(X.rs, X.lane * 16 + (i & 3) * 1024, run + (i >> 2) * 4096);
  SB();
}
template <int LO, int HIX, int R>
__device__ __forceinline__ void ring_zero(v4u (&a)[R]) {
#pragma unroll
  for (int i = LO; i < HIX; ++i) a[i] = v4u{0, 0, 0, 0};
}

template <int HI>
__device__ __forceinline__ void stage_a(const Ctx& X, int layer, const v4u (&t)[12], v4u (&wa)[rq<HI>()], v4u (&wb)[rq<HI>()], v4u (&raw)[8]) {
  constexpr int RQ = rq<HI>();
  LAS unsigned char* L = X.L;
  const rsrc_t rs = X.rs;
  const int g = X.g, j = X.j, w = X.w, lane = X.lane;
#pragma unroll
  for (int i = 0; i < 5; ++i) *(LAS v4u*)(L + A_XT + (8 * i + w) * 1024 + lane * 16) = t[i];
  STAMP(X, layer * 32 + 1);
  BAR();
  STAMP(X, layer * 32 + 2);
  v4u wt[12];
  {
    constexpr int A0 = HI ? 0 : 1, B1 = HI ? 3 : 5;
    const int h = w & 3;
    const int sa = wq_a<HI>(X), sb = wq_b<HI>(X);
    f32x4 ac[5], bc[5];
    v4u xf[2][5];
    const int rot = HI ? (w & 1) * 2 : 0;
    LAS unsigned char* xa = L + A_XT + lane * 16 + rot * 1024;
    LAS unsigned char* xb = L + A_XT + lane * 16 + (rot ? 0 : 3) * 1024;
    LAS unsigned char* xa2 = xa + 12 * 5120;
    LAS unsigned char* xb2 = xb + 12 * 5120;
#define XF(r, u) (*(LAS v4u*)(((u) < 12 ? ((r) < 3 ? xa : xb) : ((r) < 3 ? xa2 : xb2)) + ((u) < 12 ? (u) : (u) - 12) * 5120 + ((r) < 3 ? (r) : (r) - 3) * 1024))
#pragma unroll
    for (int r = 0; r < 5; ++r) xf[0][r] = XF(r, 0);
#pragma unroll
    for (int r = 0; r < 5; ++r) xf[1][r] = XF(r, 1);
    SB();
#pragma unroll
    for (int u = 0; u < KS; ++u) {
      if (u == 1) {
#pragma unroll
        for (int i = 5; i < 12; ++i)
          if (i < 11 || w < 2) *(LAS v4u*)(L + A_XT + (8 * i + w) * 1024 + lane * 16) = t[i];
        SB();
      }
      if (u == 2) { SB(); asm volatile("s_waitcnt lgkmcnt(0)" ::: "memory"); if (lane == 0) __hip_atomic_fetch_add((LAS unsigned*)(L + L_CNT + 16), 1u, RLX_WG); SB(); }
      if (u == 6) lds_wait((LAS unsigned*)(L + L_CNT + 16), 8 * (layer + 1));
#pragma unroll
      for (int r = 0; r < 5; ++r) {
        if (r >= A0) {
          if (u == 0) { if (HI) m16z(ac[r], xf[u & 1][r], wa[u % RQ]); else m16z(ac[r], wa[u % RQ], xf[u & 1][r]); }
          else { if (HI) m16(ac[r], xf[u & 1][r], wa[u % RQ]); else m16(ac[r], wa[u % RQ], xf[u & 1][r]); }
        }
        if (r < B1) { if (u == 0) m16z(bc[r], wb[u % RQ], xf[u & 1][r]); else m16(bc[r], wb[u % RQ], xf[u & 1][r]); }
        if (u + 2 < KS) xf[u & 1][r] = XF(r, u + 2);
        SB();
      }
      {
        const int l0 = u == 0 ? QPRE : cmin(KS, cmin(u + RQ, QPRE + 2 * u)), l1 = cmin(KS, cmin(u + 1 + RQ, QPRE + 2 * (u + 1)));
#pragma unroll
        for (int l = QPRE; l < KS; ++l)
          if (l >= l0 && l < l1 && (HI || l >= RQ || layer == 0)) {
            wa[l % RQ] = ld128<0>(rs, lane * 16 + (l & 3) * 1024, sa + (l >> 2) * 4096);
            wb[l % RQ] = ld128<0>(rs, lane * 16 + (l & 3) * 1024, sb + (l >> 2) * 4096);
          }
        if (u >= 10 && u < 16) {
#pragma unroll
          for (int i = 2 * (u - 10); i < 2 * (u - 10) + 2; ++i) wt[i] = ld128<0>(rs, lane * 16, OFF_WO + (j * 90 + i * 8 + w) * 1024);
        }
        if (u == RTU) rt_load(X, layer, raw);
        SB();
      }
    }
#undef XF
    MFMA_DONE();
    if (!HI) {
#pragma unroll
      for (int r = 1; r < 5; ++r) *(LAS v2u*)(L + A_Q + (h * 4 + r - 1) * 512 + lane * 8) = pk4(ac[r]);
#pragma unroll
      for (int r = 0; r < 5; ++r) *(LAS v2u*)(L + A_K + (h * 5 + r) * 512 + lane * 8) = pk4(bc[r]);
    } else {
#pragma unroll
      for (int r = 0; r < 5; ++r) *(LAS v2u*)(L + A_V + (h * 5 + (r < 3 ? rot + r : (rot ? 0 : 3) + r - 3)) * 512 + lane * 8) = pk4(ac[r]);
      if (w < 6) {
#pragma unroll
        for (int r = 0; r < 3; ++r) *(LAS v2u*)(L + A_EQK + (rot + r) * 512 + lane * 8) = pk4(bc[r]);
      } else {
        const int c = lane & 15, gq = lane >> 4;
#pragma unroll
        for (int r = 0; r < 3; ++r) {
          const v2u v = pk4(bc[r]);
          LAS u16* p = (LAS u16*)(L + A_EV + (rot + r) * 512 + ((c >> 2) * 16 + 4 * gq) * 8 + (c & 3) * 2);
          p[0] = (u16)v.x;
          p[4] = (u16)(v.x >> 16);
          p[8] = (u16)v.y;
          p[12] = (u16)(v.y >> 16);
        }
      }
    }
  }
  STAMP(X, layer * 32 + 3);
  BAR();
  STAMP(X, layer * 32 + 4);
  {
    const int c = lane & 15, gq = lane >> 4, rb = w & 3;
    typedef short s16x4 __attribute__((ext_vector_type(4)));
    auto mk = [](v2u a, v2u b, f32x4 cc) {
      return __builtin_amdgcn_mfma_f32_16x16x16bf16_1k(__builtin_bit_cast(s16x4, a), __builtin_bit_cast(s16x4, b), cc, 0, 0, 0);
    };
    LAS unsigned char* lp = L + rb * 512 + lane * 8;
    const v2u e0 = *(LAS v2u*)(lp + A_EQK), e1 = *(LAS v2u*)(lp + A_EQK + 512);
    v2u qm[2], k0[2], k1[2], v0[2], v1[2];
#pragma unroll
    for (int hh = 0; hh < 2; ++hh) {
      const int h = 2 * HI + hh;
      qm[hh] = *(LAS v2u*)(lp + A_Q + h * 4 * 512);
      k0[hh] = *(LAS v2u*)(lp + A_K + h * 5 * 512);
      k1[hh] = *(LAS v2u*)(lp + A_K + (h * 5 + 1) * 512);
    }
#pragma unroll
    for (int hh = 0; hh < 2; ++hh) {
      const int h = 2 * HI + hh;
      v0[hh] = *(LAS v2u*)(lp + A_V + h * 5 * 512);
      v1[hh] = *(LAS v2u*)(lp + A_V + (h * 5 + 1) * 512);
    }
    const v2u x0 = *(LAS v2u*)(lp + A_EV), x1 = *(LAS v2u*)(lp + A_EV + 512);
    SB();
    const bool first = (g == 0 && rb == 0);
    const bool par = c & 1;
    bool own[2], ok[2];
#pragma unroll
    for (int e = 0; e < 2; ++e) {
      own[e] = 4 * gq + 2 * e <= (c & 14);
      ok[e] = own[e] || !first;
    }
    const f32x4 z = {0, 0, 0, 0};
    f32x4 s0[2], s1[2];
#pragma unroll
    for (int hh = 0; hh < 2; ++hh) {
      s0[hh] = mk(k0[hh], qm[hh], z);
      s1[hh] = mk(k1[hh], qm[hh], z);
    }
#pragma unroll
    for (int hh = 0; hh < 2; ++hh) {
      const v2u qe = {0u, gq == 2 * HI + hh ? e1.x : 0u};
      s0[hh] = mk(e0, qe, s0[hh]);
      s1[hh] = mk(e1, qe, s1[hh]);
    }
    float sv[2][2], m[2], p[2][2], inv[2];
#pragma unroll
    for (int hh = 0; hh < 2; ++hh) {
      const float a0 = s0[hh].x, a1 = s0[hh].y, a2 = s0[hh].z, a3 = s0[hh].w, b0 = s1[hh].x, b1 = s1[hh].y, b2 = s1[hh].z, b3 = s1[hh].w;
      const float l0 = own[0] ? b0 : a0, h0 = own[0] ? b1 : a1, l1 = own[1] ? b2 : a2, h1 = own[1] ? b3 : a3;
      sv[hh][0] = ok[0] ? (par ? h0 : l0) : -3.0e38f;
      sv[hh][1] = ok[1] ? (par ? h1 : l1) : -3.0e38f;
      m[hh] = fmx(sv[hh][0], sv[hh][1]);
    }
    max1632x2(m[0], m[1]);
    constexpr float SC = 0.23570226039551584f * 1.4426950408889634f;
#pragma unroll
    for (int hh = 0; hh < 2; ++hh) {
      p[hh][0] = __builtin_amdgcn_exp2f((sv[hh][0] - m[hh]) * SC);
      p[hh][1] = __builtin_amdgcn_exp2f((sv[hh][1] - m[hh]) * SC);
      inv[hh] = p[hh][0] + p[hh][1];
    }
    sum1632x2(inv[0], inv[1]);
    f32x4 om[2], ox[2];
#pragma unroll
    for (int hh = 0; hh < 2; ++hh) {
      const float r = __builtin_amdgcn_rcpf(inv[hh]);
      const float q0 = p[hh][0] * r, q1 = p[hh][1] * r;
      const unsigned d0 = pk2(par ? 0.f : q0, par ? q0 : 0.f), d1 = pk2(par ? 0.f : q1, par ? q1 : 0.f);
      const v2u pp = {own[0] ? 0u : d0, own[1] ? 0u : d1}, po = {own[0] ? d0 : 0u, own[1] ? d1 : 0u};
      om[hh] = mk(v1[hh], po, mk(v0[hh], pp, z));
      ox[hh] = mk(x1, po, mk(x0, pp, z));
    }
    const int ob = OFF_O + layer * XBUF + (4 * g + rb) * KS * 1024;
    const v2u a = pk4(om[0]), b = pk4(om[1]);
    st128<SC1>(v4u{a.x, a.y, b.x, b.y}, rs, lane * 16, ob + (2 * j + HI) * 1024);
    if (gq == HI) st64<SC1>(v2u{pk2(ox[0].x, ox[0].y), pk2(ox[1].z, ox[1].w)}, rs, c * 8, ob + 16 * 1024 + (2 * j + HI) * 128);
  }
  STAMP(X, layer * 32 + 5);
#pragma unroll
  for (int i = 0; i < 12; ++i)
    if (i < 11 || w < 2) *(LAS v4u*)(L + B_W + (i * 8 + w) * 1024 + lane * 16) = wt[i];
  DRAIN();
  BAR();
  if (HI && w == 7) signal(cnt_ptr(X.ws, CNT_A, layer, g), lane);
  STAMP(X, layer * 32 + 6);
}

template <int HI>
__device__ __forceinline__ void stage_b(const Ctx& X, int layer, int run, v4u (&a)[ring<HI>()], const v4u (&raw)[8]) {
  LAS unsigned char* L = X.L;
  const rsrc_t rs = X.rs;
  const int g = X.g, j = X.j, w = X.w, lane = X.lane;
  constexpr int kh = HI, NK = HI ? 2 : 3;
  const int rb = w & 3, c = lane & 15, gq = lane >> 4;
  const bool poller = HI && w == 7;
  const int xb = (layer == 0 ? OFF_X0 : OFF_X2) + (4 * g + rb) * KS * 1024;
  int xo[NK];
  v2u res[NK];
#pragma unroll
  for (int i = 0; i < NK; ++i) {
    const int t = kh * 3 + i, d0 = 72 * j + 16 * t + 4 * gq;
    xo[i] = (d0 >> 5) * 1024 + (((d0 >> 3) & 3) * 16 + c) * 16 + ((d0 >> 2) & 1) * 8;
    res[i] = v2u{0, 0};
    if (t < 4 || gq < 2) res[i] = ld64<SC1>(rs, xo[i], xb);
  }
  SB();
  if (layer == 0) rt_store(X, raw);
  v4u wf[6][5];
#pragma unroll
  for (int u = 0; u < 6; ++u)
#pragma unroll
    for (int t = 0; t < 5; ++t) wf[u][t] = *(LAS v4u*)(L + B_W + ((kh * 9 + u) * 5 + t) * 1024 + lane * 16);
  SB();
  if (poller) poll_ge(cnt_ptr(X.ws, CNT_A, layer, g), 8);
  STAMP(X, layer * 32 + 7);
  BAR();
  STAMP(X, layer * 32 + 8);
  v4u of[9];
  const int ob = OFF_O + layer * XBUF + (4 * g + rb) * KS * 1024;
#pragma unroll
  for (int u = 0; u < (kh ? 7 : 9); ++u) of[u] = ld128<SC1>(rs, lane * 16 + (u & 3) * 1024, ob + kh * 9 * 1024 + (u >> 2) * 4096);
  if (kh) {
#pragma unroll
    for (int u = 7; u < 9; ++u) {
      const v2u p = ld64<SC1>(rs, gq * 256 + c * 8 + (u - 7) * 1024, ob + 16 * 1024);
      const v2u q = ld64<SC1>(rs, gq * 256 + c * 8 + (u - 7) * 1024 + 128, ob + 16 * 1024);
      of[u] = v4u{p.x, p.y, q.x, q.y};
    }
  }
  SB();
  f32x4 acc[5];
#pragma unroll
  for (int t = 0; t < 5; ++t) acc[t] = f32x4{0, 0, 0, 0};
#pragma unroll
  for (int u = 0; u < 9; ++u) {
#pragma unroll
    for (int t = 0; t < 5; ++t) {
      acc[t] = mfma16(wf[u % 6][t], of[u], acc[t]);
      if (u < 3) wf[u][t] = *(LAS v4u*)(L + B_W + ((kh * 9 + u + 6) * 5 + t) * 1024 + lane * 16);
    }
    SB();
  }
#pragma unroll
  for (int t = (kh ? 0 : 3); t < (kh ? 3 : 5); ++t) *(LAS f32x4*)(L + B_RED + (rb * 5 + t) * 1024 + lane * 16) = acc[t];
  STAMP(X, layer * 32 + 9);
  BAR();
  const int x1b = OFF_X1 + layer * XBUF + (4 * g + rb) * KS * 1024;
  float rsum = 0.f, rsq2 = 0.f;
#pragma unroll
  for (int i = 0; i < NK; ++i) {
    const int t = kh * 3 + i;
    const f32x4 o = *(LAS f32x4*)(L + B_RED + (rb * 5 + t) * 1024 + lane * 16);
    f32x4 v = o + acc[t];
    v.x += bflo(res[i].x); v.y += bfhi(res[i].x); v.z += bflo(res[i].y); v.w += bfhi(res[i].y);
    const v2u y = pk4(v);
    if (t < 4 || gq < 2) st64<SC1>(y, rs, xo[i], x1b);
    rsum = dot2(y.x, ONES, dot2(y.y, ONES, rsum));
    rsq2 = dot2(y.x, y.x, dot2(y.y, y.y, rsq2));
  }
  sum1632x2(rsum, rsq2);
  if (lane < 16) st64<SC1>(v2u{asu(rsum), asu(rsq2)}, rs, lane * 8, OFF_RS + ((layer * 128 + 4 * g + rb) * 16 + 2 * j + kh) * 128);
  STAMP(X, layer * 32 + 10);
  DRAIN();
  BAR();
  STAMP(X, layer * 32 + 11);
  if (!HI) ring_zero<0, CPF>(a);
  if (poller) {
    gu32* p = cnt_ptr(X.ws, CNT_B, layer, g);
    signal(p, lane);
    if (layer == 0) signal(cnt_ptr(X.ws, CNT_W, 0, j), lane);
    poll_ge(p, 8);
    if (layer == 0) poll_ge(cnt_ptr(X.ws, CNT_W, 0, j), 32);
  } else if (!HI && layer) {
    ring_fill<0, CPF>(X, run, a);
  }
  BAR();
  STAMP(X, layer * 32 + 12);
}

template <int NT, int BASE, int PRE, int R, typename HOOK>
__device__ __forceinline__ void c_gemm(LAS unsigned char* Xl, rsrc_t rs, int run, v4u (&a)[R], f32x4 (&acc)[NT][4], int lane, HOOK hook) {
  constexpr int TOTAL = 2 * KS * NT;
  v4u xf[2][4];
  LAS unsigned char* xp = Xl + lane * 16;
  LAS unsigned char* xq = xp + 2 * KS * 1024;
#define XF(r, u) (*(LAS v4u*)(((r) < 2 ? xp : xq) + (((r) & 1) * KS + (u)) * 1024))
#pragma unroll
  for (int r = 0; r < 4; ++r) xf[0][r] = XF(r, 0);
  SB();
#pragma unroll
  for (int u = 0; u < KS; ++u) {
    hook(u);
#pragma unroll
    for (int t = 0; t < NT; ++t) {
      const int idx = BASE + u * NT + t;
#pragma unroll
      for (int r = 3; r >= 0; --r) {
        if (u == 0) m16z(acc[t][r], a[idx % R], xf[u & 1][r]);
        else m16(acc[t][r], a[idx % R], xf[u & 1][r]);
        if (r >= 2 && t < 2 && u + 1 < KS) { xf[(u + 1) & 1][2 * t + 3 - r] = XF(2 * t + 3 - r, u + 1); SB(); }
      }
      const int l0 = cmin(TOTAL, cmin(idx + R, PRE + 2 * idx)), l1 = cmin(TOTAL, cmin(idx + 1 + R, PRE + 2 * (idx + 1)));
#pragma unroll
      for (int k = 0; k < 2; ++k)
        if (l0 + k < l1) a[(l0 + k) % R] = ld128<SC1>(rs, lane * 16 + ((l0 + k) & 3) * 1024, run + ((l0 + k) >> 2) * 4096);
      SB();
    }
  }
#undef XF
  MFMA_DONE();
}

#define LDS_ARRIVE(p) do { SB(); asm volatile("s_waitcnt lgkmcnt(0)" ::: "memory"); if (lane == 0) __hip_atomic_fetch_add((p), 1u, RLX_WG); SB(); } while (0)
template <int HI>
__device__ __forceinline__ void stage_c(const Ctx& X, int layer, int run, v4u (&a)[ring<HI>()]) {
  constexpr int NT = 5 - HI, R = ring<HI>(), PRE = HI ? HPRE : CPRE;
  LAS unsigned char* L = X.L;
  const rsrc_t rs = X.rs;
  const int g = X.g, j = X.j, w = X.w, lane = X.lane;
  const int c = lane & 15, gq = lane >> 4, rb = w & 3;
  const int T0 = HI ? 20 + 4 * (w - 4) : 5 * w;
  LAS unsigned* c_lo = (LAS unsigned*)(L + L_CNT + 20);
  LAS unsigned* c_hi = (LAS unsigned*)(L + L_CNT + 24);
  LAS unsigned* c_st = (LAS unsigned*)(L + L_CNT + 28);
  LAS unsigned* sig = (LAS unsigned*)(L + L_CNT + 32 + rb * 4);
  const unsigned four = 4 * (layer + 1);
  {
    constexpr int U0 = HI ? 6 : 0, NU = HI ? 12 : 6;
    if (HI) __builtin_amdgcn_s_setprio(2);
    const int xb = OFF_X1 + layer * XBUF + ((4 * g + rb) * KS + U0) * 1024;
    v4u v[NU];
#pragma unroll
    for (int u = 0; u < NU; ++u) v[u] = ld128<SC1>(rs, lane * 16 + (u & 3) * 1024, xb + (u >> 2) * 4096);
    v2u ps[4];
    if (HI) {
#pragma unroll
      for (int k = 0; k < 4; ++k) ps[k] = ld64<SC1>(rs, gq * 512 + k * 128 + c * 8, OFF_RS + (layer * 128 + 4 * g + rb) * 2048);
    }
    SB();
    if (!HI) {
      if (layer == 0) ring_fill<0, CPF>(X, run, a);
      ring_fill<CPF, CPRE>(X, run, a);
    }
#pragma unroll
    for (int u = 0; u < NU; ++u) *(LAS v4u*)(L + C_X + (rb * KS + U0 + u) * 1024 + lane * 16) = v[u];
    LDS_ARRIVE(HI ? c_hi : c_lo);
    STAMP(X, layer * 32 + 16);
    if (HI) {
      if (layer == 0) {
#pragma unroll
        for (int k = 0; k < 3; ++k) {
          const int n = k * 256 + rb * 64 + lane;
          if (k < 2 || rb == 0) {
            float s = 0.f;
#pragma unroll
            for (int q = 0; q < 9; ++q) s += asf(__builtin_amdgcn_raw_buffer_load_b32(rs, n * 4, OFF_CS + (q * F + j * 576) * 4, SC1));
            *(LAS f32x2*)(L + L_CB + n * 8) = f32x2{s, bflo(X.b1[j * 576 + n])};
          }
        }
      }
      float s = (asf(ps[0].x) + asf(ps[1].x)) + (asf(ps[2].x) + asf(ps[3].x)), q = (asf(ps[0].y) + asf(ps[1].y)) + (asf(ps[2].y) + asf(ps[3].y));
      sum1632x2(s, q);
      const float mean = s * (1.f / D);
      const float var = fmx(q * (1.f / D) - mean * mean, 0.f);
      const float rstd = rsq(var + 1e-5f);
      if (lane < 16) *(LAS f32x2*)(L + C_ST + (rb * 16 + c) * 8) = f32x2{rstd, -mean * rstd};
      LDS_ARRIVE(c_st);
      ring_fill<0, PRE>(X, run, a);
      __builtin_amdgcn_s_setprio(0);
      lds_wait(c_lo, four);
      lds_wait(c_hi, four);
      if (SIGK >= 0) lds_wait(sig, layer + 1);
    } else {
      lds_wait(c_lo, four);
    }
    STAMP(X, layer * 32 + 17);
  }
  f32x4 acc[NT][4];
  c_gemm<NT, 0, PRE>(L + C_X, rs, run, a, acc, lane, [&](int u) {
    if (!HI && u == 5) lds_wait(c_hi, four);
    if (!HI && u == SIGK) { __hip_atomic_store(sig, (unsigned)(layer + 1), RLX_WG); SB(); }
  });
  lds_wait(c_st, four);
  STAMP(X, layer * 32 + 19);
  {
    float rstd[4], nm[4];
#pragma unroll
    for (int r = 0; r < 4; ++r) {
      const f32x2 st = *(LAS f32x2*)(L + C_ST + (r * 16 + c) * 8);
      rstd[r] = st.x;
      nm[r] = st.y;
    }
#pragma unroll
    for (int t = 0; t < NT; ++t) {
      const int T = T0 + t;
      const f32x4 cb0 = *(LAS f32x4*)(L + L_CB + (T * 16 + 4 * gq) * 8), cb1 = *(LAS f32x4*)(L + L_CB + (T * 16 + 4 * gq) * 8 + 16);
      const float cs[4] = {cb0.x, cb0.z, cb1.x, cb1.z}, bb[4] = {cb0.y, cb0.w, cb1.y, cb1.w};
#pragma unroll
      for (int r = 0; r < 4; ++r) {
        float o[4];
#pragma unroll
        for (int e = 0; e < 4; ++e) {
          const float v = __builtin_fmaf(acc[t][r][e], rstd[r], __builtin_fmaf(cs[e], nm[r], bb[e]));
          const float p = __builtin_fmaf(v * v, -0.10294324f, -2.3022082f);
          const float ex = __builtin_amdgcn_exp2f(v * p);
          o[e] = v * __builtin_amdgcn_rcpf(1.0f + ex);
        }
        *(LAS v2u*)(L + C_H + (r * KS + (T >> 1)) * 1024 + ((2 * (T & 1) + (gq >> 1)) * 16 + c) * 16 + (gq & 1) * 8) = v2u{pk2(o[0], o[1]), pk2(o[2], o[3])};
      }
    }
  }
  STAMP(X, layer * 32 + 20);
  if (HI) {
    BAR();
  } else {
    LDS_ARRIVE((LAS unsigned*)(L + L_CNT + 8));
    lds_wait((LAS unsigned*)(L + L_CNT + 8), four);
  }
  STAMP(X, layer * 32 + 21);
  c_gemm<NT, KS * NT, PRE>(L + C_H, rs, run, a, acc, lane, [&](int u) {
    if (!HI && u == 9) BAR();
  });
  STAMP(X, layer * 32 + 22);
  const int pb = OFF_P + layer * PBUF + g * 64 * 9216 + j * 9216 + T0 * 256;
  const int pv = (c >> 3) * 8 * 9216 + (c & 7) * 32 + gq * 8;
#pragma unroll
  for (int t = 0; t < NT; ++t)
#pragma unroll
    for (int r = 0; r < 4; ++r) st64<SC1>(pk4(acc[t][r]), rs, pv + t * 256, pb + 2 * r * 8 * 9216);
  STAMP(X, layer * 32 + 23);
  DRAIN();
  if (lane == 0) {
    const unsigned old = __hip_atomic_fetch_add((LAS unsigned*)(L + L_CNT + HI * 4), 1u, RLX_WG);
    if ((old & 3u) == 3u) __hip_atomic_fetch_add(cnt_ptr(X.ws, HI ? CNT_C2 : CNT_C1, layer, g), 1u, RLX_AGENT);
  }
  STAMP(X, layer * 32 + 24);
}

template <int HI, int LAYER, typename PRE>
__device__ __forceinline__ void stage_d(const Ctx& X, PRE pre) {
  constexpr int layer = LAYER;
  LAS unsigned char* L = X.L;
  const rsrc_t rs = X.rs;
  const int g = X.g, j = X.j, w = X.w, lane = X.lane;
  const int pb = OFF_P + layer * PBUF + (g * 8 + j) * 8 * 9216;
  const int RB = 4 * g + (j >> 1);
  const int tok = (lane >> 1) & 7, hf = lane & 1;
  constexpr int I0 = HI ? 2 : 0;
  int fo[3], pp[3];
  v4u xr[3], bv[3], o[3];
  pp[0] = w * 64 + lane;
  pp[1] = 256 + w * 16 + (lane & 15);
  pp[2] = 320 + w * 32 + (lane & 31);
#pragma unroll
  for (int i = I0; i < 3; ++i) {
    const int T = pp[i] >> 4;
    fo[i] = (T >> 1) * 1024 + ((2 * (T & 1) + hf) * 16 + (j & 1) * 8 + tok) * 16;
    xr[i] = ld128<SC1>(rs, fo[i], OFF_X1 + layer * XBUF + RB * KS * 1024);
    bv[i] = ld128<0>(X.rb2, (T * 16 + hf * 8) * 2, 0);
  }
  SB();
  float s = 0.f, q = 0.f;
  auto finish = [&](int i, const float (&l2)[4], const float (&h2)[4], int nl) {
#pragma unroll
    for (int e = 0; e < 4; ++e) o[i][e] = pk2(bflo(bv[i][e]) + bflo(xr[i][e]) + l2[e], bfhi(bv[i][e]) + bfhi(xr[i][e]) + h2[e]);
    if (lane < nl) {
      if (layer == 0) st128<SC1>(o[i], rs, fo[i], OFF_X2 + RB * KS * 1024);
      else st128<0>(o[i], X.rout, ((g * 64 + j * 8 + tok) * D + (pp[i] >> 4) * 16 + hf * 8) * 2, 0);
      if (layer == 0) {
#pragma unroll
        for (int e = 0; e < 4; ++e) {
          const float lo = bflo(o[i][e]), hi = bfhi(o[i][e]);
          s += lo + hi;
          q = __builtin_fmaf(lo, lo, __builtin_fmaf(hi, hi, q));
        }
      }
    }
  };
  if (!HI) {
    poll_ge(cnt_ptr(X.ws, CNT_C1, layer, g), 8);
    STAMP(X, layer * 32 + 25);
    v4u pv[8], pz[2];
#pragma unroll
    for (int k = 0; k < 8; ++k) pv[k] = ld128<SC1>(rs, pp[0] * 16, pb + k * 9216);
#pragma unroll
    for (int k = 0; k < 2; ++k) pz[k] = ld128<SC1>(rs, pp[1] * 16 + (lane >> 4) * 2 * 9216, pb + k * 9216);
    SB();
    float l2[4], h2[4];
#pragma unroll
    for (int e = 0; e < 4; ++e) {
      l2[e] = 0.f; h2[e] = 0.f;
#pragma unroll
      for (int k = 0; k < 8; ++k) { l2[e] += bflo(pv[k][e]); h2[e] += bfhi(pv[k][e]); }
    }
    finish(0, l2, h2, 64);
#pragma unroll
    for (int e = 0; e < 4; ++e) {
      l2[e] = sum1632(bflo(pz[0][e]) + bflo(pz[1][e]));
      h2[e] = sum1632(bfhi(pz[0][e]) + bfhi(pz[1][e]));
    }
    finish(1, l2, h2, 16);
    SB();
  }
  STAMP(X, layer * 32 + 26);
  if (HI && w == 7) poll_ge(cnt_ptr(X.ws, CNT_C2, layer, g), 8);
  else pre();
  BAR();
  STAMP(X, layer * 32 + 27);
  {
    v4u pv[4];
#pragma unroll
    for (int k = 0; k < 4; ++k) pv[k] = ld128<SC1>(rs, pp[2] * 16 + (lane >> 5) * 4 * 9216, pb + k * 9216);
    SB();
    float l2[4], h2[4];
#pragma unroll
    for (int e = 0; e < 4; ++e) {
      l2[e] = 0.f; h2[e] = 0.f;
#pragma unroll
      for (int k = 0; k < 4; ++k) { l2[e] += bflo(pv[k][e]); h2[e] += bfhi(pv[k][e]); }
      l2[e] = sum32(l2[e]);
      h2[e] = sum32(h2[e]);
    }
    finish(2, l2, h2, 32);
  }
  STAMP(X, layer * 32 + 28);
  if (layer == 0) {
    s += asf(__builtin_amdgcn_mov_dpp(asu(s), 0xB1, 0xF, 0xF, true));
    q += asf(__builtin_amdgcn_mov_dpp(asu(q), 0xB1, 0xF, 0xF, true));
    s = sum1632(s);
    q = sum1632(q);
    if (lane < 16 && hf == 0) *(LAS f32x2*)(L + D_ST + (tok * 8 + w) * 8) = f32x2{s, q};
    BAR();
    float ts = 0.f, tq = 0.f;
#pragma unroll
    for (int k = 0; k < 4; ++k) {
      const f32x4 v = *(LAS f32x4*)(L + D_ST + tok * 64 + k * 16);
      ts += v.x + v.z;
      tq += v.y + v.w;
    }
    const float mean = ts * (1.f / D);
    const float var = fmx(tq * (1.f / D) - mean * mean, 0.f);
    const float rstd = rsq(var + 1e-5f), nm = -mean * rstd;
#pragma unroll
    for (int i = I0; i < 3; ++i)
      if (lane < (i == 0 ? 64 : i == 1 ? 16 : 32)) {
        v4u y;
#pragma unroll
        for (int e = 0; e < 4; ++e) y[e] = pk2(__builtin_fmaf(bflo(o[i][e]), rstd, nm), __builtin_fmaf(bfhi(o[i][e]), rstd, nm));
        st128<SC1>(y, rs, fo[i], OFF_XN + XBUF + RB * KS * 1024);
      }
    SB();
  }
}

template <int HI>
__device__ __forceinline__ void body(const Ctx& X) {
  const int run0 = OFF_WC + (X.j * 1296 + (HI ? 720 + (X.w - 4) * 144 : X.w * 180)) * 1024;
  v4u wa[rq<HI>()], wb[rq<HI>()];
  STAMP(X, 0);
  asm volatile("; role %0" ::"i"(HI) : "memory");
#pragma unroll
  for (int u = QPRE; u < rq<HI>(); ++u) { wa[u] = v4u{0, 0, 0, 0}; wb[u] = v4u{0, 0, 0, 0}; }
  wq_prefetch<HI, 0, QPRE>(fresh(X), wa, wb);
#pragma unroll 1
  for (int layer = 0;; ++layer) {
    v4u xt[12], raw[8];
    xn_load<0, 5>(fresh(X), layer, xt);
    BAR();
    xn_load<5, 12>(fresh(X), layer, xt);
    stage_a<HI>(fresh(X), layer, xt, wa, wb, raw);
    v4u a[ring<HI>()];
    int run = run0;
    asm volatile("" : "+s"(run));
    stage_b<HI>(fresh(X), layer, run, a, raw);
    asm volatile("" : "+s"(run));
    stage_c<HI>(fresh(X), layer, run, a);
    if (layer == 1) break;
    wq_prefetch<HI, 0, HI ? QPRE : rq<HI>()>(fresh(X), wa, wb);
    stage_d<HI, 0>(fresh(X), [] {});
    DRAIN();
    STAMP(X, 29);
    BAR();
    if (HI && X.w == 7) {
      signal(cnt_ptr(X.ws, CNT_D, 0, X.g), X.lane);
      poll_ge(cnt_ptr(X.ws, CNT_D, 0, X.g), 8);
      if (X.g > 0) poll_ge(cnt_ptr(X.ws, CNT_D, 0, X.g - 1), 8);
    }
    BAR();
    STAMP(X, 32);
  }
  stage_d<HI, 1>(fresh(X), [] {});
  STAMP(X, 32 + 29);
}

__global__ void __launch_bounds__(NTHR, 2) k_main(unsigned char* ws, const u16* w1, const u16* w2, const u16* b1, const u16* b2, u16* out) {
  extern __shared__ __attribute__((aligned(16))) unsigned char smem[];
  Ctx X;
  X.ws = ws;
  X.w1 = w1;
  X.w2 = w2;
  X.b1 = b1;
  X.L = (LAS unsigned char*)smem;
  X.rs = __builtin_amdgcn_make_buffer_rsrc((void*)ws, 0, WS_BYTES, 0x00020000);
  X.rb2 = __builtin_amdgcn_make_buffer_rsrc((void*)b2, 0, D * 2, 0x00020000);
  X.rout = __builtin_amdgcn_make_buffer_rsrc((void*)out, 0, S * D * 2, 0x00020000);
  X.lane = TIDX & 63;
  X.w = __builtin_amdgcn_readfirstlane(TIDX >> 6);
  const int x = BIDX & 7, i = BIDX >> 3;
  X.g = (x % RA) * (32 / RA) + i / RA;
  X.j = (x / RA) * RA + i % RA;
  if (TIDX < 16) *(LAS unsigned*)(X.L + L_CNT + TIDX * 4) = 0u;
  if (X.w < 4) body<0>(X);
  else body<1>(X);
}
}
#if defined(__HIP_DEVICE_COMPILE__)
#pragma clang attribute pop
#endif

extern "C" void kernel_launch(void* const* d_in, const int* in_sizes, int n_in, void* d_out, int out_size, void* d_ws, size_t ws_size,
                              hipStream_t stream) {
  const u16* x = (const u16*)d_in[0];
  const u16* wqkv = (const u16*)d_in[1];
  const u16* wo = (const u16*)d_in[2];
  const u16* w1 = (const u16*)d_in[4];
  const u16* b1 = (const u16*)d_in[5];
  const u16* w2 = (const u16*)d_in[6];
  const u16* b2 = (const u16*)d_in[7];
  unsigned char* ws = (unsigned char*)d_ws;
  (void)hipFuncSetAttribute((const void*)k_main, hipFuncAttributeMaxDynamicSharedMemorySize, LDS_BYTES);
  k_prep<<<K0_BLOCKS, 256, 0, stream>>>(x, wqkv, wo, ws);
  k_main<<<256, NTHR, LDS_BYTES, stream>>>(ws, w1, w2, b1, b2, (u16*)d_out);
}
```

```cpp
#include <hip/hip_runtime.h>
#include <hip/hip_bf16.h>

#if defined(__HIP_DEVICE_COMPILE__)
#pragma clang attribute push(__attribute__((target("no-packed-fp32-ops"))), apply_to = function)
#endif
namespace {
typedef unsigned short u16;
typedef unsigned v4u __attribute__((ext_vector_type(4)));
typedef unsigned v2u __attribute__((ext_vector_type(2)));
typedef float f32x4 __attribute__((ext_vector_type(4)));
typedef float f32x2 __attribute__((ext_vector_type(2)));
typedef __bf16 bf16x2_t __attribute__((ext_vector_type(2)));
typedef __bf16 bf16x8_t __attribute__((ext_vector_type(8)));
#define GAS __attribute__((address_space(1)))
#define LAS __attribute__((address_space(3)))
typedef __amdgpu_buffer_rsrc_t rsrc_t;

constexpr int S = 2048, D = 576, F = 4608;
constexpr int KS = 18;
constexpr int NTHR = 512;
constexpr int XBUF = 128 * KS * 1024;
constexpr int OFF_CTRL = 0;
constexpr int OFF_CS = 65536;
constexpr int OFF_RS = OFF_CS + 3 * 65536;
constexpr int OFF_WQ = OFF_RS + 2 * 128 * 2048;
constexpr int OFF_WO = OFF_WQ + 8 * 14 * KS * 1024;
constexpr int OFF_WC = OFF_WO + 8 * 90 * 1024;
constexpr int OFF_X0 = OFF_WC + 8 * 1296 * 1024;
constexpr int OFF_XN = OFF_X0 + XBUF;
constexpr int OFF_O = OFF_XN + 2 * XBUF;
constexpr int OFF_X1 = OFF_O + 2 * XBUF;
constexpr int OFF_X2 = OFF_X1 + 2 * XBUF;
constexpr int OFF_P = OFF_X2 + XBUF;
constexpr int PBUF = 8 * XBUF;
constexpr int WS_BYTES = OFF_P + 2 * PBUF;

__device__ __forceinline__ f32x4 mfma16(v4u a, v4u b, f32x4 c) {
  return __builtin_amdgcn_mfma_f32_16x16x32_bf16(__builtin_bit_cast(bf16x8_t, a), __builtin_bit_cast(bf16x8_t, b), c, 0, 0, 0);
}
__device__ __forceinline__ void m16(f32x4& c, v4u a, v4u b) { asm volatile("v_mfma_f32_16x16x32_bf16 %0, %1, %2, %0" : "+v"(c) : "v"(a), "v"(b)); }
__device__ __forceinline__ void m16z(f32x4& c, v4u a, v4u b) { asm volatile("v_mfma_f32_16x16x32_bf16 %0, %1, %2, 0" : "=&v"(c) : "v"(a), "v"(b)); }
template <bool Z> __device__ __forceinline__ void m16t(f32x4& c, v4u a, v4u b) { if (Z) m16z(c, a, b); else m16(c, a, b); }
__device__ __forceinline__ unsigned pk2(float lo, float hi) {
  f32x2 v = {lo, hi};
  return __builtin_bit_cast(unsigned, __builtin_convertvector(v, bf16x2_t));
}
__device__ __forceinline__ float asf(unsigned u) { return __builtin_bit_cast(float, u); }
__device__ __forceinline__ unsigned asu(float f) { return __builtin_bit_cast(unsigned, f); }
__device__ __forceinline__ float bflo(unsigned u) { return asf(u << 16); }
__device__ __forceinline__ float bfhi(unsigned u) { return asf(u & 0xffff0000u); }
__device__ __forceinline__ v2u pk4(f32x4 v) { return v2u{pk2(v.x, v.y), pk2(v.z, v.w)}; }
__device__ __forceinline__ float dot2(unsigned a, unsigned b, float c) {
  return __builtin_amdgcn_fdot2_f32_bf16(__builtin_bit_cast(bf16x2_t, a), __builtin_bit_cast(bf16x2_t, b), c, false);
}
constexpr unsigned ONES = 0x3f803f80u;

template <int AUX> __device__ __forceinline__ v4u ld128(rsrc_t rs, int voff, int soff) { return __builtin_amdgcn_raw_buffer_load_b128(rs, voff, soff, AUX); }
template <int AUX> __device__ __forceinline__ v2u ld64(rsrc_t rs, int voff, int soff) { return __builtin_amdgcn_raw_buffer_load_b64(rs, voff, soff, AUX); }
template <int AUX> __device__ __forceinline__ void st128(v4u v, rsrc_t rs, int voff, int soff) { __builtin_amdgcn_raw_buffer_store_b128(v, rs, voff + soff, 0, AUX); }
template <int AUX> __device__ __forceinline__ void st64(v2u v, rsrc_t rs, int voff, int soff) { __builtin_amdgcn_raw_buffer_store_b64(v, rs, voff, soff, AUX); }
template <int AUX> __device__ __forceinline__ void st32(unsigned v, rsrc_t rs, int voff, int soff) { __builtin_amdgcn_raw_buffer_store_b32(v, rs, voff, soff, AUX); }
#define SB() __builtin_amdgcn_sched_barrier(0)
#define MFMA_DONE() do { SB(); asm volatile("s_nop 7\n\ts_nop 1" ::: "memory"); SB(); } while (0)
#define TIDX ((int)__builtin_amdgcn_workitem_id_x())
#define BIDX ((int)__builtin_amdgcn_workgroup_id_x())
#define SYNC() do { __builtin_amdgcn_fence(__ATOMIC_RELEASE, "workgroup"); __builtin_amdgcn_s_barrier(); __builtin_amdgcn_fence(__ATOMIC_ACQUIRE, "workgroup"); } while (0)
__device__ __forceinline__ float fmx(float a, float b) { return __builtin_fmaxf(a, b); }
__device__ __forceinline__ float rsq(float a) { return __builtin_amdgcn_rsqf(a); }

__device__ __forceinline__ float sum32(float v) {
  const auto b = __builtin_amdgcn_permlane32_swap(asu(v), asu(v), false, false);
  const unsigned b0 = b[0], b1 = b[1];
  return asf(b0) + asf(b1);
}
__device__ __forceinline__ float sum1632(float v) {
  const auto a = __builtin_amdgcn_permlane16_swap(asu(v), asu(v), false, false);
  const unsigned a0 = a[0], a1 = a[1];
  return sum32(asf(a0) + asf(a1));
}
__device__ __forceinline__ float max1632(float v) {
  const auto a = __builtin_amdgcn_permlane16_swap(asu(v), asu(v), false, false);
  const unsigned a0 = a[0], a1 = a[1];
  v = fmx(asf(a0), asf(a1));
  const auto b = __builtin_amdgcn_permlane32_swap(asu(v), asu(v), false, false);
  const unsigned b0 = b[0], b1 = b[1];
  return fmx(asf(b0), asf(b1));
}

template <typename OP> __device__ __forceinline__ void red1632x2(float& a, float& b, OP op) {
  const auto s = __builtin_amdgcn_permlane16_swap(asu(a), asu(b), false, false);
  const unsigned s0 = s[0], s1 = s[1];
  const float t = op(asf(s0), asf(s1));
  const auto u = __builtin_amdgcn_permlane32_swap(asu(t), asu(t), false, false);
  const unsigned u0 = u[0], u1 = u[1];
  const float v = op(asf(u0), asf(u1));
  const auto x = __builtin_amdgcn_permlane16_swap(asu(v), asu(v), false, false);
  const unsigned x0 = x[0], x1 = x[1];
  a = asf(x0);
  b = asf(x1);
}
__device__ __forceinline__ void max1632x2(float& a, float& b) { red1632x2(a, b, [](float x, float y) { return fmx(x, y); }); }
__device__ __forceinline__ void sum1632x2(float& a, float& b) { red1632x2(a, b, [](float x, float y) { return x + y; }); }

__device__ __forceinline__ void tr8x8(const v4u (&r)[8], v4u (&o)[8]) {
#pragma unroll
  for (int c = 0; c < 4; ++c) {
#pragma unroll
    for (int p = 0; p < 4; ++p) {
      o[2 * c][p] = __builtin_amdgcn_perm(r[2 * p + 1][c], r[2 * p][c], 0x05040100u);
      o[2 * c + 1][p] = __builtin_amdgcn_perm(r[2 * p + 1][c], r[2 * p][c], 0x07060302u);
    }
  }
}
constexpr int K0_WO = 72 * 80, K0_WQ = 72 * 896;
constexpr int K0_BO = (K0_WO + 255) / 256, K0_BQ = K0_WQ / 256, K0_BX = 128;
constexpr int K0_BLOCKS = K0_BQ + K0_BX + K0_BO;

__global__ void __launch_bounds__(256, 4) k_prep(const u16* __restrict__ x, const u16* __restrict__ wqkv, const u16* __restrict__ wo,
                                                 unsigned char* __restrict__ ws) {
  __shared__ __attribute__((aligned(16))) unsigned char sm[512];
  int b = BIDX;
  const int tid = TIDX;
  if (b < 16) ((v4u*)(ws + OFF_CTRL))[b * 256 + tid] = v4u{0, 0, 0, 0};
  if (b >= 128 && b < 128 + 15 * 8) {
    unsigned long long pc;
    asm volatile("s_getpc_b64 s[20:21]\n\ts_add_u32 s20, s20, _ZN12_GLOBAL__N_16k_mainEPhPKtS2_S2_S2_Pt@rel32@lo+4\n\ts_addc_u32 s21, s21, _ZN12_GLOBAL__N_16k_mainEPhPKtS2_S2_S2_Pt@rel32@hi+12\n\ts_mov_b64 %0, s[20:21]" : "=s"(pc) : : "scc", "s20", "s21");
    const GAS v4u* cp = (const GAS v4u*)pc + (((b - 128) >> 3) * 256 + tid);
    v4u t = *cp;
    asm volatile("" ::"v"(t));
  }
  if (b < K0_BX) {
    const int wv = tid >> 6, RB = b, lane = tid & 63, c = lane & 15, gq = lane >> 4;
    v4u v[5];
#pragma unroll
    for (int i = 0; i < 5; ++i) {
      v[i] = v4u{0, 0, 0, 0};
      if (i < 4 || wv < 2) v[i] = *(const v4u*)(x + (size_t)(RB * 16 + c) * D + (i * 4 + wv) * 32 + gq * 8);
    }
    float s = 0.f, q = 0.f;
#pragma unroll
    for (int i = 0; i < 5; ++i) {
      if (i < 4 || wv < 2) *(v4u*)(ws + OFF_X0 + ((size_t)(RB * KS + i * 4 + wv) * 64 + lane) * 16) = v[i];
#pragma unroll
      for (int p = 0; p < 4; ++p) { s = dot2(v[i][p], ONES, s); q = dot2(v[i][p], v[i][p], q); }
    }
    s = sum1632(s);
    q = sum1632(q);
    if (lane < 16) *(f32x2*)(sm + (c * 4 + wv) * 8) = f32x2{s, q};
    SYNC();
    const f32x4 a0 = *(const f32x4*)(sm + c * 32), a1 = *(const f32x4*)(sm + c * 32 + 16);
    const float mean = (a0.x + a0.z + a1.x + a1.z) * (1.f / D);
    const float var = fmx((a0.y + a0.w + a1.y + a1.w) * (1.f / D) - mean * mean, 0.f);
    const float rstd = rsq(var + 1e-5f), nm = -mean * rstd;
#pragma unroll
    for (int i = 0; i < 5; ++i) {
      v4u o;
#pragma unroll
      for (int p = 0; p < 4; ++p) o[p] = pk2(__builtin_fmaf(bflo(v[i][p]), rstd, nm), __builtin_fmaf(bfhi(v[i][p]), rstd, nm));
      if (i < 4 || wv < 2) *(v4u*)(ws + OFF_XN + ((size_t)(RB * KS + i * 4 + wv) * 64 + lane) * 16) = o;
    }
    return;
  }
  b -= K0_BX;
  if (b < K0_BQ) {
    const int i = b * 256 + tid, kg = i / 896, cp = i % 896;
    const int u = kg >> 2, gq = kg & 3;
    if (cp < 864) {
      unsigned r[8];
#pragma unroll
      for (int q = 0; q < 8; ++q) r[q] = *(const unsigned*)(wqkv + (size_t)(kg * 8 + q) * 1728 + cp * 2);
      v4u o0, o1;
#pragma unroll
      for (int p = 0; p < 4; ++p) {
        o0[p] = __builtin_amdgcn_perm(r[2 * p + 1], r[2 * p], 0x05040100u);
        o1[p] = __builtin_amdgcn_perm(r[2 * p + 1], r[2 * p], 0x07060302u);
      }
      const int type = cp / 288, hp = cp % 288, head = hp / 9, dp = hp % 9, j = head >> 2, h = head & 3;
      int blk, m;
      if (dp < 8) { blk = type * 4 + h; m = dp * 2; }
      else if (type < 2) { blk = 12; m = 4 * h + 2 * type; }
      else { blk = 13; m = 2 * h; }
      v4u* dst = (v4u*)(ws + OFF_WQ + ((size_t)((j * 14 + blk) * KS + u) * 64 + gq * 16 + m) * 16);
      dst[0] = o0;
      dst[1] = o1;
    } else {
      const int z = cp - 864, j = z >> 2, h = z & 3;
      v4u* dst = (v4u*)(ws + OFF_WQ + ((size_t)((j * 14 + 13) * KS + u) * 64 + gq * 16 + 8 + 2 * h) * 16);
      dst[0] = v4u{0, 0, 0, 0};
      dst[1] = v4u{0, 0, 0, 0};
    }
    return;
  }
  b -= K0_BQ;
  {
    const int i = b * 256 + tid;
    if (i >= K0_WO) return;
    const int kg = i / 80, nb = i % 80, j = nb / 10, q8 = nb % 10, u = kg >> 2, gq = kg & 3;
    v4u o[8];
    if (q8 < 9) {
      v4u r[8];
#pragma unroll
      for (int jj = 0; jj < 8; ++jj) {
        int row;
        if (u < 16) row = (2 * u + (jj >> 2)) * 18 + 4 * gq + (jj & 3);
        else row = (2 * (8 * (u - 16) + 2 * gq + (jj >> 2)) + ((jj >> 1) & 1)) * 18 + 16 + (jj & 1);
        r[jj] = *(const v4u*)(wo + (size_t)row * D + j * 72 + q8 * 8);
      }
      tr8x8(r, o);
    } else {
#pragma unroll
      for (int c = 0; c < 8; ++c) o[c] = v4u{0, 0, 0, 0};
    }
    v4u* dst = (v4u*)(ws + OFF_WO + ((size_t)((j * KS + u) * 5 + (q8 >> 1)) * 64 + gq * 16 + (q8 & 1) * 8) * 16);
#pragma unroll
    for (int c = 0; c < 8; ++c) dst[c] = o[c];
  }
}

#define RA 2
#define RING_LO 24
#define RING_HI 30
#define QRING_LO 14
#define QRING 8
#define HPRE 12
#define SIGK 12
template <int HI> constexpr int ring() { return HI ? RING_HI : RING_LO; }
template <int HI> constexpr int rq() { return HI ? QRING : QRING_LO; }
constexpr int QPRE = 3;
constexpr int CPF = 4;
constexpr int CPRE = 12;
constexpr int cmin(int a, int b) { return a < b ? a : b; }
constexpr int SC1 = 16;
constexpr int A_XT = 0;
constexpr int A_Q = 5 * KS * 1024;
constexpr int A_K = A_Q + 16 * 512;
constexpr int A_V = A_K + 20 * 512;
constexpr int A_EQK = A_V + 20 * 512;
constexpr int A_EV = A_EQK + 5 * 512;
constexpr int B_W = 0, B_SCR = 96 * 1024, B_RED = 124 * 1024;
constexpr int C_X = 0, C_H = 4 * KS * 1024, C_ST = 8 * KS * 1024;
constexpr int L_CB = C_ST + 1024;
constexpr int D_ST = L_CB + 576 * 8;
constexpr int L_CNT = D_ST + 512;
constexpr int L_STAMP = L_CNT + 64;
#define STAMPCLK() __builtin_amdgcn_s_memrealtime()
constexpr int LDS_BYTES = L_STAMP;
#define STAMP(X_, slot) do {} while (0)
enum { CNT_A = 0, CNT_B = 1, CNT_C1 = 2, CNT_C2 = 3, CNT_D = 4, CNT_W = 5 };
typedef GAS unsigned gu32;
__device__ __forceinline__ gu32* cnt_ptr(unsigned char* ws, int kind, int layer, int g) { return (gu32*)(ws + OFF_CTRL) + ((kind * 2 + layer) * 32 + g) * 32; }
#define RLX_AGENT __ATOMIC_RELAXED, __HIP_MEMORY_SCOPE_AGENT
#define RLX_WG __ATOMIC_RELAXED, __HIP_MEMORY_SCOPE_WORKGROUP
#define BAR() do { SB(); asm volatile("s_waitcnt lgkmcnt(0)\n\ts_barrier" ::: "memory"); SB(); } while (0)
#define DRAIN() do { SB(); asm volatile("s_waitcnt vmcnt(0)" ::: "memory"); SB(); } while (0)
__device__ __forceinline__ void poll_ge(gu32* p, unsigned want) {
#pragma unroll 1
  for (unsigned spins = 0; spins < (1u << 22); ++spins) {
    const unsigned v = __builtin_amdgcn_readfirstlane(__hip_atomic_load(p, RLX_AGENT));
    if (v >= want) break;
    __builtin_amdgcn_s_sleep(1);
  }
  __builtin_amdgcn_fence(__ATOMIC_ACQUIRE, "wavefront");
  asm volatile("" ::: "memory");
}
__device__ __forceinline__ void signal(gu32* p, int lane) {
  if (lane == 0) __hip_atomic_fetch_add(p, 1u, RLX_AGENT);
}
__device__ __forceinline__ void lds_wait(LAS unsigned* p, unsigned want) {
#pragma unroll 1
  for (unsigned spins = 0; spins < (1u << 24); ++spins) {
    const unsigned v = __builtin_amdgcn_readfirstlane(__hip_atomic_load(p, RLX_WG));
    if (v >= want) break;
    __builtin_amdgcn_s_sleep(1);
  }
  asm volatile("" ::: "memory");
  SB();
}

struct Ctx {
  unsigned char* ws;
  const u16* w1;
  const u16* w2;
  const u16* b1;
  LAS unsigned char* L;
  rsrc_t rs, rb2, rout;
  int g, j, w, lane;
};
__device__ __forceinline__ Ctx fresh(const Ctx& X) {
  Ctx Y = X;
  asm volatile("" : "+v"(Y.lane));
  asm volatile("" : "+s"(Y.g), "+s"(Y.j), "+s"(Y.w));
  return Y;
}

__device__ __forceinline__ int rt_task(const Ctx& X) { return X.w < 5 ? X.g * 5 + X.w : (X.w == 5 && X.g < 2 ? 160 + X.g : -1); }
#define RTU 16
__device__ __forceinline__ void rt_load(const Ctx& X, int layer, v4u (&r)[8]) {
  const int task = layer == 0 ? rt_task(X) : -1;
  const bool is1 = task < 81;
  const int tt = is1 ? task : task - 81, chunk = tt / 9, strip = tt - 9 * chunk;
  const u16* src = is1 ? X.w1 + (size_t)chunk * 64 * F + X.j * 576 + strip * 64 : X.w2 + (size_t)(X.j * 576 + chunk * 64) * D + strip * 64;
  const int ld = is1 ? F : D;
  const rsrc_t rw = __builtin_amdgcn_make_buffer_rsrc((void*)src, 0, task < 0 ? 0 : (63 * ld + 64) * 2, 0x00020000);
  const int vo = ((X.lane >> 3) * 8 * ld + (X.lane & 7) * 8) * 2;
#pragma unroll
  for (int jj = 0; jj < 8; ++jj) r[jj] = v4u{0, 0, 0, 0};
  if (task >= 0) {
#pragma unroll
    for (int jj = 0; jj < 8; ++jj) r[jj] = ld128<0>(rw, vo, jj * ld * 2);
  }
  SB();
}
__device__ __forceinline__ void rt_store(const Ctx& X, const v4u (&r)[8]) {
  const int task = rt_task(X);
  if (task < 0) return;
  const bool is1 = task < 81;
  const int tt = is1 ? task : task - 81, chunk = tt / 9, strip = tt - 9 * chunk;
  const int lane = X.lane, kg = lane >> 3, nb = lane & 7;
  LAS unsigned char* sm = X.L + B_SCR + X.w * 8192;
  v4u o[8];
  tr8x8(r, o);
  const int base = (((nb >> 1) * 2 + (kg >> 2)) * 64 + (kg & 3) * 16 + (nb & 1) * 8) * 16;
#pragma unroll
  for (int c = 0; c < 8; ++c) *(LAS v4u*)(sm + base + ((c ^ nb) * 16)) = o[c];
  asm volatile("s_waitcnt lgkmcnt(0)" ::: "memory");
  float keep = 0.f;
#pragma unroll
  for (int tl = 0; tl < 4; ++tl) {
    const int T = strip * 4 + tl;
    const int wv = T < 20 ? T / 5 : 4 + ((T - 20) >> 2), nt = T < 20 ? 5 : 4, t = T < 20 ? T - 5 * wv : (T & 3);
    const int fr0 = X.j * 1296 + (T < 20 ? wv * 180 : 720 + (wv - 4) * 144) + ((is1 ? 0 : KS) + chunk * 2) * nt + t;
    float s = 0.f;
#pragma unroll
    for (int u = 0; u < 2; ++u) {
      const v4u v = *(LAS v4u*)(sm + ((tl * 2 + u) * 64 + (lane & ~7) + ((lane & 7) ^ (tl * 2 + ((lane >> 3) & 1)))) * 16);
      st128<SC1>(v, X.rs, lane * 16, OFF_WC + (fr0 + u * nt) * 1024);
#pragma unroll
      for (int p = 0; p < 4; ++p) s = dot2(v[p], ONES, s);
    }
    s = sum1632(s);
    if ((lane >> 4) == tl) keep = s;
  }
  if (is1) st32<SC1>(asu(keep), X.rs, lane * 4, OFF_CS + (chunk * F + X.j * 576 + strip * 64) * 4);
  SB();
}

template <int HI> __device__ __forceinline__ int wq_a(const Ctx& X) { return OFF_WQ + (X.j * 14 + (HI ? X.w + 4 : X.w)) * KS * 1024; }
template <int HI> __device__ __forceinline__ int wq_b(const Ctx& X) { return OFF_WQ + (X.j * 14 + (HI ? 12 + ((X.w - 4) >> 1) : X.w + 4)) * KS * 1024; }
template <int HI, int U0, int U1>
__device__ __forceinline__ void wq_prefetch(const Ctx& X, v4u (&wa)[rq<HI>()], v4u (&wb)[rq<HI>()]) {
  const int sa = wq_a<HI>(X), sb = wq_b<HI>(X);
#pragma unroll
  for (int u = U0; u < U1; ++u) {
    wa[u] = ld128<0>(X.rs, X.lane * 16 + (u & 3) * 1024, sa + (u >> 2) * 4096);
    wb[u] = ld128<0>(X.rs, X.lane * 16 + (u & 3) * 1024, sb + (u >> 2) * 4096);
  }
  SB();
}

template <int I0, int I1>
__device__ __forceinline__ void xn_load(const Ctx& X, int layer, v4u (&t)[12]) {
  const int base = OFF_XN + layer * XBUF + (4 * X.g - 1) * KS * 1024;
#pragma unroll
  for (int i = I0; i < I1; ++i) {
    const int f = i < 11 ? 8 * i + X.w : (X.w < 2 ? 88 + X.w : 89), u = f / 5, rb5 = f - 5 * u;
    t[i] = ld128<SC1>(X.rs, X.lane * 16, base + ((X.g == 0 && rb5 == 0 ? 1 : rb5) * KS + u) * 1024);
  }
  SB();
}

template <int LO, int HIX, int R>
__device__ __forceinline__ void ring_fill(const Ctx& X, int run, v4u (&a)[R]) {
#pragma unroll
  for (int i = LO; i < HIX; ++i) a[i] = ld128<SC1>(X.rs, X.lane * 16 + (i & 3) * 1024, run + (i >> 2) * 4096);
  SB();
}
template <int LO, int HIX, int R>
__device__ __forceinline__ void ring_zero(v4u (&a)[R]) {
#pragma unroll
  for (int i = LO; i < HIX; ++i) a[i] = v4u{0, 0, 0, 0};
}

template <int HI>
__device__ __forceinline__ void stage_a(const Ctx& X, int layer, const v4u (&t)[12], v4u (&wa)[rq<HI>()], v4u (&wb)[rq<HI>()], v4u (&raw)[8]) {
  constexpr int RQ = rq<HI>();
  LAS unsigned char* L = X.L;
  const rsrc_t rs = X.rs;
  const int g = X.g, j = X.j, w = X.w, lane = X.lane;
#pragma unroll
  for (int i = 0; i < 5; ++i) *(LAS v4u*)(L + A_XT + (8 * i + w) * 1024 + lane * 16) = t[i];
  STAMP(X, layer * 32 + 1);
  BAR();
  STAMP(X, layer * 32 + 2);
  v4u wt[12];
  {
    constexpr int A0 = HI ? 0 : 1, B1 = HI ? 3 : 5;
    const int h = w & 3;
    const int sa = wq_a<HI>(X), sb = wq_b<HI>(X);
    f32x4 ac[5], bc[5];
    v4u xf[2][5];
    const int rot = HI ? (w & 1) * 2 : 0;
    LAS unsigned char* xa = L + A_XT + lane * 16 + rot * 1024;
    LAS unsigned char* xb = L + A_XT + lane * 16 + (rot ? 0 : 3) * 1024;
    LAS unsigned char* xa2 = xa + 12 * 5120;
    LAS unsigned char* xb2 = xb + 12 * 5120;
#define XF(r, u) (*(LAS v4u*)(((u) < 12 ? ((r) < 3 ? xa : xb) : ((r) < 3 ? xa2 : xb2)) + ((u) < 12 ? (u) : (u) - 12) * 5120 + ((r) < 3 ? (r) : (r) - 3) * 1024))
#pragma unroll
    for (int r = 0; r < 5; ++r) xf[0][r] = XF(r, 0);
#pragma unroll
    for (int r = 0; r < 5; ++r) xf[1][r] = XF(r, 1);
    SB();
#pragma unroll
    for (int u = 0; u < KS; ++u) {
      if (u == 1) {
#pragma unroll
        for (int i = 5; i < 12; ++i)
          if (i < 11 || w < 2) *(LAS v4u*)(L + A_XT + (8 * i + w) * 1024 + lane * 16) = t[i];
        SB();
      }
      if (u == 2) { SB(); asm volatile("s_waitcnt lgkmcnt(0)" ::: "memory"); if (lane == 0) __hip_atomic_fetch_add((LAS unsigned*)(L + L_CNT + 16), 1u, RLX_WG); SB(); }
      if (u == 6) lds_wait((LAS unsigned*)(L + L_CNT + 16), 8 * (layer + 1));
#pragma unroll
      for (int r = 0; r < 5; ++r) {
        if (r >= A0) {
          if (u == 0) { if (HI) m16z(ac[r], xf[u & 1][r], wa[u % RQ]); else m16z(ac[r], wa[u % RQ], xf[u & 1][r]); }
          else { if (HI) m16(ac[r], xf[u & 1][r], wa[u % RQ]); else m16(ac[r], wa[u % RQ], xf[u & 1][r]); }
        }
        if (r < B1) { if (u == 0) m16z(bc[r], wb[u % RQ], xf[u & 1][r]); else m16(bc[r], wb[u % RQ], xf[u & 1][r]); }
        if (u + 2 < KS) xf[u & 1][r] = XF(r, u + 2);
        SB();
      }
      {
        const int l0 = u == 0 ? QPRE : cmin(KS, cmin(u + RQ, QPRE + 2 * u)), l1 = cmin(KS, cmin(u + 1 + RQ, QPRE + 2 * (u + 1)));
#pragma unroll
        for (int l = QPRE; l < KS; ++l)
          if (l >= l0 && l < l1 && (HI || l >= RQ || layer == 0)) {
            wa[l % RQ] = ld128<0>(rs, lane * 16 + (l & 3) * 1024, sa + (l >> 2) * 4096);
            wb[l % RQ] = ld128<0>(rs, lane * 16 + (l & 3) * 1024, sb + (l >> 2) * 4096);
          }
        if (u >= 10 && u < 16) {
#pragma unroll
          for (int i = 2 * (u - 10); i < 2 * (u - 10) + 2; ++i) wt[i] = ld128<0>(rs, lane * 16, OFF_WO + (j * 90 + i * 8 + w) * 1024);
        }
        if (u == RTU) rt_load(X, layer, raw);
        SB();
      }
    }
#undef XF
    MFMA_DONE();
    if (!HI) {
#pragma unroll
      for (int r = 1; r < 5; ++r) *(LAS v2u*)(L + A_Q + (h * 4 + r - 1) * 512 + lane * 8) = pk4(ac[r]);
#pragma unroll
      for (int r = 0; r < 5; ++r) *(LAS v2u*)(L + A_K + (h * 5 + r) * 512 + lane * 8) = pk4(bc[r]);
    } else {
#pragma unroll
      for (int r = 0; r < 5; ++r) *(LAS v2u*)(L + A_V + (h * 5 + (r < 3 ? rot + r : (rot ? 0 : 3) + r - 3)) * 512 + lane * 8) = pk4(ac[r]);
      if (w < 6) {
#pragma unroll
        for (int r = 0; r < 3; ++r) *(LAS v2u*)(L + A_EQK + (rot + r) * 512 + lane * 8) = pk4(bc[r]);
      } else {
        const int c = lane & 15, gq = lane >> 4;
#pragma unroll
        for (int r = 0; r < 3; ++r) {
          const v2u v = pk4(bc[r]);
          LAS u16* p = (LAS u16*)(L + A_EV + (rot + r) * 512 + ((c >> 2) * 16 + 4 * gq) * 8 + (c & 3) * 2);
          p[0] = (u16)v.x;
          p[4] = (u16)(v.x >> 16);
          p[8] = (u16)v.y;
          p[12] = (u16)(v.y >> 16);
        }
      }
    }
  }
  STAMP(X, layer * 32 + 3);
  BAR();
  STAMP(X, layer * 32 + 4);
  {
    const int c = lane & 15, gq = lane >> 4, rb = w & 3;
    typedef short s16x4 __attribute__((ext_vector_type(4)));
    auto mk = [](v2u a, v2u b, f32x4 cc) {
      return __builtin_amdgcn_mfma_f32_16x16x16bf16_1k(__builtin_bit_cast(s16x4, a), __builtin_bit_cast(s16x4, b), cc, 0, 0, 0);
    };
    LAS unsigned char* lp = L + rb * 512 + lane * 8;
    const v2u e0 = *(LAS v2u*)(lp + A_EQK), e1 = *(LAS v2u*)(lp + A_EQK + 512);
    v2u qm[2], k0[2], k1[2], v0[2], v1[2];
#pragma unroll
    for (int hh = 0; hh < 2; ++hh) {
      const int h = 2 * HI + hh;
      qm[hh] = *(LAS v2u*)(lp + A_Q + h * 4 * 512);
      k0[hh] = *(LAS v2u*)(lp + A_K + h * 5 * 512);
      k1[hh] = *(LAS v2u*)(lp + A_K + (h * 5 + 1) * 512);
    }
#pragma unroll
    for (int hh = 0; hh < 2; ++hh) {
      const int h = 2 * HI + hh;
      v0[hh] = *(LAS v2u*)(lp + A_V + h * 5 * 512);
      v1[hh] = *(LAS v2u*)(lp + A_V + (h * 5 + 1) * 512);
    }
    const v2u x0 = *(LAS v2u*)(lp + A_EV), x1 = *(LAS v2u*)(lp + A_EV + 512);
    SB();
    const bool first = (g == 0 && rb == 0);
    const bool par = c & 1;
    bool own[2], ok[2];
#pragma unroll
    for (int e = 0; e < 2; ++e) {
      own[e] = 4 * gq + 2 * e <= (c & 14);
      ok[e] = own[e] || !first;
    }
    const f32x4 z = {0, 0, 0, 0};
    f32x4 s0[2], s1[2];
#pragma unroll
    for (int hh = 0; hh < 2; ++hh) {
      s0[hh] = mk(k0[hh], qm[hh], z);
      s1[hh] = mk(k1[hh], qm[hh], z);
    }
#pragma unroll
    for (int hh = 0; hh < 2; ++hh) {
      const v2u qe = {0u, gq == 2 * HI + hh ? e1.x : 0u};
      s0[hh] = mk(e0, qe, s0[hh]);
      s1[hh] = mk(e1, qe, s1[hh]);
    }
    float sv[2][2], m[2], p[2][2], inv[2];
#pragma unroll
    for (int hh = 0; hh < 2; ++hh) {
      const float a0 = s0[hh].x, a1 = s0[hh].y, a2 = s0[hh].z, a3 = s0[hh].w, b0 = s1[hh].x, b1 = s1[hh].y, b2 = s1[hh].z, b3 = s1[hh].w;
      const float l0 = own[0] ? b0 : a0, h0 = own[0] ? b1 : a1, l1 = own[1] ? b2 : a2, h1 = own[1] ? b3 : a3;
      sv[hh][0] = ok[0] ? (par ? h0 : l0) : -3.0e38f;
      sv[hh][1] = ok[1] ? (par ? h1 : l1) : -3.0e38f;
      m[hh] = fmx(sv[hh][0], sv[hh][1]);
    }
    max1632x2(m[0], m[1]);
    constexpr float SC = 0.23570226039551584f * 1.4426950408889634f;
#pragma unroll
    for (int hh = 0; hh < 2; ++hh) {
      p[hh][0] = __builtin_amdgcn_exp2f((sv[hh][0] - m[hh]) * SC);
      p[hh][1] = __builtin_amdgcn_exp2f((sv[hh][1] - m[hh]) * SC);
      inv[hh] = p[hh][0] + p[hh][1];
    }
    sum1632x2(inv[0], inv[1]);
    f32x4 om[2], ox[2];
#pragma unroll
    for (int hh = 0; hh < 2; ++hh) {
      const float r = __builtin_amdgcn_rcpf(inv[hh]);
      const float q0 = p[hh][0] * r, q1 = p[hh][1] * r;
      const unsigned d0 = pk2(par ? 0.f : q0, par ? q0 : 0.f), d1 = pk2(par ? 0.f : q1, par ? q1 : 0.f);
      const v2u pp = {own[0] ? 0u : d0, own[1] ? 0u : d1}, po = {own[0] ? d0 : 0u, own[1] ? d1 : 0u};
      om[hh] = mk(v1[hh], po, mk(v0[hh], pp, z));
      ox[hh] = mk(x1, po, mk(x0, pp, z));
    }
    const int ob = OFF_O + layer * XBUF + (4 * g + rb) * KS * 1024;
    const v2u a = pk4(om[0]), b = pk4(om[1]);
    st128<SC1>(v4u{a.x, a.y, b.x, b.y}, rs, lane * 16, ob + (2 * j + HI) * 1024);
    if (gq == HI) st64<SC1>(v2u{pk2(ox[0].x, ox[0].y), pk2(ox[1].z, ox[1].w)}, rs, c * 8, ob + 16 * 1024 + (2 * j + HI) * 128);
  }
  STAMP(X, layer * 32 + 5);
#pragma unroll
  for (int i = 0; i < 12; ++i)
    if (i < 11 || w < 2) *(LAS v4u*)(L + B_W + (i * 8 + w) * 1024 + lane * 16) = wt[i];
  DRAIN();
  BAR();
  if (HI && w == 7) signal(cnt_ptr(X.ws, CNT_A, layer, g), lane);
  STAMP(X, layer * 32 + 6);
}

template <int HI>
__device__ __forceinline__ void stage_b(const Ctx& X, int layer, int run, v4u (&a)[ring<HI>()], const v4u (&raw)[8]) {
  LAS unsigned char* L = X.L;
  const rsrc_t rs = X.rs;
  const int g = X.g, j = X.j, w = X.w, lane = X.lane;
  constexpr int kh = HI, NK = HI ? 2 : 3;
  const int rb = w & 3, c = lane & 15, gq = lane >> 4;
  const bool poller = HI && w == 7;
  const int xb = (layer == 0 ? OFF_X0 : OFF_X2) + (4 * g + rb) * KS * 1024;
  int xo[NK];
  v2u res[NK];
#pragma unroll
  for (int i = 0; i < NK; ++i) {
    const int t = kh * 3 + i, d0 = 72 * j + 16 * t + 4 * gq;
    xo[i] = (d0 >> 5) * 1024 + (((d0 >> 3) & 3) * 16 + c) * 16 + ((d0 >> 2) & 1) * 8;
    res[i] = v2u{0, 0};
    if (t < 4 || gq < 2) res[i] = ld64<SC1>(rs, xo[i], xb);
  }
  SB();
  if (layer == 0) rt_store(X, raw);
  v4u wf[6][5];
#pragma unroll
  for (int u = 0; u < 6; ++u)
#pragma unroll
    for (int t = 0; t < 5; ++t) wf[u][t] = *(LAS v4u*)(L + B_W + ((kh * 9 + u) * 5 + t) * 1024 + lane * 16);
  SB();
  if (poller) poll_ge(cnt_ptr(X.ws, CNT_A, layer, g), 8);
  STAMP(X, layer * 32 + 7);
  BAR();
  STAMP(X, layer * 32 + 8);
  v4u of[9];
  const int ob = OFF_O + layer * XBUF + (4 * g + rb) * KS * 1024;
#pragma unroll
  for (int u = 0; u < (kh ? 7 : 9); ++u) of[u] = ld128<SC1>(rs, lane * 16 + (u & 3) * 1024, ob + kh * 9 * 1024 + (u >> 2) * 4096);
  if (kh) {
#pragma unroll
    for (int u = 7; u < 9; ++u) {
      const v2u p = ld64<SC1>(rs, gq * 256 + c * 8 + (u - 7) * 1024, ob + 16 * 1024);
      const v2u q = ld64<SC1>(rs, gq * 256 + c * 8 + (u - 7) * 1024 + 128, ob + 16 * 1024);
      of[u] = v4u{p.x, p.y, q.x, q.y};
    }
  }
  SB();
  f32x4 acc[5];
#pragma unroll
  for (int t = 0; t < 5; ++t) acc[t] = f32x4{0, 0, 0, 0};
#pragma unroll
  for (int u = 0; u < 9; ++u) {
#pragma unroll
    for (int t = 0; t < 5; ++t) {
      acc[t] = mfma16(wf[u % 6][t], of[u], acc[t]);
      if (u < 3) wf[u][t] = *(LAS v4u*)(L + B_W + ((kh * 9 + u + 6) * 5 + t) * 1024 + lane * 16);
    }
    SB();
  }
#pragma unroll
  for (int t = (kh ? 0 : 3); t < (kh ? 3 : 5); ++t) *(LAS f32x4*)(L + B_RED + (rb * 5 + t) * 1024 + lane * 16) = acc[t];
  STAMP(X, layer * 32 + 9);
  BAR();
  const int x1b = OFF_X1 + layer * XBUF + (4 * g + rb) * KS * 1024;
  float rsum = 0.f, rsq2 = 0.f;
#pragma unroll
  for (int i = 0; i < NK; ++i) {
    const int t = kh * 3 + i;
    const f32x4 o = *(LAS f32x4*)(L + B_RED + (rb * 5 + t) * 1024 + lane * 16);
    f32x4 v = o + acc[t];
    v.x += bflo(res[i].x); v.y += bfhi(res[i].x); v.z += bflo(res[i].y); v.w += bfhi(res[i].y);
    const v2u y = pk4(v);
    if (t < 4 || gq < 2) st64<SC1>(y, rs, xo[i], x1b);
    rsum = dot2(y.x, ONES, dot2(y.y, ONES, rsum));
    rsq2 = dot2(y.x, y.x, dot2(y.y, y.y, rsq2));
  }
  sum1632x2(rsum, rsq2);
  if (lane < 16) st64<SC1>(v2u{asu(rsum), asu(rsq2)}, rs, lane * 8, OFF_RS + ((layer * 128 + 4 * g + rb) * 16 + 2 * j + kh) * 128);
  STAMP(X, layer * 32 + 10);
  DRAIN();
  BAR();
  STAMP(X, layer * 32 + 11);
  if (!HI) ring_zero<0, CPF>(a);
  if (poller) {
    gu32* p = cnt_ptr(X.ws, CNT_B, layer, g);
    signal(p, lane);
    if (layer == 0) signal(cnt_ptr(X.ws, CNT_W, 0, j), lane);
    poll_ge(p, 8);
    if (layer == 0) poll_ge(cnt_ptr(X.ws, CNT_W, 0, j), 32);
  } else if (!HI && layer) {
    ring_fill<0, CPF>(X, run, a);
  }
  BAR();
  STAMP(X, layer * 32 + 12);
}

template <int NT, int BASE, int PRE, int R, typename HOOK>
__device__ __forceinline__ void c_gemm(LAS unsigned char* Xl, rsrc_t rs, int run, v4u (&a)[R], f32x4 (&acc)[NT][4], int lane, HOOK hook) {
  constexpr int TOTAL = 2 * KS * NT;
  v4u xf[2][4];
  LAS unsigned char* xp = Xl + lane * 16;
  LAS unsigned char* xq = xp + 2 * KS * 1024;
#define XF(r, u) (*(LAS v4u*)(((r) < 2 ? xp : xq) + (((r) & 1) * KS + (u)) * 1024))
#pragma unroll
  for (int r = 0; r < 4; ++r) xf[0][r] = XF(r, 0);
  SB();
#pragma unroll
  for (int u = 0; u < KS; ++u) {
    hook(u);
#pragma unroll
    for (int t = 0; t < NT; ++t) {
      const int idx = BASE + u * NT + t;
#pragma unroll
      for (int r = 3; r >= 0; --r) {
        if (u == 0) m16z(acc[t][r], a[idx % R], xf[u & 1][r]);
        else m16(acc[t][r], a[idx % R], xf[u & 1][r]);
        if (r >= 2 && t < 2 && u + 1 < KS) { xf[(u + 1) & 1][2 * t + 3 - r] = XF(2 * t + 3 - r, u + 1); SB(); }
      }
      const int l0 = cmin(TOTAL, cmin(idx + R, PRE + 2 * idx)), l1 = cmin(TOTAL, cmin(idx + 1 + R, PRE + 2 * (idx + 1)));
#pragma unroll
      for (int k = 0; k < 2; ++k)
        if (l0 + k < l1) a[(l0 + k) % R] = ld128<SC1>(rs, lane * 16 + ((l0 + k) & 3) * 1024, run + ((l0 + k) >> 2) * 4096);
      SB();
    }
  }
#undef XF
  MFMA_DONE();
}

#define LDS_ARRIVE(p) do { SB(); asm volatile("s_waitcnt lgkmcnt(0)" ::: "memory"); if (lane == 0) __hip_atomic_fetch_add((p), 1u, RLX_WG); SB(); } while (0)
template <int HI>
__device__ __forceinline__ void stage_c(const Ctx& X, int layer, int run, v4u (&a)[ring<HI>()]) {
  constexpr int NT = 5 - HI, R = ring<HI>(), PRE = HI ? HPRE : CPRE;
  LAS unsigned char* L = X.L;
  const rsrc_t rs = X.rs;
  const int g = X.g, j = X.j, w = X.w, lane = X.lane;
  const int c = lane & 15, gq = lane >> 4, rb = w & 3;
  const int T0 = HI ? 20 + 4 * (w - 4) : 5 * w;
  LAS unsigned* c_lo = (LAS unsigned*)(L + L_CNT + 20);
  LAS unsigned* c_hi = (LAS unsigned*)(L + L_CNT + 24);
  LAS unsigned* c_st = (LAS unsigned*)(L + L_CNT + 28);
  LAS unsigned* sig = (LAS unsigned*)(L + L_CNT + 32 + rb * 4);
  const unsigned four = 4 * (layer + 1);
  {
    constexpr int U0 = HI ? 6 : 0, NU = HI ? 12 : 6;
    if (HI) __builtin_amdgcn_s_setprio(2);
    const int xb = OFF_X1 + layer * XBUF + ((4 * g + rb) * KS + U0) * 1024;
    v4u v[NU];
#pragma unroll
    for (int u = 0; u < NU; ++u) v[u] = ld128<SC1>(rs, lane * 16 + (u & 3) * 1024, xb + (u >> 2) * 4096);
    v2u ps[4];
    if (HI) {
#pragma unroll
      for (int k = 0; k < 4; ++k) ps[k] = ld64<SC1>(rs, gq * 512 + k * 128 + c * 8, OFF_RS + (layer * 128 + 4 * g + rb) * 2048);
    }
    SB();
    if (!HI) {
      if (layer == 0) ring_fill<0, CPF>(X, run, a);
      ring_fill<CPF, CPRE>(X, run, a);
    }
#pragma unroll
    for (int u = 0; u < NU; ++u) *(LAS v4u*)(L + C_X + (rb * KS + U0 + u) * 1024 + lane * 16) = v[u];
    LDS_ARRIVE(HI ? c_hi : c_lo);
    STAMP(X, layer * 32 + 16);
    if (HI) {
      if (layer == 0) {
#pragma unroll
        for (int k = 0; k < 3; ++k) {
          const int n = k * 256 + rb * 64 + lane;
          if (k < 2 || rb == 0) {
            float s = 0.f;
#pragma unroll
            for (int q = 0; q < 9; ++q) s += asf(__builtin_amdgcn_raw_buffer_load_b32(rs, n * 4, OFF_CS + (q * F + j * 576) * 4, SC1));
            *(LAS f32x2*)(L + L_CB + n * 8) = f32x2{s, bflo(X.b1[j * 576 + n])};
          }
        }
      }
      float s = (asf(ps[0].x) + asf(ps[1].x)) + (asf(ps[2].x) + asf(ps[3].x)), q = (asf(ps[0].y) + asf(ps[1].y)) + (asf(ps[2].y) + asf(ps[3].y));
      sum1632x2(s, q);
      const float mean = s * (1.f / D);
      const float var = fmx(q * (1.f / D) - mean * mean, 0.f);
      const float rstd = rsq(var + 1e-5f);
      if (lane < 16) *(LAS f32x2*)(L + C_ST + (rb * 16 + c) * 8) = f32x2{rstd, -mean * rstd};
      LDS_ARRIVE(c_st);
      ring_fill<0, PRE>(X, run, a);
      __builtin_amdgcn_s_setprio(0);
      lds_wait(c_lo, four);
      lds_wait(c_hi, four);
      if (SIGK >= 0) lds_wait(sig, layer + 1);
    } else {
      lds_wait(c_lo, four);
    }
    STAMP(X, layer * 32 + 17);
  }
  f32x4 acc[NT][4];
  c_gemm<NT, 0, PRE>(L + C_X, rs, run, a, acc, lane, [&](int u) {
    if (!HI && u == 5) lds_wait(c_hi, four);
    if (!HI && u == SIGK) { __hip_atomic_store(sig, (unsigned)(layer + 1), RLX_WG); SB(); }
  });
  lds_wait(c_st, four);
  STAMP(X, layer * 32 + 19);
  {
    float rstd[4], nm[4];
#pragma unroll
    for (int r = 0; r < 4; ++r) {
      const f32x2 st = *(LAS f32x2*)(L + C_ST + (r * 16 + c) * 8);
      rstd[r] = st.x;
      nm[r] = st.y;
    }
#pragma unroll
    for (int t = 0; t < NT; ++t) {
      const int T = T0 + t;
      const f32x4 cb0 = *(LAS f32x4*)(L + L_CB + (T * 16 + 4 * gq) * 8), cb1 = *(LAS f32x4*)(L + L_CB + (T * 16 + 4 * gq) * 8 + 16);
      const float cs[4] = {cb0.x, cb0.z, cb1.x, cb1.z}, bb[4] = {cb0.y, cb0.w, cb1.y, cb1.w};
#pragma unroll
      for (int r = 0; r < 4; ++r) {
        float o[4];
#pragma unroll
        for (int e = 0; e < 4; ++e) {
          const float v = __builtin_fmaf(acc[t][r][e], rstd[r], __builtin_fmaf(cs[e], nm[r], bb[e]));
          const float p = __builtin_fmaf(v * v, -0.10294324f, -2.3022082f);
          const float ex = __builtin_amdgcn_exp2f(v * p);
          o[e] = v * __builtin_amdgcn_rcpf(1.0f + ex);
        }
        *(LAS v2u*)(L + C_H + (r * KS + (T >> 1)) * 1024 + ((2 * (T & 1) + (gq >> 1)) * 16 + c) * 16 + (gq & 1) * 8) = v2u{pk2(o[0], o[1]), pk2(o[2], o[3])};
      }
    }
  }
  STAMP(X, layer * 32 + 20);
  if (HI) {
    BAR();
  } else {
    LDS_ARRIVE((LAS unsigned*)(L + L_CNT + 8));
    lds_wait((LAS unsigned*)(L + L_CNT + 8), four);
  }
  STAMP(X, layer * 32 + 21);
  c_gemm<NT, KS * NT, PRE>(L + C_H, rs, run, a, acc, lane, [&](int u) {
    if (!HI && u == 9) BAR();
  });
  STAMP(X, layer * 32 + 22);
  const int pb = OFF_P + layer * PBUF + g * 64 * 9216 + j * 9216 + T0 * 256;
  const int pv = (c >> 3) * 8 * 9216 + (c & 7) * 32 + gq * 8;
#pragma unroll
  for (int t = 0; t < NT; ++t)
#pragma unroll
    for (int r = 0; r < 4; ++r) st64<SC1>(pk4(acc[t][r]), rs, pv + t * 256, pb + 2 * r * 8 * 9216);
  STAMP(X, layer * 32 + 23);
  DRAIN();
  if (lane == 0) {
    const unsigned old = __hip_atomic_fetch_add((LAS unsigned*)(L + L_CNT + HI * 4), 1u, RLX_WG);
    if ((old & 3u) == 3u) __hip_atomic_fetch_add(cnt_ptr(X.ws, HI ? CNT_C2 : CNT_C1, layer, g), 1u, RLX_AGENT);
  }
  STAMP(X, layer * 32 + 24);
}

template <int HI, int LAYER, typename PRE>
__device__ __forceinline__ void stage_d(const Ctx& X, PRE pre) {
  constexpr int layer = LAYER;
  LAS unsigned char* L = X.L;
  const rsrc_t rs = X.rs;
  const int g = X.g, j = X.j, w = X.w, lane = X.lane;
  const int pb = OFF_P + layer * PBUF + (g * 8 + j) * 8 * 9216;
  const int RB = 4 * g + (j >> 1);
  const int tok = (lane >> 1) & 7, hf = lane & 1;
  constexpr int I0 = HI ? 2 : 0;
  int fo[3], pp[3];
  v4u xr[3], bv[3], o[3];
  pp[0] = w * 64 + lane;
  pp[1] = 256 + w * 16 + (lane & 15);
  pp[2] = 320 + w * 32 + (lane & 31);
#pragma unroll
  for (int i = I0; i < 3; ++i) {
    const int T = pp[i] >> 4;
    fo[i] = (T >> 1) * 1024 + ((2 * (T & 1) + hf) * 16 + (j & 1) * 8 + tok) * 16;
    xr[i] = ld128<SC1>(rs, fo[i], OFF_X1 + layer * XBUF + RB * KS * 1024);
    bv[i] = ld128<0>(X.rb2, (T * 16 + hf * 8) * 2, 0);
  }
  SB();
  float s = 0.f, q = 0.f;
  auto finish = [&](int i, const float (&l2)[4], const float (&h2)[4], int nl) {
#pragma unroll
    for (int e = 0; e < 4; ++e) o[i][e] = pk2(bflo(bv[i][e]) + bflo(xr[i][e]) + l2[e], bfhi(bv[i][e]) + bfhi(xr[i][e]) + h2[e]);
    if (lane < nl) {
      if (layer == 0) st128<SC1>(o[i], rs, fo[i], OFF_X2 + RB * KS * 1024);
      else st128<0>(o[i], X.rout, ((g * 64 + j * 8 + tok) * D + (pp[i] >> 4) * 16 + hf * 8) * 2, 0);
      if (layer == 0) {
#pragma unroll
        for (int e = 0; e < 4; ++e) {
          const float lo = bflo(o[i][e]), hi = bfhi(o[i][e]);
          s += lo + hi;
          q = __builtin_fmaf(lo, lo, __builtin_fmaf(hi, hi, q));
        }
      }
    }
  };
  if (!HI) {
    poll_ge(cnt_ptr(X.ws, CNT_C1, layer, g), 8);
    STAMP(X, layer * 32 + 25);
    v4u pv[8], pz[2];
#pragma unroll
    for (int k = 0; k < 8; ++k) pv[k] = ld128<SC1>(rs, pp[0] * 16, pb + k * 9216);
#pragma unroll
    for (int k = 0; k < 2; ++k) pz[k] = ld128<SC1>(rs, pp[1] * 16 + (lane >> 4) * 2 * 9216, pb + k * 9216);
    SB();
    float l2[4], h2[4];
#pragma unroll
    for (int e = 0; e < 4; ++e) {
      l2[e] = 0.f; h2[e] = 0.f;
#pragma unroll
      for (int k = 0; k < 8; ++k) { l2[e] += bflo(pv[k][e]); h2[e] += bfhi(pv[k][e]); }
    }
    finish(0, l2, h2, 64);
#pragma unroll
    for (int e = 0; e < 4; ++e) {
      l2[e] = sum1632(bflo(pz[0][e]) + bflo(pz[1][e]));
      h2[e] = sum1632(bfhi(pz[0][e]) + bfhi(pz[1][e]));
    }
    finish(1, l2, h2, 16);
    SB();
  }
  STAMP(X, layer * 32 + 26);
  if (HI && w == 7) poll_ge(cnt_ptr(X.ws, CNT_C2, layer, g), 8);
  else pre();
  BAR();
  STAMP(X, layer * 32 + 27);
  {
    v4u pv[4];
#pragma unroll
    for (int k = 0; k < 4; ++k) pv[k] = ld128<SC1>(rs, pp[2] * 16 + (lane >> 5) * 4 * 9216, pb + k * 9216);
    SB();
    float l2[4], h2[4];
#pragma unroll
    for (int e = 0; e < 4; ++e) {
      l2[e] = 0.f; h2[e] = 0.f;
#pragma unroll
      for (int k = 0; k < 4; ++k) { l2[e] += bflo(pv[k][e]); h2[e] += bfhi(pv[k][e]); }
      l2[e] = sum32(l2[e]);
      h2[e] = sum32(h2[e]);
    }
    finish(2, l2, h2, 32);
  }
  STAMP(X, layer * 32 + 28);
  if (layer == 0) {
    s += asf(__builtin_amdgcn_mov_dpp(asu(s), 0xB1, 0xF, 0xF, true));
    q += asf(__builtin_amdgcn_mov_dpp(asu(q), 0xB1, 0xF, 0xF, true));
    s = sum1632(s);
    q = sum1632(q);
    if (lane < 16 && hf == 0) *(LAS f32x2*)(L + D_ST + (tok * 8 + w) * 8) = f32x2{s, q};
    BAR();
    float ts = 0.f, tq = 0.f;
#pragma unroll
    for (int k = 0; k < 4; ++k) {
      const f32x4 v = *(LAS f32x4*)(L + D_ST + tok * 64 + k * 16);
      ts += v.x + v.z;
      tq += v.y + v.w;
    }
    const float mean = ts * (1.f / D);
    const float var = fmx(tq * (1.f / D) - mean * mean, 0.f);
    const float rstd = rsq(var + 1e-5f), nm = -mean * rstd;
#pragma unroll
    for (int i = I0; i < 3; ++i)
      if (lane < (i == 0 ? 64 : i == 1 ? 16 : 32)) {
        v4u y;
#pragma unroll
        for (int e = 0; e < 4; ++e) y[e] = pk2(__builtin_fmaf(bflo(o[i][e]), rstd, nm), __builtin_fmaf(bfhi(o[i][e]), rstd, nm));
        st128<SC1>(y, rs, fo[i], OFF_XN + XBUF + RB * KS * 1024);
      }
    SB();
  }
}

template <int HI>
__device__ __forceinline__ void body(const Ctx& X) {
  const int run0 = OFF_WC + (X.j * 1296 + (HI ? 720 + (X.w - 4) * 144 : X.w * 180)) * 1024;
  v4u wa[rq<HI>()], wb[rq<HI>()];
  STAMP(X, 0);
  asm volatile("; role %0" ::"i"(HI) : "memory");
#pragma unroll
  for (int u = QPRE; u < rq<HI>(); ++u) { wa[u] = v4u{0, 0, 0, 0}; wb[u] = v4u{0, 0, 0, 0}; }
  wq_prefetch<HI, 0, QPRE>(fresh(X), wa, wb);
#pragma unroll 1
  for (int layer = 0;; ++layer) {
    v4u xt[12], raw[8];
    xn_load<0, 5>(fresh(X), layer, xt);
    BAR();
    xn_load<5, 12>(fresh(X), layer, xt);
    stage_a<HI>(fresh(X), layer, xt, wa, wb, raw);
    v4u a[ring<HI>()];
    int run = run0;
    asm volatile("" : "+s"(run));
    stage_b<HI>(fresh(X), layer, run, a, raw);
    asm volatile("" : "+s"(run));
    stage_c<HI>(fresh(X), layer, run, a);
    if (layer == 1) break;
    wq_prefetch<HI, 0, HI ? QPRE : rq<HI>()>(fresh(X), wa, wb);
    stage_d<HI, 0>(fresh(X), [] {});
    DRAIN();
    STAMP(X, 29);
    BAR();
    if (HI && X.w == 7) {
      signal(cnt_ptr(X.ws, CNT_D, 0, X.g), X.lane);
      poll_ge(cnt_ptr(X.ws, CNT_D, 0, X.g), 8);
      if (X.g > 0) poll_ge(cnt_ptr(X.ws, CNT_D, 0, X.g - 1), 8);
    }
    BAR();
    STAMP(X, 32);
  }
  stage_d<HI, 1>(fresh(X), [] {});
  STAMP(X, 32 + 29);
}

__global__ void __launch_bounds__(NTHR, 2) k_main(unsigned char* ws, const u16* w1, const u16* w2, const u16* b1, const u16* b2, u16* out) {
  extern __shared__ __attribute__((aligned(16))) unsigned char smem[];
  Ctx X;
  X.ws = ws;
  X.w1 = w1;
  X.w2 = w2;
  X.b1 = b1;
  X.L = (LAS unsigned char*)smem;
  X.rs = __builtin_amdgcn_make_buffer_rsrc((void*)ws, 0, WS_BYTES, 0x00020000);
  X.rb2 = __builtin_amdgcn_make_buffer_rsrc((void*)b2, 0, D * 2, 0x00020000);
  X.rout = __builtin_amdgcn_make_buffer_rsrc((void*)out, 0, S * D * 2, 0x00020000);
  X.lane = TIDX & 63;
  X.w = __builtin_amdgcn_readfirstlane(TIDX >> 6);
  const int x = BIDX & 7, i = BIDX >> 3;
  X.g = (x % RA) * (32 / RA) + i / RA;
  X.j = (x / RA) * RA + i % RA;
  if (TIDX < 16) *(LAS unsigned*)(X.L + L_CNT + TIDX * 4) = 0u;
  if (X.w < 4) body<0>(X);
  else body<1>(X);
}
}
#if defined(__HIP_DEVICE_COMPILE__)
#pragma clang attribute pop
#endif

extern "C" void kernel_launch(void* const* d_in, const int* in_sizes, int n_in, void* d_out, int out_size, void* d_ws, size_t ws_size,
                              hipStream_t stream) {
  const u16* x = (const u16*)d_in[0];
  const u16* wqkv = (const u16*)d_in[1];
  const u16* wo = (const u16*)d_in[2];
  const u16* w1 = (const u16*)d_in[4];
  const u16* b1 = (const u16*)d_in[5];
  const u16* w2 = (const u16*)d_in[6];
  const u16* b2 = (const u16*)d_in[7];
  unsigned char* ws = (unsigned char*)d_ws;
  (void)hipFuncSetAttribute((const void*)k_main, hipFuncAttributeMaxDynamicSharedMemorySize, LDS_BYTES);
  k_prep<<<K0_BLOCKS, 256, 0, stream>>>(x, wqkv, wo, ws);
  k_main<<<256, NTHR, LDS_BYTES, stream>>>(ws, w1, w2, b1, b2, (u16*)d_out);
}
```

```cpp
#include <hip/hip_runtime.h>
#include <hip/hip_bf16.h>

#if defined(__HIP_DEVICE_COMPILE__)
#pragma clang attribute push(__attribute__((target("no-packed-fp32-ops"))), apply_to = function)
#endif
namespace {
typedef unsigned short u16;
typedef unsigned v4u __attribute__((ext_vector_type(4)));
typedef unsigned v2u __attribute__((ext_vector_type(2)));
typedef float f32x4 __attribute__((ext_vector_type(4)));
typedef float f32x2 __attribute__((ext_vector_type(2)));
typedef __bf16 bf16x2_t __attribute__((ext_vector_type(2)));
typedef __bf16 bf16x8_t __attribute__((ext_vector_type(8)));
#define GAS __attribute__((address_space(1)))
#define LAS __attribute__((address_space(3)))
typedef __amdgpu_buffer_rsrc_t rsrc_t;

constexpr int S = 2048, D = 576, F = 4608;
constexpr int KS = 18;
constexpr int NTHR = 512;
constexpr int XBUF = 128 * KS * 1024;
constexpr int OFF_CTRL = 0;
constexpr int OFF_CS = 4 * 65536;
constexpr int OFF_RS = OFF_CS + 3 * 65536;
constexpr int OFF_WQ = OFF_RS + 128 * 2048;
constexpr int OFF_WO = OFF_WQ + 8 * 14 * KS * 1024;
constexpr int OFF_WC = OFF_WO + 8 * 90 * 1024;
constexpr int OFF_XN = OFF_WC + 8 * 1296 * 1024;
constexpr int OFF_O = OFF_XN + 2 * XBUF;
constexpr int OFF_X1 = OFF_O + XBUF;
constexpr int OFF_X2 = OFF_X1 + XBUF;
constexpr int OFF_P = OFF_X2 + XBUF;
constexpr int WS_BYTES = OFF_P + 8 * XBUF;

__device__ __forceinline__ f32x4 mfma16(v4u a, v4u b, f32x4 c) {
  return __builtin_amdgcn_mfma_f32_16x16x32_bf16(__builtin_bit_cast(bf16x8_t, a), __builtin_bit_cast(bf16x8_t, b), c, 0, 0, 0);
}
__device__ __forceinline__ void m16(f32x4& c, v4u a, v4u b) { asm volatile("v_mfma_f32_16x16x32_bf16 %0, %1, %2, %0" : "+v"(c) : "v"(a), "v"(b)); }
__device__ __forceinline__ void m16z(f32x4& c, v4u a, v4u b) { asm volatile("v_mfma_f32_16x16x32_bf16 %0, %1, %2, 0" : "=&v"(c) : "v"(a), "v"(b)); }
template <bool Z> __device__ __forceinline__ void m16t(f32x4& c, v4u a, v4u b) { if (Z) m16z(c, a, b); else m16(c, a, b); }
__device__ __forceinline__ unsigned pk2(float lo, float hi) {
  f32x2 v = {lo, hi};
  return __builtin_bit_cast(unsigned, __builtin_convertvector(v, bf16x2_t));
}
__device__ __forceinline__ float asf(unsigned u) { return __builtin_bit_cast(float, u); }
__device__ __forceinline__ unsigned asu(float f) { return __builtin_bit_cast(unsigned, f); }
__device__ __forceinline__ float bflo(unsigned u) { return asf(u << 16); }
__device__ __forceinline__ float bfhi(unsigned u) { return asf(u & 0xffff0000u); }
__device__ __forceinline__ v2u pk4(f32x4 v) { return v2u{pk2(v.x, v.y), pk2(v.z, v.w)}; }
__device__ __forceinline__ float dot2(unsigned a, unsigned b, float c) {
  return __builtin_amdgcn_fdot2_f32_bf16(__builtin_bit_cast(bf16x2_t, a), __builtin_bit_cast(bf16x2_t, b), c, false);
}
constexpr unsigned ONES = 0x3f803f80u;

template <int AUX> __device__ __forceinline__ v4u ld128(rsrc_t rs, int voff, int soff) { return __builtin_amdgcn_raw_buffer_load_b128(rs, voff, soff, AUX); }
template <int AUX> __device__ __forceinline__ v2u ld64(rsrc_t rs, int voff, int soff) { return __builtin_amdgcn_raw_buffer_load_b64(rs, voff, soff, AUX); }
template <int AUX> __device__ __forceinline__ void st128(v4u v, rsrc_t rs, int voff, int soff) { __builtin_amdgcn_raw_buffer_store_b128(v, rs, voff + soff, 0, AUX); }
template <int AUX> __device__ __forceinline__ void st64(v2u v, rsrc_t rs, int voff, int soff) { __builtin_amdgcn_raw_buffer_store_b64(v, rs, voff, soff, AUX); }
template <int AUX> __device__ __forceinline__ void st32(unsigned v, rsrc_t rs, int voff, int soff) { __builtin_amdgcn_raw_buffer_store_b32(v, rs, voff, soff, AUX); }
#define SB() __builtin_amdgcn_sched_barrier(0)
#define MFMA_DONE() do { SB(); asm volatile("s_nop 7\n\ts_nop 1" ::: "memory"); SB(); } while (0)
#define TIDX ((int)__builtin_amdgcn_workitem_id_x())
#define BIDX ((int)__builtin_amdgcn_workgroup_id_x())
#define SYNC() do { __builtin_amdgcn_fence(__ATOMIC_RELEASE, "workgroup"); __builtin_amdgcn_s_barrier(); __builtin_amdgcn_fence(__ATOMIC_ACQUIRE, "workgroup"); } while (0)
__device__ __forceinline__ float fmx(float a, float b) { return __builtin_fmaxf(a, b); }
__device__ __forceinline__ float rsq(float a) { return __builtin_amdgcn_rsqf(a); }

__device__ __forceinline__ float sum32(float v) {
  const auto b = __builtin_amdgcn_permlane32_swap(asu(v), asu(v), false, false);
  const unsigned b0 = b[0], b1 = b[1];
  return asf(b0) + asf(b1);
}
__device__ __forceinline__ float sum1632(float v) {
  const auto a = __builtin_amdgcn_permlane16_swap(asu(v), asu(v), false, false);
  const unsigned a0 = a[0], a1 = a[1];
  return sum32(asf(a0) + asf(a1));
}
__device__ __forceinline__ float max1632(float v) {
  const auto a = __builtin_amdgcn_permlane16_swap(asu(v), asu(v), false, false);
  const unsigned a0 = a[0], a1 = a[1];
  v = fmx(asf(a0), asf(a1));
  const auto b = __builtin_amdgcn_permlane32_swap(asu(v), asu(v), false, false);
  const unsigned b0 = b[0], b1 = b[1];
  return fmx(asf(b0), asf(b1));
}

template <typename OP> __device__ __forceinline__ void red1632x2(float& a, float& b, OP op) {
  const auto s = __builtin_amdgcn_permlane16_swap(asu(a), asu(b), false, false);
  const unsigned s0 = s[0], s1 = s[1];
  const float t = op(asf(s0), asf(s1));
  const auto u = __builtin_amdgcn_permlane32_swap(asu(t), asu(t), false, false);
  const unsigned u0 = u[0], u1 = u[1];
  const float v = op(asf(u0), asf(u1));
  const auto x = __builtin_amdgcn_permlane16_swap(asu(v), asu(v), false, false);
  const unsigned x0 = x[0], x1 = x[1];
  a = asf(x0);
  b = asf(x1);
}
__device__ __forceinline__ void max1632x2(float& a, float& b) { red1632x2(a, b, [](float x, float y) { return fmx(x, y); }); }
__device__ __forceinline__ void sum1632x2(float& a, float& b) { red1632x2(a, b, [](float x, float y) { return x + y; }); }

__device__ __forceinline__ void tr8x8(const v4u (&r)[8], v4u (&o)[8]) {
#pragma unroll
  for (int c = 0; c < 4; ++c) {
#pragma unroll
    for (int p = 0; p < 4; ++p) {
      o[2 * c][p] = __builtin_amdgcn_perm(r[2 * p + 1][c], r[2 * p][c], 0x05040100u);
      o[2 * c + 1][p] = __builtin_amdgcn_perm(r[2 * p + 1][c], r[2 * p][c], 0x07060302u);
    }
  }
}
#define RA 2
#define RING_LO 24
#define RING_HI 28
#define QRING_LO 14
#define QRING 8
#define HPRE 12
#define PSLEEP 1
#define BPF 6
#define RAWAUX 2
#define CODE_KB 60
#define RTU0 3
#define RTS 2
#define SIGK 12
template <int HI> constexpr int ring() { return HI ? RING_HI : RING_LO; }
template <int HI> constexpr int rq() { return HI ? QRING : QRING_LO; }
constexpr int QPRE = 3;
constexpr int CPF = 4;
constexpr int CPRE = 12;
constexpr int cmin(int a, int b) { return a < b ? a : b; }
constexpr int SC1 = 16;
constexpr int A_XT = 0;
constexpr int A_Q = 5 * KS * 1024;
constexpr int A_K = A_Q + 16 * 512;
constexpr int A_V = A_K + 20 * 512;
constexpr int A_EQK = A_V + 20 * 512;
constexpr int A_EV = A_EQK + 5 * 512;
constexpr int B_W = 0, B_RED = 124 * 1024;
constexpr int C_X = 0, C_H = 4 * KS * 1024, C_ST = 8 * KS * 1024;
constexpr int L_CB = C_ST + 1024;
constexpr int D_ST = L_CB + 576 * 8;
constexpr int L_CNT = D_ST + 512;
constexpr int L_STAMP = L_CNT + 64;
#define STAMPCLK() __builtin_amdgcn_s_memrealtime()
constexpr int LDS_BYTES = L_STAMP;
#define STAMP(X_, slot) do {} while (0)
enum { F_A = 0, F_B = 1, F_C1 = 2, F_C2 = 3, F_D = 4, F_Q = 5, F_W = 6 };
typedef unsigned long long u64;
typedef GAS u64 gu64;
__device__ __forceinline__ int flag_off(int kind, int layer, int g) { return 4096 + ((kind * 2 + layer) * 32 + g) * 256; }
#define RLX_AGENT __ATOMIC_RELAXED, __HIP_MEMORY_SCOPE_AGENT
#define RLX_WG __ATOMIC_RELAXED, __HIP_MEMORY_SCOPE_WORKGROUP
#define BAR() do { SB(); asm volatile("s_waitcnt lgkmcnt(0)\n\ts_barrier" ::: "memory"); SB(); } while (0)
#define DRAIN() do { SB(); asm volatile("s_waitcnt vmcnt(0)" ::: "memory"); SB(); } while (0)
__device__ __forceinline__ void poll(unsigned char* ws, int off, u64 tag) {
  gu64* p = (gu64*)(ws + off);
#pragma unroll 1
  for (unsigned spins = 0; spins < (1u << 22); ++spins) {
    const u64 v = __hip_atomic_load(p, RLX_AGENT);
    if (__builtin_amdgcn_ballot_w64(v != tag) == 0) break;
    __builtin_amdgcn_s_sleep(1);
  }
  __builtin_amdgcn_fence(__ATOMIC_ACQUIRE, "wavefront");
  asm volatile("" ::: "memory");
}
__device__ __forceinline__ void signal(unsigned char* ws, int off, u64 tag) { __hip_atomic_store((gu64*)(ws + off), tag, RLX_AGENT); }
__device__ __forceinline__ void lds_wait(LAS unsigned* p, unsigned want) {
#pragma unroll 1
  for (unsigned spins = 0; spins < (1u << 24); ++spins) {
    const unsigned v = __builtin_amdgcn_readfirstlane(__hip_atomic_load(p, RLX_WG));
    if (v >= want) break;
    __builtin_amdgcn_s_sleep(1);
  }
  asm volatile("" ::: "memory");
  SB();
}

#define LDS_ARRIVE(p) do { SB(); asm volatile("s_waitcnt lgkmcnt(0)" ::: "memory"); if (lane == 0) __hip_atomic_fetch_add((p), 1u, RLX_WG); SB(); } while (0)

struct Ctx {
  unsigned char* ws;
  const u16* x;
  const u16* wqkv;
  const u16* wo;
  const u16* w1;
  const u16* w2;
  const u16* b1;
  LAS unsigned char* L;
  rsrc_t rs, rb2, rout;
  u64 tag;
  int g, j, w, lane;
};
__device__ __forceinline__ Ctx fresh(const Ctx& X) {
  Ctx Y = X;
  asm volatile("" : "+v"(Y.lane));
  asm volatile("" : "+s"(Y.g), "+s"(Y.j), "+s"(Y.w));
  return Y;
}

__device__ __forceinline__ int rt_task(int g, int q) { return q < 5 ? g * 5 + q : (g < 2 ? 160 + g : -1); }
__device__ __forceinline__ void rt_load(const Ctx& X, int task, v4u (&r)[8]) {
  const bool is1 = task < 81;
  const int tt = is1 ? task : task - 81, chunk = tt / 9, strip = tt - 9 * chunk;
  const u16* src = is1 ? X.w1 + (size_t)chunk * 64 * F + X.j * 576 + strip * 64 : X.w2 + (size_t)(X.j * 576 + chunk * 64) * D + strip * 64;
  const int ld = is1 ? F : D;
  const rsrc_t rw = __builtin_amdgcn_make_buffer_rsrc((void*)src, 0, (63 * ld + 64) * 2, 0x00020000);
  const int vo = ((X.lane >> 3) * 8 * ld + (X.lane & 7) * 8) * 2;
#pragma unroll
  for (int jj = 0; jj < 8; ++jj) r[jj] = ld128<RAWAUX>(rw, vo, jj * ld * 2);
  SB();
}
__device__ __forceinline__ void rt_turn(const Ctx& X, int task, int q, const v4u (&r)[8]) {
  const int lane = X.lane, kg = lane >> 3, nb = lane & 7;
  LAS unsigned char* sm = X.L + A_Q + q * 8192;
  v4u o[8];
  tr8x8(r, o);
  const int base = (((kg >> 2) * 4 + (nb >> 1)) * 64 + (kg & 3) * 16 + (nb & 1) * 8) * 16;
#pragma unroll
  for (int c = 0; c < 8; ++c) *(LAS v4u*)(sm + base + ((c ^ nb) * 16)) = o[c];
  if (task < 81) {
    const int chunk = task / 9, strip = task - 9 * chunk;
    float sa[8];
#pragma unroll
    for (int c = 0; c < 8; ++c) {
      float t = 0.f;
#pragma unroll
      for (int p = 0; p < 4; ++p) t = dot2(o[c][p], ONES, t);
      sa[c] = t + asf(__builtin_amdgcn_mov_dpp(asu(t), 0x128, 0xF, 0xF, true));
    }
#pragma unroll
    for (int c = 0; c < 8; c += 2) sum1632x2(sa[c], sa[c + 1]);
    float v = sa[0];
#pragma unroll
    for (int c = 1; c < 8; ++c) v = kg == c ? sa[c] : v;
    st32<SC1>(asu(v), X.rs, (nb * 8 + kg) * 4, OFF_CS + (chunk * F + X.j * 576 + strip * 64) * 4);
  }
  SB();
}
__device__ __forceinline__ v4u rt_get(const Ctx& X, int q) {
  const int lane = X.lane;
  return *(LAS v4u*)(X.L + A_Q + q * 8192 + (X.w * 64 + (lane & ~7) + ((lane & 7) ^ ((X.w & 3) * 2 + ((lane >> 3) & 1)))) * 16);
}
__device__ __forceinline__ void rt_put(const Ctx& X, int task, v4u v) {
  const bool is1 = task < 81;
  const int tt = is1 ? task : task - 81, chunk = (tt * 57) >> 9, strip = tt - 9 * chunk;
  const int T = strip * 4 + (X.w & 3), lo = strip < 5;
  const int fr = X.j * 1296 + (lo ? ((T * 13) >> 6) * 175 + T : strip * 144 + (X.w & 3)) + ((is1 ? 0 : KS) + chunk * 2 + (X.w >> 2)) * (lo ? 5 : 4);
  st128<SC1>(v, X.rs, X.lane * 16, OFF_WC + fr * 1024);
}

__device__ __forceinline__ void qo_tile(const Ctx& X) {
  constexpr int NQ = 2;
  const rsrc_t rq_ = __builtin_amdgcn_make_buffer_rsrc((void*)X.wqkv, 0, D * 3 * D * 2, 0x00020000);
  const int j = X.j;
  unsigned e[NQ][8];
  int dst[NQ];
  bool zero[NQ];
#pragma unroll
  for (int i = 0; i < NQ; ++i) {
    const int q = i * 256 + (X.w - 4) * 64 + X.lane, P = X.g * 504 + (q < 504 ? q : 503);
    const int fr = P >> 6, blk = (fr * 3641) >> 16, u = fr - blk * KS, gq = (P >> 4) & 3, m = P & 15;
    const int ty = blk < 12 ? blk >> 2 : blk == 12 ? (m >> 1) & 1 : 2;
    const int hd = blk < 12 ? blk & 3 : blk == 12 ? m >> 2 : (m >> 1) & 3;
    const int col = ty * D + (4 * j + hd) * 18 + (blk < 12 ? m : 16 + (m & 1));
    zero[i] = blk == 13 && m >= 8;
    dst[i] = OFF_WQ + (j * 14 * KS * 64 + P) * 16;
    const int vo = ((u * 32 + gq * 8) * 3 * D + col) * 2;
#pragma unroll
    for (int jj = 0; jj < 8; ++jj) e[i][jj] = __builtin_amdgcn_raw_buffer_load_b16(rq_, vo, jj * 3 * D * 2, 0);
  }
  const rsrc_t ro_ = __builtin_amdgcn_make_buffer_rsrc((void*)X.wo, 0, D * D * 2, 0x00020000);
  const int t = (X.w - 4) * 64 + X.lane;
  const int P = X.g * 180 + (t < 180 ? t : 179);
  const int fr = P >> 6, u = (fr * 205) >> 10, tl = fr - u * 5, gq = (P >> 4) & 3, m = P & 15;
  const bool zo = tl * 16 + m >= 72;
  const int col = j * 72 + (zo ? 0 : tl * 16 + m);
  const int r0 = u < 16 ? 36 * u + 4 * gq : 72 * (4 * (u - 16) + gq) + 16;
  unsigned eo[8];
#pragma unroll
  for (int jj = 0; jj < 8; ++jj) {
    const int row = r0 + (u < 16 ? (jj >> 2) * 18 + (jj & 3) : (jj >> 1) * 18 + (jj & 1));
    eo[jj] = __builtin_amdgcn_raw_buffer_load_b16(ro_, (row * D + col) * 2, 0, 0);
  }
  SB();
#pragma unroll
  for (int i = 0; i < NQ; ++i) {
    v4u o;
#pragma unroll
    for (int p = 0; p < 4; ++p) o[p] = zero[i] ? 0u : (e[i][2 * p] | (e[i][2 * p + 1] << 16));
    st128<SC1>(o, X.rs, dst[i], 0);
  }
  v4u o;
#pragma unroll
  for (int p = 0; p < 4; ++p) o[p] = zo ? 0u : (eo[2 * p] | (eo[2 * p + 1] << 16));
  if (t < 180) st128<SC1>(o, X.rs, OFF_WO + (j * 90 * 64 + P) * 16, 0);
  SB();
}

__device__ __forceinline__ void x0_rows(const Ctx& X) {
  const rsrc_t rx = __builtin_amdgcn_make_buffer_rsrc((void*)X.x, 0, S * D * 2, 0x00020000);
  const int tok = X.lane & 7, q8 = X.lane >> 3;
  v4u v[9];
#pragma unroll
  for (int i = 0; i < 9; ++i) v[i] = ld128<0>(rx, (tok * D + q8 * 8 + i * 64) * 2, (X.g * 64 + X.j * 8) * D * 2);
  SB();
  float s = 0.f, q = 0.f;
#pragma unroll
  for (int i = 0; i < 9; ++i)
#pragma unroll
    for (int p = 0; p < 4; ++p) { s = dot2(v[i][p], ONES, s); q = dot2(v[i][p], v[i][p], q); }
  s += asf(__builtin_amdgcn_mov_dpp(asu(s), 0x128, 0xF, 0xF, true));
  q += asf(__builtin_amdgcn_mov_dpp(asu(q), 0x128, 0xF, 0xF, true));
  sum1632x2(s, q);
  const float mean = s * (1.f / D);
  const float var = fmx(q * (1.f / D) - mean * mean, 0.f);
  const float rstd = rsq(var + 1e-5f), nm = -mean * rstd;
#pragma unroll
  for (int i = 0; i < 9; ++i) {
    v4u o;
#pragma unroll
    for (int p = 0; p < 4; ++p) o[p] = pk2(__builtin_fmaf(bflo(v[i][p]), rstd, nm), __builtin_fmaf(bfhi(v[i][p]), rstd, nm));
    st128<SC1>(o, X.rs, (q8 * 16 + tok) * 16 + i * 2048, OFF_XN + (4 * X.g + (X.j >> 1)) * KS * 1024 + (X.j & 1) * 128);
  }
  SB();
}

template <int HI> __device__ __forceinline__ int wq_a(const Ctx& X) { return OFF_WQ + (X.j * 14 + (HI ? X.w + 4 : X.w)) * KS * 1024; }
template <int HI> __device__ __forceinline__ int wq_b(const Ctx& X) { return OFF_WQ + (X.j * 14 + (HI ? 12 + ((X.w - 4) >> 1) : X.w + 4)) * KS * 1024; }
template <int HI, int U0, int U1>
__device__ __forceinline__ void wq_prefetch(const Ctx& X, v4u (&wa)[rq<HI>()], v4u (&wb)[rq<HI>()]) {
  const int sa = wq_a<HI>(X), sb = wq_b<HI>(X);
#pragma unroll
  for (int u = U0; u < U1; ++u) {
    wa[u] = ld128<SC1>(X.rs, X.lane * 16 + (u & 3) * 1024, sa + (u >> 2) * 4096);
    wb[u] = ld128<SC1>(X.rs, X.lane * 16 + (u & 3) * 1024, sb + (u >> 2) * 4096);
  }
  SB();
}

template <int I0, int I1>
__device__ __forceinline__ void xn_load(const Ctx& X, int layer, v4u (&t)[12]) {
  const int base = OFF_XN + layer * XBUF + (4 * X.g - 1) * KS * 1024;
#pragma unroll
  for (int i = I0; i < I1; ++i) {
    const int f = i < 11 ? 8 * i + X.w : (X.w < 2 ? 88 + X.w : 89), u = f / 5, rb5 = f - 5 * u;
    t[i] = ld128<SC1>(X.rs, X.lane * 16, base + ((X.g == 0 && rb5 == 0 ? 1 : rb5) * KS + u) * 1024);
  }
  SB();
}

template <int LO, int HIX, int R>
__device__ __forceinline__ void ring_fill(const Ctx& X, int run, v4u (&a)[R]) {
#pragma unroll
  for (int i = LO; i < HIX; ++i) a[i] = ld128<SC1>(X.rs, X.lane * 16 + (i & 3) * 1024, run + (i >> 2) * 4096);
  SB();
}
template <int LO, int HIX, int R>
__device__ __forceinline__ void ring_zero(v4u (&a)[R]) {
#pragma unroll
  for (int i = LO; i < HIX; ++i) a[i] = v4u{0, 0, 0, 0};
}

template <int HI>
__device__ __forceinline__ void stage_a(const Ctx& X, int layer, const v4u (&t)[12], v4u (&wa)[rq<HI>()], v4u (&wb)[rq<HI>()]) {
  constexpr int RQ = rq<HI>();
  LAS unsigned char* L = X.L;
  const rsrc_t rs = X.rs;
  const int g = X.g, j = X.j, w = X.w, lane = X.lane;
  v4u wt[12];
  {
    constexpr int A0 = HI ? 0 : 1, B1 = HI ? 3 : 5;
    const int h = w & 3;
    const int sa = wq_a<HI>(X), sb = wq_b<HI>(X);
    f32x4 ac[5], bc[5];
    v4u xf[2][5];
    const int rot = HI ? (w & 1) * 2 : 0;
    LAS unsigned char* xa = L + A_XT + lane * 16 + rot * 1024;
    LAS unsigned char* xb = L + A_XT + lane * 16 + (rot ? 0 : 3) * 1024;
    LAS unsigned char* xa2 = xa + 12 * 5120;
    LAS unsigned char* xb2 = xb + 12 * 5120;
#define XF(r, u) (*(LAS v4u*)(((u) < 12 ? ((r) < 3 ? xa : xb) : ((r) < 3 ? xa2 : xb2)) + ((u) < 12 ? (u) : (u) - 12) * 5120 + ((r) < 3 ? (r) : (r) - 3) * 1024))
#pragma unroll
    for (int r = 0; r < 5; ++r) xf[0][r] = XF(r, 0);
#pragma unroll
    for (int r = 0; r < 5; ++r) xf[1][r] = XF(r, 1);
    SB();
#pragma unroll
    for (int u = 0; u < KS; ++u) {
      if (u == 1) {
#pragma unroll
        for (int i = 5; i < 12; ++i)
          if (i < 11 || w < 2) *(LAS v4u*)(L + A_XT + (8 * i + w) * 1024 + lane * 16) = t[i];
        SB();
      }
      if (u == 2) { SB(); asm volatile("s_waitcnt lgkmcnt(0)" ::: "memory"); if (lane == 0) __hip_atomic_fetch_add((LAS unsigned*)(L + L_CNT + 16), 1u, RLX_WG); SB(); }
      if (u == 6) lds_wait((LAS unsigned*)(L + L_CNT + 16), 8 * (layer + 1));
#pragma unroll
      for (int r = 0; r < 5; ++r) {
        if (r >= A0) {
          if (u == 0) { if (HI) m16z(ac[r], xf[u & 1][r], wa[u % RQ]); else m16z(ac[r], wa[u % RQ], xf[u & 1][r]); }
          else { if (HI) m16(ac[r], xf[u & 1][r], wa[u % RQ]); else m16(ac[r], wa[u % RQ], xf[u & 1][r]); }
        }
        if (r < B1) { if (u == 0) m16z(bc[r], wb[u % RQ], xf[u & 1][r]); else m16(bc[r], wb[u % RQ], xf[u & 1][r]); }
        if (u + 2 < KS) xf[u & 1][r] = XF(r, u + 2);
        SB();
      }
      {
        const int l0 = u == 0 ? QPRE : cmin(KS, cmin(u + RQ, QPRE + 2 * u)), l1 = cmin(KS, cmin(u + 1 + RQ, QPRE + 2 * (u + 1)));
#pragma unroll
        for (int l = QPRE; l < KS; ++l)
          if (l >= l0 && l < l1 && (HI || l >= RQ || layer == 0)) {
            wa[l % RQ] = ld128<SC1>(rs, lane * 16 + (l & 3) * 1024, sa + (l >> 2) * 4096);
            wb[l % RQ] = ld128<SC1>(rs, lane * 16 + (l & 3) * 1024, sb + (l >> 2) * 4096);
          }
        if (u >= RTU0 && u < RTU0 + 6 * RTS && (u - RTU0) % RTS == 0 && layer == 0) {
          const int q = (u - RTU0) / RTS, task = rt_task(g, q);
          if (task >= 0) rt_put(X, task, rt_get(X, q));
          if (q == 5) LDS_ARRIVE((LAS unsigned*)(L + L_CNT + 52));
        }
        if (u >= 10 && u < 16) {
#pragma unroll
          for (int i = 2 * (u - 10); i < 2 * (u - 10) + 2; ++i) wt[i] = ld128<SC1>(rs, lane * 16, OFF_WO + (j * 90 + i * 8 + w) * 1024);
        }
        SB();
      }
    }
#undef XF
    MFMA_DONE();
    lds_wait((LAS unsigned*)(L + L_CNT + 52), 8);
    if (!HI) {
#pragma unroll
      for (int r = 1; r < 5; ++r) *(LAS v2u*)(L + A_Q + (h * 4 + r - 1) * 512 + lane * 8) = pk4(ac[r]);
#pragma unroll
      for (int r = 0; r < 5; ++r) *(LAS v2u*)(L + A_K + (h * 5 + r) * 512 + lane * 8) = pk4(bc[r]);
    } else {
#pragma unroll
      for (int r = 0; r < 5; ++r) *(LAS v2u*)(L + A_V + (h * 5 + (r < 3 ? rot + r : (rot ? 0 : 3) + r - 3)) * 512 + lane * 8) = pk4(ac[r]);
      if (w < 6) {
#pragma unroll
        for (int r = 0; r < 3; ++r) *(LAS v2u*)(L + A_EQK + (rot + r) * 512 + lane * 8) = pk4(bc[r]);
      } else {
        const int c = lane & 15, gq = lane >> 4;
#pragma unroll
        for (int r = 0; r < 3; ++r) {
          const v2u v = pk4(bc[r]);
          LAS u16* p = (LAS u16*)(L + A_EV + (rot + r) * 512 + ((c >> 2) * 16 + 4 * gq) * 8 + (c & 3) * 2);
          p[0] = (u16)v.x;
          p[4] = (u16)(v.x >> 16);
          p[8] = (u16)v.y;
          p[12] = (u16)(v.y >> 16);
        }
      }
    }
  }
  STAMP(X, layer * 32 + 3);
  BAR();
  STAMP(X, layer * 32 + 4);
  {
    const int c = lane & 15, gq = lane >> 4, rb = w & 3;
    typedef short s16x4 __attribute__((ext_vector_type(4)));
    auto mk = [](v2u a, v2u b, f32x4 cc) {
      return __builtin_amdgcn_mfma_f32_16x16x16bf16_1k(__builtin_bit_cast(s16x4, a), __builtin_bit_cast(s16x4, b), cc, 0, 0, 0);
    };
    LAS unsigned char* lp = L + rb * 512 + lane * 8;
    const v2u e0 = *(LAS v2u*)(lp + A_EQK), e1 = *(LAS v2u*)(lp + A_EQK + 512);
    v2u qm[2], k0[2], k1[2], v0[2], v1[2];
#pragma unroll
    for (int hh = 0; hh < 2; ++hh) {
      const int h = 2 * HI + hh;
      qm[hh] = *(LAS v2u*)(lp + A_Q + h * 4 * 512);
      k0[hh] = *(LAS v2u*)(lp + A_K + h * 5 * 512);
      k1[hh] = *(LAS v2u*)(lp + A_K + (h * 5 + 1) * 512);
    }
#pragma unroll
    for (int hh = 0; hh < 2; ++hh) {
      const int h = 2 * HI + hh;
      v0[hh] = *(LAS v2u*)(lp + A_V + h * 5 * 512);
      v1[hh] = *(LAS v2u*)(lp + A_V + (h * 5 + 1) * 512);
    }
    const v2u x0 = *(LAS v2u*)(lp + A_EV), x1 = *(LAS v2u*)(lp + A_EV + 512);
    SB();
    const bool first = (g == 0 && rb == 0);
    const bool par = c & 1;
    bool own[2], ok[2];
#pragma unroll
    for (int e = 0; e < 2; ++e) {
      own[e] = 4 * gq + 2 * e <= (c & 14);
      ok[e] = own[e] || !first;
    }
    const f32x4 z = {0, 0, 0, 0};
    f32x4 s0[2], s1[2];
#pragma unroll
    for (int hh = 0; hh < 2; ++hh) {
      s0[hh] = mk(k0[hh], qm[hh], z);
      s1[hh] = mk(k1[hh], qm[hh], z);
    }
#pragma unroll
    for (int hh = 0; hh < 2; ++hh) {
      const v2u qe = {0u, gq == 2 * HI + hh ? e1.x : 0u};
      s0[hh] = mk(e0, qe, s0[hh]);
      s1[hh] = mk(e1, qe, s1[hh]);
    }
    float sv[2][2], m[2], p[2][2], inv[2];
#pragma unroll
    for (int hh = 0; hh < 2; ++hh) {
      const float a0 = s0[hh].x, a1 = s0[hh].y, a2 = s0[hh].z, a3 = s0[hh].w, b0 = s1[hh].x, b1 = s1[hh].y, b2 = s1[hh].z, b3 = s1[hh].w;
      const float l0 = own[0] ? b0 : a0, h0 = own[0] ? b1 : a1, l1 = own[1] ? b2 : a2, h1 = own[1] ? b3 : a3;
      sv[hh][0] = ok[0] ? (par ? h0 : l0) : -3.0e38f;
      sv[hh][1] = ok[1] ? (par ? h1 : l1) : -3.0e38f;
      m[hh] = fmx(sv[hh][0], sv[hh][1]);
    }
    max1632x2(m[0], m[1]);
    constexpr float SC = 0.23570226039551584f * 1.4426950408889634f;
#pragma unroll
    for (int hh = 0; hh < 2; ++hh) {
      p[hh][0] = __builtin_amdgcn_exp2f((sv[hh][0] - m[hh]) * SC);
      p[hh][1] = __builtin_amdgcn_exp2f((sv[hh][1] - m[hh]) * SC);
      inv[hh] = p[hh][0] + p[hh][1];
    }
    sum1632x2(inv[0], inv[1]);
    f32x4 om[2], ox[2];
#pragma unroll
    for (int hh = 0; hh < 2; ++hh) {
      const float r = __builtin_amdgcn_rcpf(inv[hh]);
      const float q0 = p[hh][0] * r, q1 = p[hh][1] * r;
      const unsigned d0 = pk2(par ? 0.f : q0, par ? q0 : 0.f), d1 = pk2(par ? 0.f : q1, par ? q1 : 0.f);
      const v2u pp = {own[0] ? 0u : d0, own[1] ? 0u : d1}, po = {own[0] ? d0 : 0u, own[1] ? d1 : 0u};
      om[hh] = mk(v1[hh], po, mk(v0[hh], pp, z));
      ox[hh] = mk(x1, po, mk(x0, pp, z));
    }
    const int ob = OFF_O + (4 * g + rb) * KS * 1024;
    const v2u a = pk4(om[0]), b = pk4(om[1]);
    st128<SC1>(v4u{a.x, a.y, b.x, b.y}, rs, lane * 16, ob + (2 * j + HI) * 1024);
    if (gq == HI) st64<SC1>(v2u{pk2(ox[0].x, ox[0].y), pk2(ox[1].z, ox[1].w)}, rs, c * 8, ob + 16 * 1024 + (2 * j + HI) * 128);
  }
  STAMP(X, layer * 32 + 5);
#pragma unroll
  for (int i = 0; i < 12; ++i)
    if (i < 11 || w < 2) *(LAS v4u*)(L + B_W + (i * 8 + w) * 1024 + lane * 16) = wt[i];
  DRAIN();
  BAR();
  if (HI && w == 7 && lane < 2 - layer) signal(X.ws, lane ? flag_off(F_W, 0, j) + g * 8 : flag_off(F_A, layer, g) + j * 8, X.tag);
  STAMP(X, layer * 32 + 6);
}

template <int HI>
__device__ __forceinline__ void stage_b(const Ctx& X, int layer) {
  LAS unsigned char* L = X.L;
  const rsrc_t rs = X.rs;
  const int g = X.g, j = X.j, w = X.w, lane = X.lane;
  constexpr int kh = HI, NK = HI ? 2 : 3;
  const int rb = w & 3, c = lane & 15, gq = lane >> 4;
  const bool poller = HI && w == 7;
  const int xb = OFF_X2 + (4 * g + rb) * KS * 1024;
  const rsrc_t rx = __builtin_amdgcn_make_buffer_rsrc((void*)X.x, 0, S * D * 2, 0x00020000);
  int xo[NK];
  v2u res[NK];
#pragma unroll
  for (int i = 0; i < NK; ++i) {
    const int t = kh * 3 + i, d0 = 72 * j + 16 * t + 4 * gq;
    xo[i] = (d0 >> 5) * 1024 + (((d0 >> 3) & 3) * 16 + c) * 16 + ((d0 >> 2) & 1) * 8;
    res[i] = v2u{0, 0};
    if (t < 4 || gq < 2) {
      if (layer) res[i] = ld64<SC1>(rs, xo[i], xb);
      else res[i] = ld64<0>(rx, (c * D + d0) * 2, (4 * g + rb) * 16 * D * 2);
    }
  }
  SB();
  v4u wf[6][5];
#pragma unroll
  for (int u = 0; u < 6; ++u)
#pragma unroll
    for (int t = 0; t < 5; ++t) wf[u][t] = *(LAS v4u*)(L + B_W + ((kh * 9 + u) * 5 + t) * 1024 + lane * 16);
  SB();
  if (poller) {
    poll(X.ws, flag_off(F_A, layer, g) + (lane & 7) * 8, X.tag);
  }
  STAMP(X, layer * 32 + 7);
  BAR();
  STAMP(X, layer * 32 + 8);
  v4u of[9];
  const int ob = OFF_O + (4 * g + rb) * KS * 1024;
#pragma unroll
  for (int u = 0; u < (kh ? 7 : 9); ++u) of[u] = ld128<SC1>(rs, lane * 16 + (u & 3) * 1024, ob + kh * 9 * 1024 + (u >> 2) * 4096);
  if (kh) {
#pragma unroll
    for (int u = 7; u < 9; ++u) {
      const v2u p = ld64<SC1>(rs, gq * 256 + c * 8 + (u - 7) * 1024, ob + 16 * 1024);
      const v2u q = ld64<SC1>(rs, gq * 256 + c * 8 + (u - 7) * 1024 + 128, ob + 16 * 1024);
      of[u] = v4u{p.x, p.y, q.x, q.y};
    }
  }
  SB();
  f32x4 acc[5];
#pragma unroll
  for (int t = 0; t < 5; ++t) acc[t] = f32x4{0, 0, 0, 0};
#pragma unroll
  for (int u = 0; u < 9; ++u) {
#pragma unroll
    for (int t = 0; t < 5; ++t) {
      acc[t] = mfma16(wf[u % 6][t], of[u], acc[t]);
      if (u < 3) wf[u][t] = *(LAS v4u*)(L + B_W + ((kh * 9 + u + 6) * 5 + t) * 1024 + lane * 16);
    }
    SB();
  }
#pragma unroll
  for (int t = (kh ? 0 : 3); t < (kh ? 3 : 5); ++t) *(LAS f32x4*)(L + B_RED + (rb * 5 + t) * 1024 + lane * 16) = acc[t];
  STAMP(X, layer * 32 + 9);
  BAR();
  const int x1b = OFF_X1 + (4 * g + rb) * KS * 1024;
  float rsum = 0.f, rsq2 = 0.f;
#pragma unroll
  for (int i = 0; i < NK; ++i) {
    const int t = kh * 3 + i;
    const f32x4 o = *(LAS f32x4*)(L + B_RED + (rb * 5 + t) * 1024 + lane * 16);
    f32x4 v = o + acc[t];
    v.x += bflo(res[i].x); v.y += bfhi(res[i].x); v.z += bflo(res[i].y); v.w += bfhi(res[i].y);
    const v2u y = pk4(v);
    if (t < 4 || gq < 2) st64<SC1>(y, rs, xo[i], x1b);
    rsum = dot2(y.x, ONES, dot2(y.y, ONES, rsum));
    rsq2 = dot2(y.x, y.x, dot2(y.y, y.y, rsq2));
  }
  sum1632x2(rsum, rsq2);
  if (lane < 16) st64<SC1>(v2u{asu(rsum), asu(rsq2)}, rs, lane * 8, OFF_RS + ((4 * g + rb) * 16 + 2 * j + kh) * 128);
  STAMP(X, layer * 32 + 10);
  DRAIN();
  BAR();
  STAMP(X, layer * 32 + 11);
  if (poller) {
    if (lane == 0) signal(X.ws, flag_off(F_B, layer, g) + j * 8, X.tag);
    poll(X.ws, (lane >= 32 && layer == 0) ? flag_off(F_W, 0, j) + (lane & 31) * 8 : flag_off(F_B, layer, g) + (lane & 7) * 8, X.tag);
  }
  BAR();
  STAMP(X, layer * 32 + 12);
}

template <int NT, int BASE, int PRE, int R, typename HOOK>
__device__ __forceinline__ void c_gemm(LAS unsigned char* Xl, rsrc_t rs, int run, v4u (&a)[R], f32x4 (&acc)[NT][4], int lane, HOOK hook) {
  constexpr int TOTAL = 2 * KS * NT;
  v4u xf[2][4];
  LAS unsigned char* xp = Xl + lane * 16;
  LAS unsigned char* xq = xp + 2 * KS * 1024;
#define XF(r, u) (*(LAS v4u*)(((r) < 2 ? xp : xq) + (((r) & 1) * KS + (u)) * 1024))
#pragma unroll
  for (int r = 0; r < 4; ++r) xf[0][r] = XF(r, 0);
  SB();
#pragma unroll
  for (int u = 0; u < KS; ++u) {
    hook(u);
#pragma unroll
    for (int t = 0; t < NT; ++t) {
      const int idx = BASE + u * NT + t;
#pragma unroll
      for (int r = 3; r >= 0; --r) {
        if (u == 0) m16z(acc[t][r], a[idx % R], xf[u & 1][r]);
        else m16(acc[t][r], a[idx % R], xf[u & 1][r]);
        if (r >= 2 && t < 2 && u + 1 < KS) { xf[(u + 1) & 1][2 * t + 3 - r] = XF(2 * t + 3 - r, u + 1); SB(); }
      }
      const int l0 = cmin(TOTAL, cmin(idx + R, PRE + 2 * idx)), l1 = cmin(TOTAL, cmin(idx + 1 + R, PRE + 2 * (idx + 1)));
#pragma unroll
      for (int k = 0; k < 2; ++k)
        if (l0 + k < l1) a[(l0 + k) % R] = ld128<SC1>(rs, lane * 16 + ((l0 + k) & 3) * 1024, run + ((l0 + k) >> 2) * 4096);
      SB();
    }
  }
#undef XF
  MFMA_DONE();
}

template <int HI>
__device__ __forceinline__ void stage_c(const Ctx& X, int layer, int run) {
  v4u a[ring<HI>()];
  constexpr int NT = 5 - HI, R = ring<HI>(), PRE = CPRE;
  LAS unsigned char* L = X.L;
  const rsrc_t rs = X.rs;
  const int g = X.g, j = X.j, w = X.w, lane = X.lane;
  const int c = lane & 15, gq = lane >> 4, rb = w & 3;
  const int T0 = HI ? 20 + 4 * (w - 4) : 5 * w;
  LAS unsigned* c_lo = (LAS unsigned*)(L + L_CNT + 20);
  LAS unsigned* c_hi = (LAS unsigned*)(L + L_CNT + 24);
  LAS unsigned* c_st = (LAS unsigned*)(L + L_CNT + 28);
  LAS unsigned* sig = (LAS unsigned*)(L + L_CNT + 32 + rb * 4);
  const unsigned four = 4 * (layer + 1);
  {
    constexpr int U0 = HI ? 6 : 0, NU = HI ? 12 : 6;
    __builtin_amdgcn_s_setprio(HI ? 2 : 3);
    const int xb = OFF_X1 + ((4 * g + rb) * KS + U0) * 1024;
    v4u v[NU];
#pragma unroll
    for (int u = 0; u < NU; ++u) v[u] = ld128<SC1>(rs, lane * 16 + (u & 3) * 1024, xb + (u >> 2) * 4096);
    v2u ps[4];
    if (HI) {
#pragma unroll
      for (int k = 0; k < 4; ++k) ps[k] = ld64<SC1>(rs, gq * 512 + k * 128 + c * 8, OFF_RS + (4 * g + rb) * 2048);
    }
    SB();
    if (!HI) ring_fill<0, CPRE>(X, run, a);
#pragma unroll
    for (int u = 0; u < NU; ++u) *(LAS v4u*)(L + C_X + (rb * KS + U0 + u) * 1024 + lane * 16) = v[u];
    LDS_ARRIVE(HI ? c_hi : c_lo);
    STAMP(X, layer * 32 + 16);
    if (HI) {
      if (layer == 0) {
#pragma unroll
        for (int k = 0; k < 3; ++k) {
          const int n = k * 256 + rb * 64 + lane;
          if (k < 2 || rb == 0) {
            float s = 0.f;
#pragma unroll
            for (int q = 0; q < 9; ++q) s += asf(__builtin_amdgcn_raw_buffer_load_b32(rs, n * 4, OFF_CS + (q * F + j * 576) * 4, SC1));
            *(LAS f32x2*)(L + L_CB + n * 8) = f32x2{s, bflo(X.b1[j * 576 + n])};
          }
        }
      }
      float s = (asf(ps[0].x) + asf(ps[1].x)) + (asf(ps[2].x) + asf(ps[3].x)), q = (asf(ps[0].y) + asf(ps[1].y)) + (asf(ps[2].y) + asf(ps[3].y));
      sum1632x2(s, q);
      const float mean = s * (1.f / D);
      const float var = fmx(q * (1.f / D) - mean * mean, 0.f);
      const float rstd = rsq(var + 1e-5f);
      if (lane < 16) *(LAS f32x2*)(L + C_ST + (rb * 16 + c) * 8) = f32x2{rstd, -mean * rstd};
      LDS_ARRIVE(c_st);
      ring_fill<0, CPRE>(X, run, a);
      __builtin_amdgcn_s_setprio(0);
      lds_wait(c_lo, four);
      lds_wait(c_hi, four);
      if (SIGK >= 0) lds_wait(sig, layer + 1);
    } else {
      __builtin_amdgcn_s_setprio(0);
      lds_wait(c_lo, four);
    }
    STAMP(X, layer * 32 + 17);
  }
  f32x4 acc[NT][4];
  c_gemm<NT, 0, PRE>(L + C_X, rs, run, a, acc, lane, [&](int u) {
    if (!HI && u == 5) lds_wait(c_hi, four);
    if (!HI && u == SIGK) { __hip_atomic_store(sig, (unsigned)(layer + 1), RLX_WG); SB(); }
  });
  lds_wait(c_st, four);
  STAMP(X, layer * 32 + 19);
  {
    float rstd[4], nm[4];
#pragma unroll
    for (int r = 0; r < 4; ++r) {
      const f32x2 st = *(LAS f32x2*)(L + C_ST + (r * 16 + c) * 8);
      rstd[r] = st.x;
      nm[r] = st.y;
    }
#pragma unroll
    for (int t = 0; t < NT; ++t) {
      const int T = T0 + t;
      const f32x4 cb0 = *(LAS f32x4*)(L + L_CB + (T * 16 + 4 * gq) * 8), cb1 = *(LAS f32x4*)(L + L_CB + (T * 16 + 4 * gq) * 8 + 16);
      const float cs[4] = {cb0.x, cb0.z, cb1.x, cb1.z}, bb[4] = {cb0.y, cb0.w, cb1.y, cb1.w};
#pragma unroll
      for (int r = 0; r < 4; ++r) {
        float o[4];
#pragma unroll
        for (int e = 0; e < 4; ++e) {
          const float v = __builtin_fmaf(acc[t][r][e], rstd[r], __builtin_fmaf(cs[e], nm[r], bb[e]));
          const float p = __builtin_fmaf(v * v, -0.10294324f, -2.3022082f);
          const float ex = __builtin_amdgcn_exp2f(v * p);
          o[e] = v * __builtin_amdgcn_rcpf(1.0f + ex);
        }
        *(LAS v2u*)(L + C_H + (r * KS + (T >> 1)) * 1024 + ((2 * (T & 1) + (gq >> 1)) * 16 + c) * 16 + (gq & 1) * 8) = v2u{pk2(o[0], o[1]), pk2(o[2], o[3])};
      }
    }
  }
  STAMP(X, layer * 32 + 20);
  if (HI) {
    BAR();
  } else {
    LDS_ARRIVE((LAS unsigned*)(L + L_CNT + 8));
    lds_wait((LAS unsigned*)(L + L_CNT + 8), four);
  }
  STAMP(X, layer * 32 + 21);
  c_gemm<NT, KS * NT, PRE>(L + C_H, rs, run, a, acc, lane, [&](int u) {
    if (!HI && u == 9) BAR();
  });
  STAMP(X, layer * 32 + 22);
  const int pb = OFF_P + g * 64 * 9216 + j * 9216 + T0 * 256;
  const int pv = (c >> 3) * 8 * 9216 + (c & 7) * 32 + gq * 8;
#pragma unroll
  for (int t = 0; t < NT; ++t)
#pragma unroll
    for (int r = 0; r < 4; ++r) st64<SC1>(pk4(acc[t][r]), rs, pv + t * 256, pb + 2 * r * 8 * 9216);
  STAMP(X, layer * 32 + 23);
  DRAIN();
  if (lane == 0) {
    const unsigned old = __hip_atomic_fetch_add((LAS unsigned*)(L + L_CNT + HI * 4), 1u, RLX_WG);
    if ((old & 3u) == 3u) signal(X.ws, flag_off(HI ? F_C2 : F_C1, layer, g) + j * 8, X.tag);
  }
  STAMP(X, layer * 32 + 24);
}

template <int HI, int LAYER, typename PRE>
__device__ __forceinline__ void stage_d(const Ctx& X, PRE pre) {
  constexpr int layer = LAYER;
  LAS unsigned char* L = X.L;
  const rsrc_t rs = X.rs;
  const int g = X.g, j = X.j, w = X.w, lane = X.lane;
  const int pb = OFF_P + (g * 8 + j) * 8 * 9216;
  const int RB = 4 * g + (j >> 1);
  const int tok = (lane >> 1) & 7, hf = lane & 1;
  constexpr int I0 = HI ? 2 : 0;
  int fo[3], pp[3];
  v4u xr[3], bv[3], o[3];
  pp[0] = w * 64 + lane;
  pp[1] = 256 + w * 16 + (lane & 15);
  pp[2] = 320 + w * 32 + (lane & 31);
#pragma unroll
  for (int i = I0; i < 3; ++i) {
    const int T = pp[i] >> 4;
    fo[i] = (T >> 1) * 1024 + ((2 * (T & 1) + hf) * 16 + (j & 1) * 8 + tok) * 16;
    xr[i] = ld128<SC1>(rs, fo[i], OFF_X1 + RB * KS * 1024);
    bv[i] = ld128<0>(X.rb2, (T * 16 + hf * 8) * 2, 0);
  }
  SB();
  float s = 0.f, q = 0.f;
  auto finish = [&](int i, const float (&l2)[4], const float (&h2)[4], int nl) {
#pragma unroll
    for (int e = 0; e < 4; ++e) o[i][e] = pk2(bflo(bv[i][e]) + bflo(xr[i][e]) + l2[e], bfhi(bv[i][e]) + bfhi(xr[i][e]) + h2[e]);
    if (lane < nl) {
      if (layer == 0) st128<SC1>(o[i], rs, fo[i], OFF_X2 + RB * KS * 1024);
      else st128<0>(o[i], X.rout, ((g * 64 + j * 8 + tok) * D + (pp[i] >> 4) * 16 + hf * 8) * 2, 0);
      if (layer == 0) {
#pragma unroll
        for (int e = 0; e < 4; ++e) {
          const float lo = bflo(o[i][e]), hi = bfhi(o[i][e]);
          s += lo + hi;
          q = __builtin_fmaf(lo, lo, __builtin_fmaf(hi, hi, q));
        }
      }
    }
  };
  if (!HI) {
    poll(X.ws, flag_off(F_C1, layer, g) + (lane & 7) * 8, X.tag);
    STAMP(X, layer * 32 + 25);
    v4u pv[8], pz[2];
#pragma unroll
    for (int k = 0; k < 8; ++k) pv[k] = ld128<SC1>(rs, pp[0] * 16, pb + k * 9216);
#pragma unroll
    for (int k = 0; k < 2; ++k) pz[k] = ld128<SC1>(rs, pp[1] * 16 + (lane >> 4) * 2 * 9216, pb + k * 9216);
    SB();
    float l2[4], h2[4];
#pragma unroll
    for (int e = 0; e < 4; ++e) {
      l2[e] = 0.f; h2[e] = 0.f;
#pragma unroll
      for (int k = 0; k < 8; ++k) { l2[e] += bflo(pv[k][e]); h2[e] += bfhi(pv[k][e]); }
    }
    finish(0, l2, h2, 64);
#pragma unroll
    for (int e = 0; e < 4; ++e) {
      l2[e] = sum1632(bflo(pz[0][e]) + bflo(pz[1][e]));
      h2[e] = sum1632(bfhi(pz[0][e]) + bfhi(pz[1][e]));
    }
    finish(1, l2, h2, 16);
    SB();
  }
  STAMP(X, layer * 32 + 26);
  if (HI && w == 7) poll(X.ws, flag_off(F_C2, layer, g) + (lane & 7) * 8, X.tag);
  else pre();
  BAR();
  STAMP(X, layer * 32 + 27);
  {
    v4u pv[4];
#pragma unroll
    for (int k = 0; k < 4; ++k) pv[k] = ld128<SC1>(rs, pp[2] * 16 + (lane >> 5) * 4 * 9216, pb + k * 9216);
    SB();
    float l2[4], h2[4];
#pragma unroll
    for (int e = 0; e < 4; ++e) {
      l2[e] = 0.f; h2[e] = 0.f;
#pragma unroll
      for (int k = 0; k < 4; ++k) { l2[e] += bflo(pv[k][e]); h2[e] += bfhi(pv[k][e]); }
      l2[e] = sum32(l2[e]);
      h2[e] = sum32(h2[e]);
    }
    finish(2, l2, h2, 32);
  }
  STAMP(X, layer * 32 + 28);
  if (layer == 0) {
    s += asf(__builtin_amdgcn_mov_dpp(asu(s), 0xB1, 0xF, 0xF, true));
    q += asf(__builtin_amdgcn_mov_dpp(asu(q), 0xB1, 0xF, 0xF, true));
    s = sum1632(s);
    q = sum1632(q);
    if (lane < 16 && hf == 0) *(LAS f32x2*)(L + D_ST + (tok * 8 + w) * 8) = f32x2{s, q};
    BAR();
    float ts = 0.f, tq = 0.f;
#pragma unroll
    for (int k = 0; k < 4; ++k) {
      const f32x4 v = *(LAS f32x4*)(L + D_ST + tok * 64 + k * 16);
      ts += v.x + v.z;
      tq += v.y + v.w;
    }
    const float mean = ts * (1.f / D);
    const float var = fmx(tq * (1.f / D) - mean * mean, 0.f);
    const float rstd = rsq(var + 1e-5f), nm = -mean * rstd;
#pragma unroll
    for (int i = I0; i < 3; ++i)
      if (lane < (i == 0 ? 64 : i == 1 ? 16 : 32)) {
        v4u y;
#pragma unroll
        for (int e = 0; e < 4; ++e) y[e] = pk2(__builtin_fmaf(bflo(o[i][e]), rstd, nm), __builtin_fmaf(bfhi(o[i][e]), rstd, nm));
        st128<SC1>(y, rs, fo[i], OFF_XN + XBUF + RB * KS * 1024);
      }
    SB();
  }
}

template <int HI>
__device__ __forceinline__ void body(Ctx& X) {
  const int run0 = OFF_WC + (X.j * 1296 + (HI ? 720 + (X.w - 4) * 144 : X.w * 180)) * 1024;
  v4u wa[rq<HI>()], wb[rq<HI>()];
  asm volatile("; role %0" ::"i"(HI) : "memory");
#pragma unroll
  for (int u = 0; u < rq<HI>(); ++u) { wa[u] = v4u{0, 0, 0, 0}; wb[u] = v4u{0, 0, 0, 0}; }
#pragma unroll 1
  for (int layer = 0;; ++layer) {
    {
      v4u xt[12];
      xn_load<0, 5>(fresh(X), layer, xt);
      if (layer == 0) wq_prefetch<HI, 0, QPRE>(fresh(X), wa, wb);
      asm volatile("s_barrier" ::: "memory");
      xn_load<5, 12>(fresh(X), layer, xt);
#pragma unroll
      for (int i = 0; i < 5; ++i) *(LAS v4u*)(X.L + A_XT + (8 * i + X.w) * 1024 + X.lane * 16) = xt[i];
      STAMP(X, layer * 32 + 1);
      BAR();
      STAMP(X, layer * 32 + 2);
      stage_a<HI>(fresh(X), layer, xt, wa, wb);
    }
    stage_b<HI>(fresh(X), layer);
    int run = run0;
    asm volatile("" : "+s"(run));
    stage_c<HI>(fresh(X), layer, run);
    if (layer == 1) break;
    wq_prefetch<HI, 0, HI ? QPRE : rq<HI>()>(fresh(X), wa, wb);
    stage_d<HI, 0>(fresh(X), [] {});
    DRAIN();
    STAMP(X, 29);
    BAR();
    if (HI && X.w == 7) {
      if (X.lane == 0) signal(X.ws, flag_off(F_D, 1, X.g) + X.j * 8, X.tag);
      if (BIDX == 0 && X.lane == 1) signal(X.ws, 0, X.tag);
      poll(X.ws, (X.lane >= 32 && X.g > 0) ? flag_off(F_D, 1, X.g - 1) + (6 + (X.lane & 1)) * 8 : flag_off(F_D, 1, X.g) + (X.lane & 7) * 8, X.tag);
    }
    BAR();
    STAMP(X, 32);
  }
  stage_d<HI, 1>(fresh(X), [] {});
  STAMP(X, 32 + 29);
}

}

extern "C" __global__ void __launch_bounds__(NTHR, 2) ilk_main(unsigned char* ws, const u16* x, const u16* wqkv, const u16* wo, const u16* w1, const u16* w2,
                                                  const u16* b1, const u16* b2, u16* out) {
  extern __shared__ __attribute__((aligned(16))) unsigned char smem[];
  Ctx X;
  X.ws = ws;
  X.x = x;
  X.wqkv = wqkv;
  X.wo = wo;
  X.w1 = w1;
  X.w2 = w2;
  X.b1 = b1;
  X.L = (LAS unsigned char*)smem;
  X.rs = __builtin_amdgcn_make_buffer_rsrc((void*)ws, 0, WS_BYTES, 0x00020000);
  X.rb2 = __builtin_amdgcn_make_buffer_rsrc((void*)b2, 0, D * 2, 0x00020000);
  X.rout = __builtin_amdgcn_make_buffer_rsrc((void*)out, 0, S * D * 2, 0x00020000);
  X.tag = 0;
  X.lane = TIDX & 63;
  X.w = __builtin_amdgcn_readfirstlane(TIDX >> 6);
  const int xc = BIDX & 7, i = BIDX >> 3;
  X.g = (xc % RA) * (32 / RA) + i / RA;
  X.j = (xc / RA) * RA + i % RA;
  STAMP(X, 0);
  {
    const u64 e = __hip_atomic_load((gu64*)X.ws, RLX_AGENT);
    if (TIDX < 16) *(LAS unsigned*)(X.L + L_CNT + TIDX * 4) = 0u;
    BAR();
    if (X.w >= 3) {
      __builtin_amdgcn_s_setprio(3);
      if (X.w == 3) x0_rows(X);
      else qo_tile(X);
      DRAIN();
    } else {
      {
        u64 pc;
        asm volatile("s_getpc_b64 s[20:21]\n\ts_add_u32 s20, s20, ilk_main@rel32@lo+4\n\ts_addc_u32 s21, s21, ilk_main@rel32@hi+12\n\ts_mov_b64 %0, s[20:21]"
                     : "=s"(pc) : : "scc", "s20", "s21");
        const v4u t = *((const GAS v4u*)pc + (cmin(i * 3 + X.w, CODE_KB - 1) * 64 + X.lane));
        asm volatile("" ::"v"(t));
      }
      __builtin_amdgcn_s_sleep(PSLEEP);
      const int t0 = rt_task(X.g, X.w), t1 = rt_task(X.g, X.w + 3);
      v4u r0[8], r1[8];
      rt_load(X, t0, r0);
      rt_load(X, t1 < 0 ? t0 : t1, r1);
      rt_turn(X, t0, X.w, r0);
      if (t1 >= 0) rt_turn(X, t1, X.w + 3, r1);
    }
    const unsigned elo = __builtin_amdgcn_readfirstlane((unsigned)e), ehi = __builtin_amdgcn_readfirstlane((unsigned)(e >> 32));
    X.tag = (((u64)ehi << 32) | elo) + 0x9E3779B97F4A7C15ull;
    STAMP(X, 30);
    if (X.w >= 3) {
      unsigned old = 0;
      if (X.lane == 0) old = __hip_atomic_fetch_add((LAS unsigned*)(X.L + L_CNT + 12), 1u, RLX_WG);
      if (__builtin_amdgcn_readfirstlane(old) == 4u) {
        if (X.lane < 2) signal(X.ws, X.lane ? flag_off(F_Q, 0, X.j) + X.g * 8 : flag_off(F_D, 0, X.g) + X.j * 8, X.tag);
        poll(X.ws, X.lane >= 32 ? flag_off(F_Q, 0, X.j) + (X.lane & 31) * 8
                                : (X.lane >= 16 && X.g > 0) ? flag_off(F_D, 0, X.g - 1) + (6 + (X.lane & 1)) * 8 : flag_off(F_D, 0, X.g) + (X.lane & 7) * 8, X.tag);
      }
      __builtin_amdgcn_s_setprio(0);
    }
    BAR();
    STAMP(X, 31);
  }
  if (X.w < 4) body<0>(X);
  else body<1>(X);
}
#if defined(__HIP_DEVICE_COMPILE__)
#pragma clang attribute pop
#endif

extern "C" void kernel_launch(void* const* d_in, const int* in_sizes, int n_in, void* d_out, int out_size, void* d_ws, size_t ws_size,
                              hipStream_t stream) {
  (void)hipFuncSetAttribute((const void*)ilk_main, hipFuncAttributeMaxDynamicSharedMemorySize, LDS_BYTES);
  ilk_main<<<256, NTHR, LDS_BYTES, stream>>>((unsigned char*)d_ws, (const u16*)d_in[0], (const u16*)d_in[1], (const u16*)d_in[2], (const u16*)d_in[4],
                                           (const u16*)d_in[6], (const u16*)d_in[5], (const u16*)d_in[7], (u16*)d_out);
}
```

```cpp
#include <hip/hip_runtime.h>
#include <hip/hip_bf16.h>

#if defined(__HIP_DEVICE_COMPILE__)
#pragma clang attribute push(__attribute__((target("no-packed-fp32-ops"))), apply_to = function)
#endif
namespace {
typedef unsigned short u16;
typedef unsigned v4u __attribute__((ext_vector_type(4)));
typedef unsigned v2u __attribute__((ext_vector_type(2)));
typedef float f32x4 __attribute__((ext_vector_type(4)));
typedef float f32x2 __attribute__((ext_vector_type(2)));
typedef __bf16 bf16x2_t __attribute__((ext_vector_type(2)));
typedef __bf16 bf16x8_t __attribute__((ext_vector_type(8)));
#define GAS __attribute__((address_space(1)))
#define LAS __attribute__((address_space(3)))
typedef __amdgpu_buffer_rsrc_t rsrc_t;

constexpr int S = 2048, D = 576, F = 4608;
constexpr int KS = 18;
constexpr int NTHR = 512;
constexpr int XBUF = 128 * KS * 1024;
constexpr int OFF_CTRL = 0;
constexpr int OFF_CS = 4 * 65536;
constexpr int OFF_RS = OFF_CS + 3 * 65536;
constexpr int OFF_WQ = OFF_RS + 128 * 2048;
constexpr int OFF_WO = OFF_WQ + 8 * 14 * KS * 1024;
constexpr int OFF_WC = OFF_WO + 8 * 90 * 1024;
constexpr int OFF_XN = OFF_WC + 8 * 1296 * 1024;
constexpr int OFF_O = OFF_XN + 2 * XBUF;
constexpr int OFF_X1 = OFF_O + XBUF;
constexpr int OFF_X2 = OFF_X1 + XBUF;
constexpr int OFF_P = OFF_X2 + XBUF;
constexpr int WS_BYTES = OFF_P + 8 * XBUF;

__device__ __forceinline__ f32x4 mfma16(v4u a, v4u b, f32x4 c) {
  return __builtin_amdgcn_mfma_f32_16x16x32_bf16(__builtin_bit_cast(bf16x8_t, a), __builtin_bit_cast(bf16x8_t, b), c, 0, 0, 0);
}
__device__ __forceinline__ void m16(f32x4& c, v4u a, v4u b) { asm volatile("v_mfma_f32_16x16x32_bf16 %0, %1, %2, %0" : "+v"(c) : "v"(a), "v"(b)); }
__device__ __forceinline__ void m16z(f32x4& c, v4u a, v4u b) { asm volatile("v_mfma_f32_16x16x32_bf16 %0, %1, %2, 0" : "=&v"(c) : "v"(a), "v"(b)); }
__device__ __forceinline__ void m16c(f32x4& d, v4u a, v4u b, f32x4 c) { asm volatile("v_mfma_f32_16x16x32_bf16 %0, %1, %2, %3" : "=&v"(d) : "v"(a), "v"(b), "v"(c)); }
template <bool Z> __device__ __forceinline__ void m16t(f32x4& c, v4u a, v4u b) { if (Z) m16z(c, a, b); else m16(c, a, b); }
__device__ __forceinline__ unsigned pk2(float lo, float hi) {
  f32x2 v = {lo, hi};
  return __builtin_bit_cast(unsigned, __builtin_convertvector(v, bf16x2_t));
}
__device__ __forceinline__ float asf(unsigned u) { return __builtin_bit_cast(float, u); }
__device__ __forceinline__ unsigned asu(float f) { return __builtin_bit_cast(unsigned, f); }
__device__ __forceinline__ float bflo(unsigned u) { return asf(u << 16); }
__device__ __forceinline__ float bfhi(unsigned u) { return asf(u & 0xffff0000u); }
__device__ __forceinline__ v2u pk4(f32x4 v) { return v2u{pk2(v.x, v.y), pk2(v.z, v.w)}; }
__device__ __forceinline__ float dot2(unsigned a, unsigned b, float c) {
  return __builtin_amdgcn_fdot2_f32_bf16(__builtin_bit_cast(bf16x2_t, a), __builtin_bit_cast(bf16x2_t, b), c, false);
}
constexpr unsigned ONES = 0x3f803f80u;

template <int AUX> __device__ __forceinline__ v4u ld128(rsrc_t rs, int voff, int soff) { return __builtin_amdgcn_raw_buffer_load_b128(rs, voff, soff, AUX); }
template <int AUX> __device__ __forceinline__ v2u ld64(rsrc_t rs, int voff, int soff) { return __builtin_amdgcn_raw_buffer_load_b64(rs, voff, soff, AUX); }
template <int AUX> __device__ __forceinline__ void st128(v4u v, rsrc_t rs, int voff, int soff) { __builtin_amdgcn_raw_buffer_store_b128(v, rs, voff + soff, 0, AUX); }
template <int AUX> __device__ __forceinline__ void st64(v2u v, rsrc_t rs, int voff, int soff) { __builtin_amdgcn_raw_buffer_store_b64(v, rs, voff, soff, AUX); }
template <int AUX> __device__ __forceinline__ void st32(unsigned v, rsrc_t rs, int voff, int soff) { __builtin_amdgcn_raw_buffer_store_b32(v, rs, voff, soff, AUX); }
#define SB() __builtin_amdgcn_sched_barrier(0)
#define MFMA_DONE() do { SB(); asm volatile("s_nop 7\n\ts_nop 1" ::: "memory"); SB(); } while (0)
#define TIDX ((int)__builtin_amdgcn_workitem_id_x())
#define BIDX ((int)__builtin_amdgcn_workgroup_id_x())
#define SYNC() do { __builtin_amdgcn_fence(__ATOMIC_RELEASE, "workgroup"); __builtin_amdgcn_s_barrier(); __builtin_amdgcn_fence(__ATOMIC_ACQUIRE, "workgroup"); } while (0)
__device__ __forceinline__ float fmx(float a, float b) { return __builtin_fmaxf(a, b); }
__device__ __forceinline__ float rsq(float a) { return __builtin_amdgcn_rsqf(a); }

__device__ __forceinline__ float sum32(float v) {
  const auto b = __builtin_amdgcn_permlane32_swap(asu(v), asu(v), false, false);
  const unsigned b0 = b[0], b1 = b[1];
  return asf(b0) + asf(b1);
}
__device__ __forceinline__ float sum1632(float v) {
  const auto a = __builtin_amdgcn_permlane16_swap(asu(v), asu(v), false, false);
  const unsigned a0 = a[0], a1 = a[1];
  return sum32(asf(a0) + asf(a1));
}
__device__ __forceinline__ float max1632(float v) {
  const auto a = __builtin_amdgcn_permlane16_swap(asu(v), asu(v), false, false);
  const unsigned a0 = a[0], a1 = a[1];
  v = fmx(asf(a0), asf(a1));
  const auto b = __builtin_amdgcn_permlane32_swap(asu(v), asu(v), false, false);
  const unsigned b0 = b[0], b1 = b[1];
  return fmx(asf(b0), asf(b1));
}

template <typename OP> __device__ __forceinline__ void red1632x2(float& a, float& b, OP op) {
  const auto s = __builtin_amdgcn_permlane16_swap(asu(a), asu(b), false, false);
  const unsigned s0 = s[0], s1 = s[1];
  const float t = op(asf(s0), asf(s1));
  const auto u = __builtin_amdgcn_permlane32_swap(asu(t), asu(t), false, false);
  const unsigned u0 = u[0], u1 = u[1];
  const float v = op(asf(u0), asf(u1));
  const auto x = __builtin_amdgcn_permlane16_swap(asu(v), asu(v), false, false);
  const unsigned x0 = x[0], x1 = x[1];
  a = asf(x0);
  b = asf(x1);
}
__device__ __forceinline__ void max1632x2(float& a, float& b) { red1632x2(a, b, [](float x, float y) { return fmx(x, y); }); }
__device__ __forceinline__ void sum1632x2(float& a, float& b) { red1632x2(a, b, [](float x, float y) { return x + y; }); }

__device__ __forceinline__ void tr8x8(const v4u (&r)[8], v4u (&o)[8]) {
#pragma unroll
  for (int c = 0; c < 4; ++c) {
#pragma unroll
    for (int p = 0; p < 4; ++p) {
      o[2 * c][p] = __builtin_amdgcn_perm(r[2 * p + 1][c], r[2 * p][c], 0x05040100u);
      o[2 * c + 1][p] = __builtin_amdgcn_perm(r[2 * p + 1][c], r[2 * p][c], 0x07060302u);
    }
  }
}
#define RA 2
#define RING_LO 12
#define RING_HI 32
#define QRING_LO 14
#define QRING 8
#define HPRE 12
#define PSLEEP 1
#define BPF 6
#define RAWAUX 2
#define CODE_KB 60
#define RTU0 3
#define RTS 2
#define LOPRE 3
#define LOLATE 0
#define CLEAD 12
#define XLO 6
#define SIGK 12
template <int HI> constexpr int ring() { return HI ? RING_HI : RING_LO; }
template <int HI> constexpr int rq() { return HI ? QRING : QRING_LO; }
constexpr int QPRE = 3;
constexpr int CPRE = CLEAD;
constexpr int cmin(int a, int b) { return a < b ? a : b; }
constexpr int SC1 = 16;
constexpr int A_XT = 0;
constexpr int A_Q = 5 * KS * 1024;
constexpr int A_K = A_Q + 16 * 512;
constexpr int A_V = A_K + 20 * 512;
constexpr int A_EQK = A_V + 20 * 512;
constexpr int A_EV = A_EQK + 5 * 512;
constexpr int B_W = 0, B_RED = 124 * 1024;
constexpr int C_X = 0, C_H = 4 * KS * 1024, C_ST = 8 * KS * 1024;
constexpr int L_CB = C_ST + 1024;
constexpr int D_ST = L_CB + 576 * 8;
constexpr int L_CNT = D_ST + 512;
constexpr int L_STAMP = L_CNT + 64;
#define STAMPCLK() __builtin_amdgcn_s_memrealtime()
constexpr int LDS_BYTES = L_STAMP;
#define STAMP(X_, slot) do {} while (0)
enum { F_A = 0, F_B = 1, F_C1 = 2, F_C2 = 3, F_D = 4, F_Q = 5, F_W = 6 };
typedef unsigned long long u64;
typedef GAS u64 gu64;
__device__ __forceinline__ int flag_off(int kind, int layer, int g) { return 4096 + ((kind * 2 + layer) * 32 + g) * 256; }
#define RLX_AGENT __ATOMIC_RELAXED, __HIP_MEMORY_SCOPE_AGENT
#define RLX_WG __ATOMIC_RELAXED, __HIP_MEMORY_SCOPE_WORKGROUP
#define BAR() do { SB(); asm volatile("s_waitcnt lgkmcnt(0)\n\ts_barrier" ::: "memory"); SB(); } while (0)
#define DRAIN() do { SB(); asm volatile("s_waitcnt vmcnt(0)" ::: "memory"); SB(); } while (0)
__device__ __forceinline__ void poll(unsigned char* ws, int off, u64 tag) {
  gu64* p = (gu64*)(ws + off);
#pragma unroll 1
  for (unsigned spins = 0; spins < (1u << 22); ++spins) {
    const u64 v = __hip_atomic_load(p, RLX_AGENT);
    if (__builtin_amdgcn_ballot_w64(v != tag) == 0) break;
    __builtin_amdgcn_s_sleep(1);
  }
  __builtin_amdgcn_fence(__ATOMIC_ACQUIRE, "wavefront");
  asm volatile("" ::: "memory");
}
__device__ __forceinline__ void signal(unsigned char* ws, int off, u64 tag) { __hip_atomic_store((gu64*)(ws + off), tag, RLX_AGENT); }
__device__ __forceinline__ void lds_wait(LAS unsigned* p, unsigned want) {
#pragma unroll 1
  for (unsigned spins = 0; spins < (1u << 24); ++spins) {
    const unsigned v = __builtin_amdgcn_readfirstlane(__hip_atomic_load(p, RLX_WG));
    if (v >= want) break;
    __builtin_amdgcn_s_sleep(1);
  }
  asm volatile("" ::: "memory");
  SB();
}

#define LDS_ARRIVE(p) do { SB(); asm volatile("s_waitcnt lgkmcnt(0)" ::: "memory"); if (lane == 0) __hip_atomic_fetch_add((p), 1u, RLX_WG); SB(); } while (0)

struct Ctx {
  unsigned char* ws;
  const u16* x;
  const u16* wqkv;
  const u16* wo;
  const u16* w1;
  const u16* w2;
  const u16* b1;
  LAS unsigned char* L;
  rsrc_t rs, rb2, rout;
  u64 tag;
  int g, j, w, lane;
};
__device__ __forceinline__ Ctx fresh(const Ctx& X) {
  Ctx Y = X;
  asm volatile("" : "+v"(Y.lane));
  asm volatile("" : "+s"(Y.g), "+s"(Y.j), "+s"(Y.w));
  return Y;
}

__device__ __forceinline__ int rt_task(int g, int q) { return q < 5 ? g * 5 + q : (g < 2 ? 160 + g : -1); }
__device__ __forceinline__ void rt_load(const Ctx& X, int task, v4u (&r)[8]) {
  const bool is1 = task < 81;
  const int tt = is1 ? task : task - 81, chunk = tt / 9, strip = tt - 9 * chunk;
  const u16* src = is1 ? X.w1 + (size_t)chunk * 64 * F + X.j * 576 + strip * 64 : X.w2 + (size_t)(X.j * 576 + chunk * 64) * D + strip * 64;
  const int ld = is1 ? F : D;
  const rsrc_t rw = __builtin_amdgcn_make_buffer_rsrc((void*)src, 0, (63 * ld + 64) * 2, 0x00020000);
  const int vo = ((X.lane >> 3) * 8 * ld + (X.lane & 7) * 8) * 2;
#pragma unroll
  for (int jj = 0; jj < 8; ++jj) r[jj] = ld128<RAWAUX>(rw, vo, jj * ld * 2);
  SB();
}
__device__ __forceinline__ void rt_turn(const Ctx& X, int task, int q, const v4u (&r)[8]) {
  const int lane = X.lane, kg = lane >> 3, nb = lane & 7;
  LAS unsigned char* sm = X.L + A_Q + q * 8192;
  v4u o[8];
  tr8x8(r, o);
  const int base = (((kg >> 2) * 4 + (nb >> 1)) * 64 + (kg & 3) * 16 + (nb & 1) * 8) * 16;
#pragma unroll
  for (int c = 0; c < 8; ++c) *(LAS v4u*)(sm + base + ((c ^ nb) * 16)) = o[c];
  SB();
}
__device__ __forceinline__ v4u rt_get(const Ctx& X, int q) {
  const int lane = X.lane;
  return *(LAS v4u*)(X.L + A_Q + q * 8192 + (X.w * 64 + (lane & ~7) + ((lane & 7) ^ ((X.w & 3) * 2 + ((lane >> 3) & 1)))) * 16);
}
__device__ __forceinline__ void rt_put(const Ctx& X, int task, v4u v) {
  const bool is1 = task < 81;
  const int tt = is1 ? task : task - 81, chunk = (tt * 57) >> 9, strip = tt - 9 * chunk;
  const int T = strip * 4 + (X.w & 3), lo = strip < 5;
  const int fr = X.j * 1296 + (lo ? ((T * 13) >> 6) * 175 + T : strip * 144 + (X.w & 3)) + ((is1 ? 0 : KS) + chunk * 2 + (X.w >> 2)) * (lo ? 5 : 4);
  st128<SC1>(v, X.rs, X.lane * 16, OFF_WC + fr * 1024);
}

__device__ __forceinline__ void qo_tile(const Ctx& X) {
  constexpr int NQ = 2;
  const rsrc_t rq_ = __builtin_amdgcn_make_buffer_rsrc((void*)X.wqkv, 0, D * 3 * D * 2, 0x00020000);
  const int j = X.j;
  unsigned e[NQ][8];
  int dst[NQ];
  bool zero[NQ];
#pragma unroll
  for (int i = 0; i < NQ; ++i) {
    const int q = i * 256 + (X.w - 4) * 64 + X.lane, P = X.g * 504 + (q < 504 ? q : 503);
    const int fr = P >> 6, blk = (fr * 3641) >> 16, u = fr - blk * KS, gq = (P >> 4) & 3, m = P & 15;
    const int ty = blk < 12 ? blk >> 2 : blk == 12 ? (m >> 1) & 1 : 2;
    const int hd = blk < 12 ? blk & 3 : blk == 12 ? m >> 2 : (m >> 1) & 3;
    const int col = ty * D + (4 * j + hd) * 18 + (blk < 12 ? m : 16 + (m & 1));
    zero[i] = blk == 13 && m >= 8;
    dst[i] = OFF_WQ + (j * 14 * KS * 64 + P) * 16;
    const int vo = ((u * 32 + gq * 8) * 3 * D + col) * 2;
#pragma unroll
    for (int jj = 0; jj < 8; ++jj) e[i][jj] = __builtin_amdgcn_raw_buffer_load_b16(rq_, vo, jj * 3 * D * 2, 0);
  }
  const rsrc_t ro_ = __builtin_amdgcn_make_buffer_rsrc((void*)X.wo, 0, D * D * 2, 0x00020000);
  const int t = (X.w - 4) * 64 + X.lane;
  const int P = X.g * 180 + (t < 180 ? t : 179);
  const int fr = P >> 6, u = (fr * 205) >> 10, tl = fr - u * 5, gq = (P >> 4) & 3, m = P & 15;
  const bool zo = tl * 16 + m >= 72;
  const int col = j * 72 + (zo ? 0 : tl * 16 + m);
  const int r0 = u < 16 ? 36 * u + 4 * gq : 72 * (4 * (u - 16) + gq) + 16;
  unsigned eo[8];
#pragma unroll
  for (int jj = 0; jj < 8; ++jj) {
    const int row = r0 + (u < 16 ? (jj >> 2) * 18 + (jj & 3) : (jj >> 1) * 18 + (jj & 1));
    eo[jj] = __builtin_amdgcn_raw_buffer_load_b16(ro_, (row * D + col) * 2, 0, 0);
  }
  SB();
#pragma unroll
  for (int i = 0; i < NQ; ++i) {
    v4u o;
#pragma unroll
    for (int p = 0; p < 4; ++p) o[p] = zero[i] ? 0u : (e[i][2 * p] | (e[i][2 * p + 1] << 16));
    st128<SC1>(o, X.rs, dst[i], 0);
  }
  v4u o;
#pragma unroll
  for (int p = 0; p < 4; ++p) o[p] = zo ? 0u : (eo[2 * p] | (eo[2 * p + 1] << 16));
  if (t < 180) st128<SC1>(o, X.rs, OFF_WO + (j * 90 * 64 + P) * 16, 0);
  SB();
}

__device__ __forceinline__ void x0_rows(const Ctx& X) {
  const rsrc_t rx = __builtin_amdgcn_make_buffer_rsrc((void*)X.x, 0, S * D * 2, 0x00020000);
  const int tok = X.lane & 7, q8 = X.lane >> 3;
  v4u v[9];
#pragma unroll
  for (int i = 0; i < 9; ++i) v[i] = ld128<0>(rx, (tok * D + q8 * 8 + i * 64) * 2, (X.g * 64 + X.j * 8) * D * 2);
  SB();
  float s = 0.f, q = 0.f;
#pragma unroll
  for (int i = 0; i < 9; ++i)
#pragma unroll
    for (int p = 0; p < 4; ++p) { s = dot2(v[i][p], ONES, s); q = dot2(v[i][p], v[i][p], q); }
  s += asf(__builtin_amdgcn_mov_dpp(asu(s), 0x128, 0xF, 0xF, true));
  q += asf(__builtin_amdgcn_mov_dpp(asu(q), 0x128, 0xF, 0xF, true));
  sum1632x2(s, q);
  const float mean = s * (1.f / D);
  const float var = fmx(q * (1.f / D) - mean * mean, 0.f);
  const float rstd = rsq(var + 1e-5f), nm = -mean * rstd;
#pragma unroll
  for (int i = 0; i < 9; ++i) {
    v4u o;
#pragma unroll
    for (int p = 0; p < 4; ++p) o[p] = pk2(__builtin_fmaf(bflo(v[i][p]), rstd, nm), __builtin_fmaf(bfhi(v[i][p]), rstd, nm));
    st128<SC1>(o, X.rs, (q8 * 16 + tok) * 16 + i * 2048, OFF_XN + (4 * X.g + (X.j >> 1)) * KS * 1024 + (X.j & 1) * 128);
  }
  SB();
}

template <int HI> __device__ __forceinline__ int wq_a(const Ctx& X) { return OFF_WQ + (X.j * 14 + (HI ? X.w + 4 : X.w)) * KS * 1024; }
template <int HI> __device__ __forceinline__ int wq_b(const Ctx& X) { return OFF_WQ + (X.j * 14 + (HI ? 12 + ((X.w - 4) >> 1) : X.w + 4)) * KS * 1024; }
template <int HI, int U0, int U1>
__device__ __forceinline__ void wq_prefetch(const Ctx& X, v4u (&wa)[rq<HI>()], v4u (&wb)[rq<HI>()]) {
  const int sa = wq_a<HI>(X), sb = wq_b<HI>(X);
#pragma unroll
  for (int u = U0; u < U1; ++u) {
    wa[u] = ld128<SC1>(X.rs, X.lane * 16 + (u & 3) * 1024, sa + (u >> 2) * 4096);
    wb[u] = ld128<SC1>(X.rs, X.lane * 16 + (u & 3) * 1024, sb + (u >> 2) * 4096);
  }
  SB();
}

template <int I0, int I1>
__device__ __forceinline__ void xn_load(const Ctx& X, int layer, v4u (&t)[12]) {
  const int base = OFF_XN + layer * XBUF + (4 * X.g - 1) * KS * 1024;
#pragma unroll
  for (int i = I0; i < I1; ++i) {
    const int f = i < 11 ? 8 * i + X.w : (X.w < 2 ? 88 + X.w : 89), u = f / 5, rb5 = f - 5 * u;
    t[i] = ld128<SC1>(X.rs, X.lane * 16, base + ((X.g == 0 && rb5 == 0 ? 1 : rb5) * KS + u) * 1024);
  }
  SB();
}

template <int LO, int HIX, int R>
__device__ __forceinline__ void ring_fill(const Ctx& X, int run, v4u (&a)[R]) {
#pragma unroll
  for (int i = LO; i < HIX; ++i) a[i] = ld128<SC1>(X.rs, X.lane * 16 + (i & 3) * 1024, run + (i >> 2) * 4096);
  SB();
}
template <int LO, int HIX, int R>
__device__ __forceinline__ void ring_zero(v4u (&a)[R]) {
#pragma unroll
  for (int i = LO; i < HIX; ++i) a[i] = v4u{0, 0, 0, 0};
}

template <int HI>
__device__ __forceinline__ void stage_a(const Ctx& X, int layer, const v4u (&t)[12], v4u (&wa)[rq<HI>()], v4u (&wb)[rq<HI>()]) {
  constexpr int RQ = rq<HI>();
  LAS unsigned char* L = X.L;
  const rsrc_t rs = X.rs;
  const int g = X.g, j = X.j, w = X.w, lane = X.lane;
  v4u wt[12];
  {
    constexpr int A0 = HI ? 0 : 1, B1 = HI ? 3 : 5;
    const int h = w & 3;
    const int sa = wq_a<HI>(X), sb = wq_b<HI>(X);
    f32x4 ac[5], bc[5];
    v4u xf[2][5];
    const int rot = HI ? (w & 1) * 2 : 0;
    LAS unsigned char* xa = L + A_XT + lane * 16 + rot * 1024;
    LAS unsigned char* xb = L + A_XT + lane * 16 + (rot ? 0 : 3) * 1024;
    LAS unsigned char* xa2 = xa + 12 * 5120;
    LAS unsigned char* xb2 = xb + 12 * 5120;
#define XF(r, u) (*(LAS v4u*)(((u) < 12 ? ((r) < 3 ? xa : xb) : ((r) < 3 ? xa2 : xb2)) + ((u) < 12 ? (u) : (u) - 12) * 5120 + ((r) < 3 ? (r) : (r) - 3) * 1024))
#pragma unroll
    for (int r = 0; r < 5; ++r) xf[0][r] = XF(r, 0);
#pragma unroll
    for (int r = 0; r < 5; ++r) xf[1][r] = XF(r, 1);
    SB();
#pragma unroll
    for (int u = 0; u < KS; ++u) {
      if (u == 1) {
#pragma unroll
        for (int i = 5; i < 12; ++i)
          if (i < 11 || w < 2) *(LAS v4u*)(L + A_XT + (8 * i + w) * 1024 + lane * 16) = t[i];
        SB();
      }
      if (u == 2) { SB(); asm volatile("s_waitcnt lgkmcnt(0)" ::: "memory"); if (lane == 0) __hip_atomic_fetch_add((LAS unsigned*)(L + L_CNT + 16), 1u, RLX_WG); SB(); }
      if (u == 6) lds_wait((LAS unsigned*)(L + L_CNT + 16), 8 * (layer + 1));
#pragma unroll
      for (int r = 0; r < 5; ++r) {
        if (r >= A0) {
          if (u == 0) { if (HI) m16z(ac[r], xf[u & 1][r], wa[u % RQ]); else m16z(ac[r], wa[u % RQ], xf[u & 1][r]); }
          else { if (HI) m16(ac[r], xf[u & 1][r], wa[u % RQ]); else m16(ac[r], wa[u % RQ], xf[u & 1][r]); }
        }
        if (r < B1) { if (u == 0) m16z(bc[r], wb[u % RQ], xf[u & 1][r]); else m16(bc[r], wb[u % RQ], xf[u & 1][r]); }
        if (u + 2 < KS) xf[u & 1][r] = XF(r, u + 2);
        SB();
      }
      {
        const int l0 = u == 0 ? QPRE : cmin(KS, cmin(u + RQ, QPRE + 2 * u)), l1 = cmin(KS, cmin(u + 1 + RQ, QPRE + 2 * (u + 1)));
#pragma unroll
        for (int l = QPRE; l < KS; ++l)
          if (l >= l0 && l < l1 && (HI || l >= LOPRE || layer == 0)) {
            wa[l % RQ] = ld128<SC1>(rs, lane * 16 + (l & 3) * 1024, sa + (l >> 2) * 4096);
            wb[l % RQ] = ld128<SC1>(rs, lane * 16 + (l & 3) * 1024, sb + (l >> 2) * 4096);
          }
        if (u >= RTU0 && u < RTU0 + 6 * RTS && (u - RTU0) % RTS == 0 && layer == 0) {
          const int q = (u - RTU0) / RTS, task = rt_task(g, q);
          if (task >= 0) rt_put(X, task, rt_get(X, q));
          if (q == 5) LDS_ARRIVE((LAS unsigned*)(L + L_CNT + 52));
        }
        if (u >= 10 && u < 16) {
#pragma unroll
          for (int i = 2 * (u - 10); i < 2 * (u - 10) + 2; ++i) wt[i] = ld128<SC1>(rs, lane * 16, OFF_WO + (j * 90 + i * 8 + w) * 1024);
        }
        SB();
      }
    }
#undef XF
    MFMA_DONE();
    lds_wait((LAS unsigned*)(L + L_CNT + 52), 8);
    if (!HI) {
#pragma unroll
      for (int r = 1; r < 5; ++r) *(LAS v2u*)(L + A_Q + (h * 4 + r - 1) * 512 + lane * 8) = pk4(ac[r]);
#pragma unroll
      for (int r = 0; r < 5; ++r) *(LAS v2u*)(L + A_K + (h * 5 + r) * 512 + lane * 8) = pk4(bc[r]);
    } else {
#pragma unroll
      for (int r = 0; r < 5; ++r) *(LAS v2u*)(L + A_V + (h * 5 + (r < 3 ? rot + r : (rot ? 0 : 3) + r - 3)) * 512 + lane * 8) = pk4(ac[r]);
      if (w < 6) {
#pragma unroll
        for (int r = 0; r < 3; ++r) *(LAS v2u*)(L + A_EQK + (rot + r) * 512 + lane * 8) = pk4(bc[r]);
      } else {
        const int c = lane & 15, gq = lane >> 4;
#pragma unroll
        for (int r = 0; r < 3; ++r) {
          const v2u v = pk4(bc[r]);
          LAS u16* p = (LAS u16*)(L + A_EV + (rot + r) * 512 + ((c >> 2) * 16 + 4 * gq) * 8 + (c & 3) * 2);
          p[0] = (u16)v.x;
          p[4] = (u16)(v.x >> 16);
          p[8] = (u16)v.y;
          p[12] = (u16)(v.y >> 16);
        }
      }
    }
  }
  STAMP(X, layer * 32 + 3);
  BAR();
  STAMP(X, layer * 32 + 4);
  {
    const int c = lane & 15, gq = lane >> 4, rb = w & 3;
    typedef short s16x4 __attribute__((ext_vector_type(4)));
    auto mk = [](v2u a, v2u b, f32x4 cc) {
      return __builtin_amdgcn_mfma_f32_16x16x16bf16_1k(__builtin_bit_cast(s16x4, a), __builtin_bit_cast(s16x4, b), cc, 0, 0, 0);
    };
    LAS unsigned char* lp = L + rb * 512 + lane * 8;
    const v2u e0 = *(LAS v2u*)(lp + A_EQK), e1 = *(LAS v2u*)(lp + A_EQK + 512);
    v2u qm[2], k0[2], k1[2], v0[2], v1[2];
#pragma unroll
    for (int hh = 0; hh < 2; ++hh) {
      const int h = 2 * HI + hh;
      qm[hh] = *(LAS v2u*)(lp + A_Q + h * 4 * 512);
      k0[hh] = *(LAS v2u*)(lp + A_K + h * 5 * 512);
      k1[hh] = *(LAS v2u*)(lp + A_K + (h * 5 + 1) * 512);
    }
#pragma unroll
    for (int hh = 0; hh < 2; ++hh) {
      const int h = 2 * HI + hh;
      v0[hh] = *(LAS v2u*)(lp + A_V + h * 5 * 512);
      v1[hh] = *(LAS v2u*)(lp + A_V + (h * 5 + 1) * 512);
    }
    const v2u x0 = *(LAS v2u*)(lp + A_EV), x1 = *(LAS v2u*)(lp + A_EV + 512);
    SB();
    const bool first = (g == 0 && rb == 0);
    const bool par = c & 1;
    bool own[2], ok[2];
#pragma unroll
    for (int e = 0; e < 2; ++e) {
      own[e] = 4 * gq + 2 * e <= (c & 14);
      ok[e] = own[e] || !first;
    }
    const f32x4 z = {0, 0, 0, 0};
    f32x4 s0[2], s1[2];
#pragma unroll
    for (int hh = 0; hh < 2; ++hh) {
      s0[hh] = mk(k0[hh], qm[hh], z);
      s1[hh] = mk(k1[hh], qm[hh], z);
    }
#pragma unroll
    for (int hh = 0; hh < 2; ++hh) {
      const v2u qe = {0u, gq == 2 * HI + hh ? e1.x : 0u};
      s0[hh] = mk(e0, qe, s0[hh]);
      s1[hh] = mk(e1, qe, s1[hh]);
    }
    float sv[2][2], m[2], p[2][2], inv[2];
#pragma unroll
    for (int hh = 0; hh < 2; ++hh) {
      const float a0 = s0[hh].x, a1 = s0[hh].y, a2 = s0[hh].z, a3 = s0[hh].w, b0 = s1[hh].x, b1 = s1[hh].y, b2 = s1[hh].z, b3 = s1[hh].w;
      const float l0 = own[0] ? b0 : a0, h0 = own[0] ? b1 : a1, l1 = own[1] ? b2 : a2, h1 = own[1] ? b3 : a3;
      sv[hh][0] = ok[0] ? (par ? h0 : l0) : -3.0e38f;
      sv[hh][1] = ok[1] ? (par ? h1 : l1) : -3.0e38f;
      m[hh] = fmx(sv[hh][0], sv[hh][1]);
    }
    max1632x2(m[0], m[1]);
    constexpr float SC = 0.23570226039551584f * 1.4426950408889634f;
#pragma unroll
    for (int hh = 0; hh < 2; ++hh) {
      p[hh][0] = __builtin_amdgcn_exp2f((sv[hh][0] - m[hh]) * SC);
      p[hh][1] = __builtin_amdgcn_exp2f((sv[hh][1] - m[hh]) * SC);
      inv[hh] = p[hh][0] + p[hh][1];
    }
    sum1632x2(inv[0], inv[1]);
    f32x4 om[2], ox[2];
#pragma unroll
    for (int hh = 0; hh < 2; ++hh) {
      const float r = __builtin_amdgcn_rcpf(inv[hh]);
      const float q0 = p[hh][0] * r, q1 = p[hh][1] * r;
      const unsigned d0 = pk2(par ? 0.f : q0, par ? q0 : 0.f), d1 = pk2(par ? 0.f : q1, par ? q1 : 0.f);
      const v2u pp = {own[0] ? 0u : d0, own[1] ? 0u : d1}, po = {own[0] ? d0 : 0u, own[1] ? d1 : 0u};
      om[hh] = mk(v1[hh], po, mk(v0[hh], pp, z));
      ox[hh] = mk(x1, po, mk(x0, pp, z));
    }
    const int ob = OFF_O + (4 * g + rb) * KS * 1024;
    const v2u a = pk4(om[0]), b = pk4(om[1]);
    st128<SC1>(v4u{a.x, a.y, b.x, b.y}, rs, lane * 16, ob + (2 * j + HI) * 1024);
    if (gq == HI) st64<SC1>(v2u{pk2(ox[0].x, ox[0].y), pk2(ox[1].z, ox[1].w)}, rs, c * 8, ob + 16 * 1024 + (2 * j + HI) * 128);
  }
  STAMP(X, layer * 32 + 5);
#pragma unroll
  for (int i = 0; i < 12; ++i)
    if (i < 11 || w < 2) *(LAS v4u*)(L + B_W + (i * 8 + w) * 1024 + lane * 16) = wt[i];
  DRAIN();
  BAR();
  if (HI && w == 7 && lane < 2 - layer) signal(X.ws, lane ? flag_off(F_W, 0, j) + g * 8 : flag_off(F_A, layer, g) + j * 8, X.tag);
  STAMP(X, layer * 32 + 6);
}

template <int HI>
__device__ __forceinline__ void stage_b(const Ctx& X, int layer) {
  LAS unsigned char* L = X.L;
  const rsrc_t rs = X.rs;
  const int g = X.g, j = X.j, w = X.w, lane = X.lane;
  constexpr int kh = HI, NK = HI ? 2 : 3;
  const int rb = w & 3, c = lane & 15, gq = lane >> 4;
  const bool poller = HI && w == 7;
  const int xb = OFF_X2 + (4 * g + rb) * KS * 1024;
  const rsrc_t rx = __builtin_amdgcn_make_buffer_rsrc((void*)X.x, 0, S * D * 2, 0x00020000);
  int xo[NK];
  v2u res[NK];
#pragma unroll
  for (int i = 0; i < NK; ++i) {
    const int t = kh * 3 + i, d0 = 72 * j + 16 * t + 4 * gq;
    xo[i] = (d0 >> 5) * 1024 + (((d0 >> 3) & 3) * 16 + c) * 16 + ((d0 >> 2) & 1) * 8;
    res[i] = v2u{0, 0};
    if (t < 4 || gq < 2) {
      if (layer) res[i] = ld64<SC1>(rs, xo[i], xb);
      else res[i] = ld64<0>(rx, (c * D + d0) * 2, (4 * g + rb) * 16 * D * 2);
    }
  }
  SB();
  v4u wf[6][5];
#pragma unroll
  for (int u = 0; u < 6; ++u)
#pragma unroll
    for (int t = 0; t < 5; ++t) wf[u][t] = *(LAS v4u*)(L + B_W + ((kh * 9 + u) * 5 + t) * 1024 + lane * 16);
  SB();
  if (poller) {
    poll(X.ws, flag_off(F_A, layer, g) + (lane & 7) * 8, X.tag);
  }
  STAMP(X, layer * 32 + 7);
  BAR();
  STAMP(X, layer * 32 + 8);
  v4u of[9];
  const int ob = OFF_O + (4 * g + rb) * KS * 1024;
#pragma unroll
  for (int u = 0; u < (kh ? 7 : 9); ++u) of[u] = ld128<SC1>(rs, lane * 16 + (u & 3) * 1024, ob + kh * 9 * 1024 + (u >> 2) * 4096);
  if (kh) {
#pragma unroll
    for (int u = 7; u < 9; ++u) {
      const v2u p = ld64<SC1>(rs, gq * 256 + c * 8 + (u - 7) * 1024, ob + 16 * 1024);
      const v2u q = ld64<SC1>(rs, gq * 256 + c * 8 + (u - 7) * 1024 + 128, ob + 16 * 1024);
      of[u] = v4u{p.x, p.y, q.x, q.y};
    }
  }
  SB();
  f32x4 acc[5];
#pragma unroll
  for (int t = 0; t < 5; ++t) acc[t] = f32x4{0, 0, 0, 0};
#pragma unroll
  for (int u = 0; u < 9; ++u) {
#pragma unroll
    for (int t = 0; t < 5; ++t) {
      acc[t] = mfma16(wf[u % 6][t], of[u], acc[t]);
      if (u < 3) wf[u][t] = *(LAS v4u*)(L + B_W + ((kh * 9 + u + 6) * 5 + t) * 1024 + lane * 16);
    }
    SB();
  }
#pragma unroll
  for (int t = (kh ? 0 : 3); t < (kh ? 3 : 5); ++t) *(LAS f32x4*)(L + B_RED + (rb * 5 + t) * 1024 + lane * 16) = acc[t];
  STAMP(X, layer * 32 + 9);
  BAR();
  const int x1b = OFF_X1 + (4 * g + rb) * KS * 1024;
  float rsum = 0.f, rsq2 = 0.f;
#pragma unroll
  for (int i = 0; i < NK; ++i) {
    const int t = kh * 3 + i;
    const f32x4 o = *(LAS f32x4*)(L + B_RED + (rb * 5 + t) * 1024 + lane * 16);
    f32x4 v = o + acc[t];
    v.x += bflo(res[i].x); v.y += bfhi(res[i].x); v.z += bflo(res[i].y); v.w += bfhi(res[i].y);
    const v2u y = pk4(v);
    if (t < 4 || gq < 2) st64<SC1>(y, rs, xo[i], x1b);
    rsum = dot2(y.x, ONES, dot2(y.y, ONES, rsum));
    rsq2 = dot2(y.x, y.x, dot2(y.y, y.y, rsq2));
  }
  sum1632x2(rsum, rsq2);
  if (lane < 16) st64<SC1>(v2u{asu(rsum), asu(rsq2)}, rs, lane * 8, OFF_RS + ((4 * g + rb) * 16 + 2 * j + kh) * 128);
  STAMP(X, layer * 32 + 10);
  DRAIN();
  BAR();
  STAMP(X, layer * 32 + 11);
  if (poller) {
    if (lane == 0) signal(X.ws, flag_off(F_B, layer, g) + j * 8, X.tag);
    poll(X.ws, (lane >= 32 && layer == 0) ? flag_off(F_W, 0, j) + (lane & 31) * 8 : flag_off(F_B, layer, g) + (lane & 7) * 8, X.tag);
  }
  BAR();
  STAMP(X, layer * 32 + 12);
}

template <int NT, int BASE, int LEAD, int R, typename HOOK>
__device__ __forceinline__ void c_gemm(LAS unsigned char* Xl, rsrc_t rs, int run, v4u (&a)[R], f32x4 (&acc)[NT][4], const f32x4 (&c0)[NT], int lane, HOOK hook) {
  constexpr int TOTAL = 2 * KS * NT;
  v4u xf[2][4];
  LAS unsigned char* xp = Xl + lane * 16;
  LAS unsigned char* xq = xp + 2 * KS * 1024;
#define XF(r, u) (*(LAS v4u*)(((r) < 2 ? xp : xq) + (((r) & 1) * KS + (u)) * 1024))
#pragma unroll
  for (int r = 0; r < 4; ++r) xf[0][r] = XF(r, 0);
  SB();
#pragma unroll
  for (int u = 0; u < KS; ++u) {
    hook(u);
#pragma unroll
    for (int t = 0; t < NT; ++t) {
      const int idx = BASE + u * NT + t;
#pragma unroll
      for (int r = 3; r >= 0; --r) {
        if (u == 0 && BASE == 0) m16c(acc[t][r], a[idx % R], xf[u & 1][r], c0[t]);
        else if (u == 0) m16z(acc[t][r], a[idx % R], xf[u & 1][r]);
        else m16(acc[t][r], a[idx % R], xf[u & 1][r]);
        if (r >= 2 && t < 2 && u + 1 < KS) { xf[(u + 1) & 1][2 * t + 3 - r] = XF(2 * t + 3 - r, u + 1); SB(); }
      }
      if (idx + LEAD < TOTAL) a[(idx + LEAD) % R] = ld128<SC1>(rs, lane * 16 + ((idx + LEAD) & 3) * 1024, run + ((idx + LEAD) >> 2) * 4096);
      SB();
    }
  }
#undef XF
  MFMA_DONE();
}

template <int HI>
__device__ __forceinline__ void stage_c(const Ctx& X, int layer, int run) {
  v4u a[ring<HI>()];
  constexpr int NT = 5 - HI, R = ring<HI>();
  LAS unsigned char* L = X.L;
  const rsrc_t rs = X.rs;
  const int g = X.g, j = X.j, w = X.w, lane = X.lane;
  const int c = lane & 15, gq = lane >> 4, rb = w & 3;
  const int T0 = HI ? 20 + 4 * (w - 4) : 5 * w;
  LAS unsigned* c_lo = (LAS unsigned*)(L + L_CNT + 20);
  LAS unsigned* c_hi = (LAS unsigned*)(L + L_CNT + 24);
  LAS unsigned* sig = (LAS unsigned*)(L + L_CNT + 32 + rb * 4);
  const unsigned four = 4 * (layer + 1);
  f32x4 bias[NT];
  {
    constexpr int U0 = HI ? XLO : 0, NU = HI ? KS - XLO : XLO;
    __builtin_amdgcn_s_setprio(HI ? 2 : 3);
    const int xb = OFF_X1 + ((4 * g + rb) * KS + U0) * 1024;
    v2u ps[4];
#pragma unroll
    for (int k = 0; k < 4; ++k) ps[k] = ld64<SC1>(rs, gq * 512 + k * 128 + c * 8, OFF_RS + (4 * g + rb) * 2048);
    v4u v[NU];
#pragma unroll
    for (int u = 0; u < NU; ++u) v[u] = ld128<SC1>(rs, lane * 16 + (u & 3) * 1024, xb + (u >> 2) * 4096);
    SB();
    if (!HI) ring_fill<0, CPRE>(X, run, a);
#pragma unroll
    for (int t = 0; t < NT; ++t) bias[t] = *(LAS f32x4*)(L + L_CB + ((T0 + t) * 16 + 4 * gq) * 4);
    float s = (asf(ps[0].x) + asf(ps[1].x)) + (asf(ps[2].x) + asf(ps[3].x)), q = (asf(ps[0].y) + asf(ps[1].y)) + (asf(ps[2].y) + asf(ps[3].y));
    sum1632x2(s, q);
    const float mean = s * (1.f / D);
    const float var = fmx(q * (1.f / D) - mean * mean, 0.f);
    const float rstd = rsq(var + 1e-5f), nm = -mean * rstd;
#pragma unroll
    for (int u = 0; u < NU; ++u) {
      v4u o;
#pragma unroll
      for (int p = 0; p < 4; ++p) o[p] = pk2(__builtin_fmaf(bflo(v[u][p]), rstd, nm), __builtin_fmaf(bfhi(v[u][p]), rstd, nm));
      *(LAS v4u*)(L + C_X + (rb * KS + U0 + u) * 1024 + lane * 16) = o;
    }
    LDS_ARRIVE(HI ? c_hi : c_lo);
    STAMP(X, layer * 32 + 16);
    if (HI) ring_fill<0, CPRE>(X, run, a);
    __builtin_amdgcn_s_setprio(0);
    lds_wait(c_lo, four);
    if (HI) {
      lds_wait(c_hi, four);
      if (SIGK >= 0) lds_wait(sig, layer + 1);
    }
    STAMP(X, layer * 32 + 17);
  }
  f32x4 acc[NT][4];
  c_gemm<NT, 0, CPRE>(L + C_X, rs, run, a, acc, bias, lane, [&](int u) {
    if (!HI && u == XLO - 1) lds_wait(c_hi, four);
    if (!HI && u == SIGK) { __hip_atomic_store(sig, (unsigned)(layer + 1), RLX_WG); SB(); }
  });
  STAMP(X, layer * 32 + 19);
  {
#pragma unroll
    for (int t = 0; t < NT; ++t) {
      const int T = T0 + t;
#pragma unroll
      for (int r = 0; r < 4; ++r) {
        float o[4];
#pragma unroll
        for (int e = 0; e < 4; ++e) {
          const float v = acc[t][r][e];
          const float p = __builtin_fmaf(v * v, -0.10294324f, -2.3022082f);
          const float ex = __builtin_amdgcn_exp2f(v * p);
          o[e] = v * __builtin_amdgcn_rcpf(1.0f + ex);
        }
        *(LAS v2u*)(L + C_H + (r * KS + (T >> 1)) * 1024 + ((2 * (T & 1) + (gq >> 1)) * 16 + c) * 16 + (gq & 1) * 8) = v2u{pk2(o[0], o[1]), pk2(o[2], o[3])};
#pragma unroll
        for (int k = CPRE; k < R; ++k)
          if ((k - CPRE) * (4 * NT) / (R > CPRE ? R - CPRE : 1) == t * 4 + r) {
            const int l = KS * NT + k;
            a[l % R] = ld128<SC1>(rs, lane * 16 + (l & 3) * 1024, run + (l >> 2) * 4096);
          }
        SB();
      }
    }
  }
  STAMP(X, layer * 32 + 20);
  if (HI) {
    BAR();
  } else {
    LDS_ARRIVE((LAS unsigned*)(L + L_CNT + 8));
    lds_wait((LAS unsigned*)(L + L_CNT + 8), four);
  }
  STAMP(X, layer * 32 + 21);
  c_gemm<NT, KS * NT, R>(L + C_H, rs, run, a, acc, bias, lane, [&](int u) {
    if (!HI && u == 9) BAR();
  });
  STAMP(X, layer * 32 + 22);
  const int pb = OFF_P + g * 64 * 9216 + j * 9216 + T0 * 256;
  const int pv = (c >> 3) * 8 * 9216 + (c & 7) * 32 + gq * 8;
#pragma unroll
  for (int t = 0; t < NT; ++t)
#pragma unroll
    for (int r = 0; r < 4; ++r) st64<SC1>(pk4(acc[t][r]), rs, pv + t * 256, pb + 2 * r * 8 * 9216);
  STAMP(X, layer * 32 + 23);
  DRAIN();
  if (lane == 0) {
    const unsigned old = __hip_atomic_fetch_add((LAS unsigned*)(L + L_CNT + HI * 4), 1u, RLX_WG);
    if ((old & 3u) == 3u) signal(X.ws, flag_off(HI ? F_C2 : F_C1, layer, g) + j * 8, X.tag);
  }
  STAMP(X, layer * 32 + 24);
}

template <int HI, int LAYER, typename PRE>
__device__ __forceinline__ void stage_d(const Ctx& X, PRE pre) {
  constexpr int layer = LAYER;
  LAS unsigned char* L = X.L;
  const rsrc_t rs = X.rs;
  const int g = X.g, j = X.j, w = X.w, lane = X.lane;
  const int pb = OFF_P + (g * 8 + j) * 8 * 9216;
  const int RB = 4 * g + (j >> 1);
  const int tok = (lane >> 1) & 7, hf = lane & 1;
  constexpr int I0 = HI ? 2 : 0;
  int fo[3], pp[3];
  v4u xr[3], bv[3], o[3];
  pp[0] = w * 64 + lane;
  pp[1] = 256 + w * 16 + (lane & 15);
  pp[2] = 320 + w * 32 + (lane & 31);
#pragma unroll
  for (int i = I0; i < 3; ++i) {
    const int T = pp[i] >> 4;
    fo[i] = (T >> 1) * 1024 + ((2 * (T & 1) + hf) * 16 + (j & 1) * 8 + tok) * 16;
    xr[i] = ld128<SC1>(rs, fo[i], OFF_X1 + RB * KS * 1024);
    bv[i] = ld128<0>(X.rb2, (T * 16 + hf * 8) * 2, 0);
  }
  SB();
  float s = 0.f, q = 0.f;
  auto finish = [&](int i, const float (&l2)[4], const float (&h2)[4], int nl) {
#pragma unroll
    for (int e = 0; e < 4; ++e) o[i][e] = pk2(bflo(bv[i][e]) + bflo(xr[i][e]) + l2[e], bfhi(bv[i][e]) + bfhi(xr[i][e]) + h2[e]);
    if (lane < nl) {
      if (layer == 0) st128<SC1>(o[i], rs, fo[i], OFF_X2 + RB * KS * 1024);
      else st128<0>(o[i], X.rout, ((g * 64 + j * 8 + tok) * D + (pp[i] >> 4) * 16 + hf * 8) * 2, 0);
      if (layer == 0) {
#pragma unroll
        for (int e = 0; e < 4; ++e) {
          const float lo = bflo(o[i][e]), hi = bfhi(o[i][e]);
          s += lo + hi;
          q = __builtin_fmaf(lo, lo, __builtin_fmaf(hi, hi, q));
        }
      }
    }
  };
  if (!HI) {
    poll(X.ws, flag_off(F_C1, layer, g) + (lane & 7) * 8, X.tag);
    STAMP(X, layer * 32 + 25);
    v4u pv[8], pz[2];
#pragma unroll
    for (int k = 0; k < 8; ++k) pv[k] = ld128<SC1>(rs, pp[0] * 16, pb + k * 9216);
#pragma unroll
    for (int k = 0; k < 2; ++k) pz[k] = ld128<SC1>(rs, pp[1] * 16 + (lane >> 4) * 2 * 9216, pb + k * 9216);
    SB();
    float l2[4], h2[4];
#pragma unroll
    for (int e = 0; e < 4; ++e) {
      l2[e] = 0.f; h2[e] = 0.f;
#pragma unroll
      for (int k = 0; k < 8; ++k) { l2[e] += bflo(pv[k][e]); h2[e] += bfhi(pv[k][e]); }
    }
    finish(0, l2, h2, 64);
#pragma unroll
    for (int e = 0; e < 4; ++e) {
      l2[e] = sum1632(bflo(pz[0][e]) + bflo(pz[1][e]));
      h2[e] = sum1632(bfhi(pz[0][e]) + bfhi(pz[1][e]));
    }
    finish(1, l2, h2, 16);
    SB();
  }
  STAMP(X, layer * 32 + 26);
  if (HI && w == 7) poll(X.ws, flag_off(F_C2, layer, g) + (lane & 7) * 8, X.tag);
  else pre();
  BAR();
  STAMP(X, layer * 32 + 27);
  {
    v4u pv[4];
#pragma unroll
    for (int k = 0; k < 4; ++k) pv[k] = ld128<SC1>(rs, pp[2] * 16 + (lane >> 5) * 4 * 9216, pb + k * 9216);
    SB();
    float l2[4], h2[4];
#pragma unroll
    for (int e = 0; e < 4; ++e) {
      l2[e] = 0.f; h2[e] = 0.f;
#pragma unroll
      for (int k = 0; k < 4; ++k) { l2[e] += bflo(pv[k][e]); h2[e] += bfhi(pv[k][e]); }
      l2[e] = sum32(l2[e]);
      h2[e] = sum32(h2[e]);
    }
    finish(2, l2, h2, 32);
  }
  STAMP(X, layer * 32 + 28);
  if (layer == 0) {
    s += asf(__builtin_amdgcn_mov_dpp(asu(s), 0xB1, 0xF, 0xF, true));
    q += asf(__builtin_amdgcn_mov_dpp(asu(q), 0xB1, 0xF, 0xF, true));
    s = sum1632(s);
    q = sum1632(q);
    if (lane < 16 && hf == 0) *(LAS f32x2*)(L + D_ST + (tok * 8 + w) * 8) = f32x2{s, q};
    BAR();
    float ts = 0.f, tq = 0.f;
#pragma unroll
    for (int k = 0; k < 4; ++k) {
      const f32x4 v = *(LAS f32x4*)(L + D_ST + tok * 64 + k * 16);
      ts += v.x + v.z;
      tq += v.y + v.w;
    }
    const float mean = ts * (1.f / D);
    const float var = fmx(tq * (1.f / D) - mean * mean, 0.f);
    const float rstd = rsq(var + 1e-5f), nm = -mean * rstd;
#pragma unroll
    for (int i = I0; i < 3; ++i)
      if (lane < (i == 0 ? 64 : i == 1 ? 16 : 32)) {
        v4u y;
#pragma unroll
        for (int e = 0; e < 4; ++e) y[e] = pk2(__builtin_fmaf(bflo(o[i][e]), rstd, nm), __builtin_fmaf(bfhi(o[i][e]), rstd, nm));
        st128<SC1>(y, rs, fo[i], OFF_XN + XBUF + RB * KS * 1024);
      }
    SB();
  }
}

template <int HI>
__device__ __forceinline__ void body(Ctx& X) {
  const int run0 = OFF_WC + (X.j * 1296 + (HI ? 720 + (X.w - 4) * 144 : X.w * 180)) * 1024;
  v4u wa[rq<HI>()], wb[rq<HI>()];
  asm volatile("; role %0" ::"i"(HI) : "memory");
#pragma unroll
  for (int u = 0; u < rq<HI>(); ++u) { wa[u] = v4u{0, 0, 0, 0}; wb[u] = v4u{0, 0, 0, 0}; }
#pragma unroll 1
  for (int layer = 0;; ++layer) {
    {
      v4u xt[12];
      xn_load<0, 5>(fresh(X), layer, xt);
      if (layer == 0) wq_prefetch<HI, 0, QPRE>(fresh(X), wa, wb);
      asm volatile("s_barrier" ::: "memory");
      xn_load<5, 12>(fresh(X), layer, xt);
#pragma unroll
      for (int i = 0; i < 5; ++i) *(LAS v4u*)(X.L + A_XT + (8 * i + X.w) * 1024 + X.lane * 16) = xt[i];
      STAMP(X, layer * 32 + 1);
      BAR();
      STAMP(X, layer * 32 + 2);
      stage_a<HI>(fresh(X), layer, xt, wa, wb);
    }
    stage_b<HI>(fresh(X), layer);
    int run = run0;
    asm volatile("" : "+s"(run));
    stage_c<HI>(fresh(X), layer, run);
    if (layer == 1) break;
    if (HI || !LOLATE) wq_prefetch<HI, 0, HI ? QPRE : LOPRE>(fresh(X), wa, wb);
    stage_d<HI, 0>(fresh(X), [&] { if (!HI && LOLATE) wq_prefetch<HI, 0, HI ? QPRE : LOPRE>(fresh(X), wa, wb); });
    DRAIN();
    STAMP(X, 29);
    BAR();
    if (HI && X.w == 7) {
      if (X.lane == 0) signal(X.ws, flag_off(F_D, 1, X.g) + X.j * 8, X.tag);
      if (BIDX == 0 && X.lane == 1) signal(X.ws, 0, X.tag);
      poll(X.ws, (X.lane >= 32 && X.g > 0) ? flag_off(F_D, 1, X.g - 1) + (6 + (X.lane & 1)) * 8 : flag_off(F_D, 1, X.g) + (X.lane & 7) * 8, X.tag);
    }
    BAR();
    STAMP(X, 32);
  }
  stage_d<HI, 1>(fresh(X), [] {});
  STAMP(X, 32 + 29);
}

}

extern "C" __global__ void __launch_bounds__(NTHR, 2) ilk_main(unsigned char* ws, const u16* x, const u16* wqkv, const u16* wo, const u16* w1, const u16* w2,
                                                  const u16* b1, const u16* b2, u16* out) {
  extern __shared__ __attribute__((aligned(16))) unsigned char smem[];
  Ctx X;
  X.ws = ws;
  X.x = x;
  X.wqkv = wqkv;
  X.wo = wo;
  X.w1 = w1;
  X.w2 = w2;
  X.b1 = b1;
  X.L = (LAS unsigned char*)smem;
  X.rs = __builtin_amdgcn_make_buffer_rsrc((void*)ws, 0, WS_BYTES, 0x00020000);
  X.rb2 = __builtin_amdgcn_make_buffer_rsrc((void*)b2, 0, D * 2, 0x00020000);
  X.rout = __builtin_amdgcn_make_buffer_rsrc((void*)out, 0, S * D * 2, 0x00020000);
  X.tag = 0;
  X.lane = TIDX & 63;
  X.w = __builtin_amdgcn_readfirstlane(TIDX >> 6);
  const int xc = BIDX & 7, i = BIDX >> 3;
  X.g = (xc % RA) * (32 / RA) + i / RA;
  X.j = (xc / RA) * RA + i % RA;
  STAMP(X, 0);
  {
    const u64 e = __hip_atomic_load((gu64*)X.ws, RLX_AGENT);
    if (TIDX < 16) *(LAS unsigned*)(X.L + L_CNT + TIDX * 4) = 0u;
    BAR();
    unsigned bw = 0;
    if (TIDX < 288) bw = *(const GAS unsigned*)(X.b1 + X.j * 576 + TIDX * 2);
    if (X.w >= 3) {
      __builtin_amdgcn_s_setprio(3);
      if (X.w == 3) x0_rows(X);
      else qo_tile(X);
      DRAIN();
    } else {
      {
        u64 pc;
        asm volatile("s_getpc_b64 s[20:21]\n\ts_add_u32 s20, s20, ilk_main@rel32@lo+4\n\ts_addc_u32 s21, s21, ilk_main@rel32@hi+12\n\ts_mov_b64 %0, s[20:21]"
                     : "=s"(pc) : : "scc", "s20", "s21");
        const v4u t = *((const GAS v4u*)pc + (cmin(i * 3 + X.w, CODE_KB - 1) * 64 + X.lane));
        asm volatile("" ::"v"(t));
      }
      __builtin_amdgcn_s_sleep(PSLEEP);
      const int t0 = rt_task(X.g, X.w), t1 = rt_task(X.g, X.w + 3);
      v4u r0[8], r1[8];
      rt_load(X, t0, r0);
      rt_load(X, t1 < 0 ? t0 : t1, r1);
      rt_turn(X, t0, X.w, r0);
      if (t1 >= 0) rt_turn(X, t1, X.w + 3, r1);
    }
    if (TIDX < 288) *(LAS f32x2*)(X.L + L_CB + TIDX * 8) = f32x2{bflo(bw), bfhi(bw)};
    const unsigned elo = __builtin_amdgcn_readfirstlane((unsigned)e), ehi = __builtin_amdgcn_readfirstlane((unsigned)(e >> 32));
    X.tag = (((u64)ehi << 32) | elo) + 0x9E3779B97F4A7C15ull;
    STAMP(X, 30);
    if (X.w >= 3) {
      unsigned old = 0;
      if (X.lane == 0) old = __hip_atomic_fetch_add((LAS unsigned*)(X.L + L_CNT + 12), 1u, RLX_WG);
      if (__builtin_amdgcn_readfirstlane(old) == 4u) {
        if (X.lane < 2) signal(X.ws, X.lane ? flag_off(F_Q, 0, X.j) + X.g * 8 : flag_off(F_D, 0, X.g) + X.j * 8, X.tag);
        poll(X.ws, X.lane >= 32 ? flag_off(F_Q, 0, X.j) + (X.lane & 31) * 8
                                : (X.lane >= 16 && X.g > 0) ? flag_off(F_D, 0, X.g - 1) + (6 + (X.lane & 1)) * 8 : flag_off(F_D, 0, X.g) + (X.lane & 7) * 8, X.tag);
      }
      __builtin_amdgcn_s_setprio(0);
    }
    BAR();
    STAMP(X, 31);
  }
  if (X.w < 4) body<0>(X);
  else body<1>(X);
}
#if defined(__HIP_DEVICE_COMPILE__)
#pragma clang attribute pop
#endif

extern "C" void kernel_launch(void* const* d_in, const int* in_sizes, int n_in, void* d_out, int out_size, void* d_ws, size_t ws_size,
                              hipStream_t stream) {
  (void)hipFuncSetAttribute((const void*)ilk_main, hipFuncAttributeMaxDynamicSharedMemorySize, LDS_BYTES);
  ilk_main<<<256, NTHR, LDS_BYTES, stream>>>((unsigned char*)d_ws, (const u16*)d_in[0], (const u16*)d_in[1], (const u16*)d_in[2], (const u16*)d_in[4],
                                           (const u16*)d_in[6], (const u16*)d_in[5], (const u16*)d_in[7], (u16*)d_out);
}
```

```cpp
#include <hip/hip_runtime.h>
#include <hip/hip_bf16.h>

#if defined(__HIP_DEVICE_COMPILE__)
#pragma clang attribute push(__attribute__((target("no-packed-fp32-ops"))), apply_to = function)
#endif
namespace {
typedef unsigned short u16;
typedef unsigned v4u __attribute__((ext_vector_type(4)));
typedef unsigned v2u __attribute__((ext_vector_type(2)));
typedef float f32x4 __attribute__((ext_vector_type(4)));
typedef float f32x2 __attribute__((ext_vector_type(2)));
typedef __bf16 bf16x2_t __attribute__((ext_vector_type(2)));
typedef __bf16 bf16x8_t __attribute__((ext_vector_type(8)));
#define GAS __attribute__((address_space(1)))
#define LAS __attribute__((address_space(3)))
typedef __amdgpu_buffer_rsrc_t rsrc_t;

constexpr int S = 2048, D = 576, F = 4608;
constexpr int KS = 18;
constexpr int NTHR = 512;
constexpr int XBUF = 128 * KS * 1024;
constexpr int OFF_CTRL = 0;
constexpr int OFF_CS = 4 * 65536;
constexpr int OFF_RS = OFF_CS + 3 * 65536;
constexpr int OFF_WQ = OFF_RS + 128 * 2048;
constexpr int OFF_WO = OFF_WQ + 8 * 14 * KS * 1024;
constexpr int OFF_WC = OFF_WO + 8 * 90 * 1024;
constexpr int OFF_XN = OFF_WC + 8 * 1296 * 1024;
constexpr int OFF_O = OFF_XN + 2 * XBUF;
constexpr int OFF_X1 = OFF_O + XBUF;
constexpr int OFF_X2 = OFF_X1 + XBUF;
constexpr int OFF_P = OFF_X2 + XBUF;
constexpr int WS_BYTES = OFF_P + 8 * XBUF;

__device__ __forceinline__ f32x4 mfma16(v4u a, v4u b, f32x4 c) {
  return __builtin_amdgcn_mfma_f32_16x16x32_bf16(__builtin_bit_cast(bf16x8_t, a), __builtin_bit_cast(bf16x8_t, b), c, 0, 0, 0);
}
__device__ __forceinline__ void m16(f32x4& c, v4u a, v4u b) { asm volatile("v_mfma_f32_16x16x32_bf16 %0, %1, %2, %0" : "+v"(c) : "v"(a), "v"(b)); }
__device__ __forceinline__ void m16z(f32x4& c, v4u a, v4u b) { asm volatile("v_mfma_f32_16x16x32_bf16 %0, %1, %2, 0" : "=&v"(c) : "v"(a), "v"(b)); }
__device__ __forceinline__ void m16c(f32x4& d, v4u a, v4u b, f32x4 c) { asm volatile("v_mfma_f32_16x16x32_bf16 %0, %1, %2, %3" : "=&v"(d) : "v"(a), "v"(b), "v"(c)); }
#define M16(c, a, b) do { if (u == 0) m16z(c, a, b); else m16(c, a, b); } while (0)
__device__ __forceinline__ unsigned pk2(float lo, float hi) {
  f32x2 v = {lo, hi};
  return __builtin_bit_cast(unsigned, __builtin_convertvector(v, bf16x2_t));
}
__device__ __forceinline__ float asf(unsigned u) { return __builtin_bit_cast(float, u); }
__device__ __forceinline__ unsigned asu(float f) { return __builtin_bit_cast(unsigned, f); }
__device__ __forceinline__ float bflo(unsigned u) { return asf(u << 16); }
__device__ __forceinline__ float bfhi(unsigned u) { return asf(u & 0xffff0000u); }
__device__ __forceinline__ v2u pk4(f32x4 v) { return v2u{pk2(v.x, v.y), pk2(v.z, v.w)}; }
__device__ __forceinline__ float dot2(unsigned a, unsigned b, float c) {
  return __builtin_amdgcn_fdot2_f32_bf16(__builtin_bit_cast(bf16x2_t, a), __builtin_bit_cast(bf16x2_t, b), c, false);
}
constexpr unsigned ONES = 0x3f803f80u;

template <int AUX> __device__ __forceinline__ v4u ld128(rsrc_t rs, int voff, int soff) { return __builtin_amdgcn_raw_buffer_load_b128(rs, voff, soff, AUX); }
template <int AUX> __device__ __forceinline__ v2u ld64(rsrc_t rs, int voff, int soff) { return __builtin_amdgcn_raw_buffer_load_b64(rs, voff, soff, AUX); }
template <int AUX> __device__ __forceinline__ void st128(v4u v, rsrc_t rs, int voff, int soff) { __builtin_amdgcn_raw_buffer_store_b128(v, rs, voff + soff, 0, AUX); }
template <int AUX> __device__ __forceinline__ void st64(v2u v, rsrc_t rs, int voff, int soff) { __builtin_amdgcn_raw_buffer_store_b64(v, rs, voff, soff, AUX); }
template <int AUX> __device__ __forceinline__ void st32(unsigned v, rsrc_t rs, int voff, int soff) { __builtin_amdgcn_raw_buffer_store_b32(v, rs, voff, soff, AUX); }
#define SB() __builtin_amdgcn_sched_barrier(0)
#define MFMA_DONE() do { SB(); asm volatile("s_nop 7\n\ts_nop 1" ::: "memory"); SB(); } while (0)
#define TIDX ((int)__builtin_amdgcn_workitem_id_x())
#define BIDX ((int)__builtin_amdgcn_workgroup_id_x())
#define SYNC() do { __builtin_amdgcn_fence(__ATOMIC_RELEASE, "workgroup"); __builtin_amdgcn_s_barrier(); __builtin_amdgcn_fence(__ATOMIC_ACQUIRE, "workgroup"); } while (0)
__device__ __forceinline__ float fmx(float a, float b) { return __builtin_fmaxf(a, b); }
__device__ __forceinline__ float rsq(float a) { return __builtin_amdgcn_rsqf(a); }

__device__ __forceinline__ float sum32(float v) {
  const auto b = __builtin_amdgcn_permlane32_swap(asu(v), asu(v), false, false);
  const unsigned b0 = b[0], b1 = b[1];
  return asf(b0) + asf(b1);
}
__device__ __forceinline__ float sum1632(float v) {
  const auto a = __builtin_amdgcn_permlane16_swap(asu(v), asu(v), false, false);
  const unsigned a0 = a[0], a1 = a[1];
  return sum32(asf(a0) + asf(a1));
}
__device__ __forceinline__ float max1632(float v) {
  const auto a = __builtin_amdgcn_permlane16_swap(asu(v), asu(v), false, false);
  const unsigned a0 = a[0], a1 = a[1];
  v = fmx(asf(a0), asf(a1));
  const auto b = __builtin_amdgcn_permlane32_swap(asu(v), asu(v), false, false);
  const unsigned b0 = b[0], b1 = b[1];
  return fmx(asf(b0), asf(b1));
}

template <typename OP> __device__ __forceinline__ void red1632x2(float& a, float& b, OP op) {
  const auto s = __builtin_amdgcn_permlane16_swap(asu(a), asu(b), false, false);
  const unsigned s0 = s[0], s1 = s[1];
  const float t = op(asf(s0), asf(s1));
  const auto u = __builtin_amdgcn_permlane32_swap(asu(t), asu(t), false, false);
  const unsigned u0 = u[0], u1 = u[1];
  const float v = op(asf(u0), asf(u1));
  const auto x = __builtin_amdgcn_permlane16_swap(asu(v), asu(v), false, false);
  const unsigned x0 = x[0], x1 = x[1];
  a = asf(x0);
  b = asf(x1);
}
__device__ __forceinline__ void max1632x2(float& a, float& b) { red1632x2(a, b, [](float x, float y) { return fmx(x, y); }); }
__device__ __forceinline__ void sum1632x2(float& a, float& b) { red1632x2(a, b, [](float x, float y) { return x + y; }); }

__device__ __forceinline__ void tr8x8(const v4u (&r)[8], v4u (&o)[8]) {
#pragma unroll
  for (int c = 0; c < 4; ++c) {
#pragma unroll
    for (int p = 0; p < 4; ++p) {
      o[2 * c][p] = __builtin_amdgcn_perm(r[2 * p + 1][c], r[2 * p][c], 0x05040100u);
      o[2 * c + 1][p] = __builtin_amdgcn_perm(r[2 * p + 1][c], r[2 * p][c], 0x07060302u);
    }
  }
}
#define RA 2
#define RING_LO 12
#define RING_HI 32
#define QRING_LO 14
#define QRING 4
#define HPRE 12
#define PSLEEP 1
#define BPF 6
#define RAWAUX 2
#define CODE_KB 61
#define RTU0 3
#define RTS 2
#define LOPRE 3
#define LOLATE 0
#define CLEAD 12
#define XLO 6
#define CPF 5
#define OUTAUX 0
#define RTSLEEP 4
#define WOK 15
#define WARM 0
#define APRIO 3
#define PUBK 10
#define WOFIRST 0
#define SIGK 12
template <int HI> constexpr int ring() { return HI ? RING_HI : RING_LO; }
constexpr int RQ = QRING;
constexpr int CPRE = CLEAD;
constexpr int cmin(int a, int b) { return a < b ? a : b; }
template <int N> struct IC { static constexpr int value = N; };
constexpr int SC1 = 16;
constexpr int A_XT = 0;
constexpr int A_Q = 5 * KS * 1024;
constexpr int A_K = A_Q + 16 * 512;
constexpr int A_V = A_K + 20 * 512;
constexpr int A_EQK = A_V + 20 * 512;
constexpr int A_EV = A_EQK + 5 * 512;
constexpr int B_W = 0, B_RED = 124 * 1024;
constexpr int C_X = 0, C_OWN = 0, C_H = 4 * KS * 1024, C_ST = 8 * KS * 1024;
constexpr int L_CB = C_ST + 1024;
constexpr int D_ST = L_CB + 576 * 8;
constexpr int L_CNT = D_ST + 512;
constexpr int L_STAMP = L_CNT + 128;
#define STAMPCLK() __builtin_amdgcn_s_memrealtime()
constexpr int LDS_BYTES = L_STAMP;
#define STAMP(X_, slot) do {} while (0)
enum { F_A = 0, F_B = 1, F_C1 = 2, F_C2 = 3, F_C3 = 4, F_D = 5, F_Q = 6, F_W = 7 };
typedef unsigned long long u64;
typedef GAS u64 gu64;
__device__ __forceinline__ int flag_off(int kind, int layer, int g) { return 4096 + ((kind * 2 + layer) * 32 + g) * 256; }
#define RLX_AGENT __ATOMIC_RELAXED, __HIP_MEMORY_SCOPE_AGENT
#define RLX_WG __ATOMIC_RELAXED, __HIP_MEMORY_SCOPE_WORKGROUP
#define BAR() do { SB(); asm volatile("s_waitcnt lgkmcnt(0)\n\ts_barrier" ::: "memory"); SB(); } while (0)
#define DRAIN() do { SB(); asm volatile("s_waitcnt vmcnt(0)" ::: "memory"); SB(); } while (0)
__device__ __forceinline__ void poll(unsigned char* ws, int off, u64 tag) {
  gu64* p = (gu64*)(ws + off);
#pragma unroll 1
  for (unsigned spins = 0; spins < (1u << 22); ++spins) {
    const u64 v = __hip_atomic_load(p, RLX_AGENT);
    if (__builtin_amdgcn_ballot_w64(v != tag) == 0) break;
    __builtin_amdgcn_s_sleep(1);
  }
  __builtin_amdgcn_fence(__ATOMIC_ACQUIRE, "wavefront");
  asm volatile("" ::: "memory");
}
__device__ __forceinline__ void signal(unsigned char* ws, int off, u64 tag) { __hip_atomic_store((gu64*)(ws + off), tag, RLX_AGENT); }
__device__ __forceinline__ void lds_wait(LAS unsigned* p, unsigned want) {
#pragma unroll 1
  for (unsigned spins = 0; spins < (1u << 24); ++spins) {
    const unsigned v = __builtin_amdgcn_readfirstlane(__hip_atomic_load(p, RLX_WG));
    if (v >= want) break;
    __builtin_amdgcn_s_sleep(1);
  }
  asm volatile("" ::: "memory");
  SB();
}

#define LDS_ARRIVE(p) do { SB(); asm volatile("s_waitcnt lgkmcnt(0)" ::: "memory"); if (lane == 0) __hip_atomic_fetch_add((p), 1u, RLX_WG); SB(); } while (0)

struct Ctx {
  unsigned char* ws;
  const u16* x;
  const u16* wqkv;
  const u16* wo;
  const u16* w1;
  const u16* w2;
  const u16* b1;
  LAS unsigned char* L;
  rsrc_t rs, rb2, rout;
  u64 tag;
  int g, j, w, lane;
};
__device__ __forceinline__ Ctx fresh(const Ctx& X) {
  Ctx Y = X;
  asm volatile("" : "+v"(Y.lane));
  asm volatile("" : "+s"(Y.g), "+s"(Y.j), "+s"(Y.w));
  return Y;
}

__device__ __forceinline__ int rt_task(int g, int q) { return q < 5 ? g * 5 + q : (g < 2 ? 160 + g : -1); }
__device__ __forceinline__ void rt_load(const Ctx& X, int task, v4u (&r)[8]) {
  const bool is1 = task < 81;
  const int tt = is1 ? task : task - 81, chunk = tt / 9, strip = tt - 9 * chunk;
  const u16* src = is1 ? X.w1 + (size_t)chunk * 64 * F + X.j * 576 + strip * 64 : X.w2 + (size_t)(X.j * 576 + chunk * 64) * D + strip * 64;
  const int ld = is1 ? F : D;
  const rsrc_t rw = __builtin_amdgcn_make_buffer_rsrc((void*)src, 0, (63 * ld + 64) * 2, 0x00020000);
  const int vo = ((X.lane >> 3) * 8 * ld + (X.lane & 7) * 8) * 2;
#pragma unroll
  for (int jj = 0; jj < 8; ++jj) r[jj] = ld128<RAWAUX>(rw, vo, jj * ld * 2);
  SB();
}
__device__ __forceinline__ void rt_turn(const Ctx& X, int task, int q, const v4u (&r)[8]) {
  const int lane = X.lane, kg = lane >> 3, nb = lane & 7;
  LAS unsigned char* sm = X.L + A_Q + q * 8192;
  v4u o[8];
  tr8x8(r, o);
  const int base = (((kg >> 2) * 4 + (nb >> 1)) * 64 + (kg & 3) * 16 + (nb & 1) * 8) * 16;
#pragma unroll
  for (int c = 0; c < 8; ++c) *(LAS v4u*)(sm + base + ((c ^ nb) * 16)) = o[c];
  SB();
}
__device__ __forceinline__ v4u rt_get(const Ctx& X, int q, int f) {
  const int lane = X.lane;
  return *(LAS v4u*)(X.L + A_Q + q * 8192 + (f * 64 + (lane & ~7) + ((lane & 7) ^ ((f & 3) * 2 + ((lane >> 3) & 1)))) * 16);
}
__device__ __forceinline__ void rt_put(const Ctx& X, int task, int f, v4u v) {
  const bool is1 = task < 81;
  const int tt = is1 ? task : task - 81, chunk = (tt * 57) >> 9, strip = tt - 9 * chunk;
  const int T = strip * 4 + (f & 3), lo = strip < 5;
  const int ks = chunk * 2 + (f >> 2);
  int o;
  if (lo) o = ((T * 13) >> 6) * 175 + T + ((is1 ? 0 : KS) + ks) * 5;
  else o = strip * 144 + (f & 3) + ((is1 ? 0 : KS) + ks) * 4;
  const int fr = X.j * 1296 + o;
  st128<SC1>(v, X.rs, X.lane * 16, OFF_WC + fr * 1024);
}

__device__ __forceinline__ void qo_tile(const Ctx& X) {
  constexpr int NQ = 2;
  const rsrc_t rq_ = __builtin_amdgcn_make_buffer_rsrc((void*)X.wqkv, 0, D * 3 * D * 2, 0x00020000);
  const int j = X.j;
  unsigned e[NQ][8];
  int dst[NQ];
  bool zero[NQ];
#pragma unroll
  for (int i = 0; i < NQ; ++i) {
    const int q = i * 256 + (X.w - 4) * 64 + X.lane, P = X.g * 504 + (q < 504 ? q : 503);
    const int kg = ((P >> 4) * 4682) >> 16, blk = (P >> 4) - kg * 14, u = kg >> 2, gq = kg & 3, m = P & 15;
    const int ty = blk < 12 ? blk >> 2 : blk == 12 ? (m >> 1) & 1 : 2;
    const int hd = blk < 12 ? blk & 3 : blk == 12 ? m >> 2 : (m >> 1) & 3;
    const int col = ty * D + (4 * j + hd) * 18 + (blk < 12 ? m : 16 + (m & 1));
    zero[i] = blk == 13 && m >= 8;
    dst[i] = OFF_WQ + (((j * 14 + blk) * KS + u) * 64 + gq * 16 + m) * 16;
    const int vo = ((u * 32 + gq * 8) * 3 * D + col) * 2;
#pragma unroll
    for (int jj = 0; jj < 8; ++jj) e[i][jj] = __builtin_amdgcn_raw_buffer_load_b16(rq_, vo, jj * 3 * D * 2, 0);
  }
  const rsrc_t ro_ = __builtin_amdgcn_make_buffer_rsrc((void*)X.wo, 0, D * D * 2, 0x00020000);
  const int t = (X.w - 4) * 64 + X.lane;
  const int P = X.g * 180 + (t < 180 ? t : 179);
  const int kg = ((P >> 4) * 13108) >> 16, tl = (P >> 4) - kg * 5, u = kg >> 2, gq = kg & 3, m = P & 15;
  const bool zo = tl * 16 + m >= 72;
  const int col = j * 72 + (zo ? 0 : tl * 16 + m);
  const int r0 = u < 16 ? 36 * u + 4 * gq : 72 * (4 * (u - 16) + gq) + 16;
  unsigned eo[8];
#pragma unroll
  for (int jj = 0; jj < 8; ++jj) {
    const int row = r0 + (u < 16 ? (jj >> 2) * 18 + (jj & 3) : (jj >> 1) * 18 + (jj & 1));
    eo[jj] = __builtin_amdgcn_raw_buffer_load_b16(ro_, (row * D + col) * 2, 0, 0);
  }
  SB();
#pragma unroll
  for (int i = 0; i < NQ; ++i) {
    v4u o;
#pragma unroll
    for (int p = 0; p < 4; ++p) o[p] = zero[i] ? 0u : (e[i][2 * p] | (e[i][2 * p + 1] << 16));
    st128<SC1>(o, X.rs, dst[i], 0);
  }
  v4u o;
#pragma unroll
  for (int p = 0; p < 4; ++p) o[p] = zo ? 0u : (eo[2 * p] | (eo[2 * p + 1] << 16));
  if (t < 180) st128<SC1>(o, X.rs, OFF_WO + ((j * 90 + u * 5 + tl) * 64 + gq * 16 + m) * 16, 0);
  SB();
}

__device__ __forceinline__ void x0_rows(const Ctx& X) {
  const rsrc_t rx = __builtin_amdgcn_make_buffer_rsrc((void*)X.x, 0, S * D * 2, 0x00020000);
  const int tok = X.lane & 7, q8 = X.lane >> 3;
  v4u v[9];
#pragma unroll
  for (int i = 0; i < 9; ++i) v[i] = ld128<0>(rx, (tok * D + q8 * 8 + i * 64) * 2, (X.g * 64 + X.j * 8) * D * 2);
  SB();
  float s = 0.f, q = 0.f;
#pragma unroll
  for (int i = 0; i < 9; ++i)
#pragma unroll
    for (int p = 0; p < 4; ++p) { s = dot2(v[i][p], ONES, s); q = dot2(v[i][p], v[i][p], q); }
  s += asf(__builtin_amdgcn_mov_dpp(asu(s), 0x128, 0xF, 0xF, true));
  q += asf(__builtin_amdgcn_mov_dpp(asu(q), 0x128, 0xF, 0xF, true));
  sum1632x2(s, q);
  const float mean = s * (1.f / D);
  const float var = fmx(q * (1.f / D) - mean * mean, 0.f);
  const float rstd = rsq(var + 1e-5f), nm = -mean * rstd;
#pragma unroll
  for (int i = 0; i < 9; ++i) {
    v4u o;
#pragma unroll
    for (int p = 0; p < 4; ++p) o[p] = pk2(__builtin_fmaf(bflo(v[i][p]), rstd, nm), __builtin_fmaf(bfhi(v[i][p]), rstd, nm));
    st128<SC1>(o, X.rs, (q8 * 16 + tok) * 16 + i * 2048, OFF_XN + (4 * X.g + (X.j >> 1)) * KS * 1024 + (X.j & 1) * 128);
  }
  SB();
}

__device__ __forceinline__ v4u wq_load(const Ctx& X, int b, int u) {
  const int blk = b < 3 ? b * 4 + X.w : 12 + (X.w >> 1);
  return ld128<SC1>(X.rs, X.lane * 16 + (u & 3) * 1024, OFF_WQ + (X.j * 14 + blk) * KS * 1024 + (u >> 2) * 4096);
}
__device__ __forceinline__ void wq_prefetch(const Ctx& X, v4u (&wq)[RQ][4]) {
#pragma unroll
  for (int u = 0; u < RQ - 1; ++u)
#pragma unroll
    for (int b = 0; b < 4; ++b) wq[u][b] = wq_load(X, b, u);
  SB();
}
__device__ __forceinline__ v4u xn_load(const Ctx& X, int layer, int f) {
  const int u = (f * 205) >> 10, rb5 = f - 5 * u;
  return ld128<SC1>(X.rs, X.lane * 16, OFF_XN + layer * XBUF + ((4 * X.g - 1 + (X.g == 0 && rb5 == 0 ? 1 : rb5)) * KS + u) * 1024);
}

template <int LO, int HIX, int R>
__device__ __forceinline__ void ring_fill(const Ctx& X, int run, v4u (&a)[R]) {
#pragma unroll
  for (int i = LO; i < HIX; ++i) a[i] = ld128<SC1>(X.rs, X.lane * 16 + (i & 3) * 1024, run + (i >> 2) * 4096);
  SB();
}
template <int LO, int HIX, int R>
__device__ __forceinline__ void ring_zero(v4u (&a)[R]) {
#pragma unroll
  for (int i = LO; i < HIX; ++i) a[i] = v4u{0, 0, 0, 0};
}

template <int HI>
__device__ __forceinline__ void stage_a(const Ctx& X, int layer, v4u (&wq)[RQ][4]) {
  LAS unsigned char* L = X.L;
  const rsrc_t rs = X.rs;
  const int g = X.g, j = X.j, w = X.w, lane = X.lane;
  LAS unsigned* cx = (LAS unsigned*)(L + L_CNT + 64);
  const unsigned four = 4 * (layer + 1);
  v4u wt[23];
  if (HI) {
    const int q = w - 4;
    __builtin_amdgcn_s_setprio(2);
    v4u t[18];
#pragma unroll
    for (int i = 0; i < 18; ++i) t[i] = xn_load(X, layer, i < 8 ? cmin(20 + 4 * i + q, 49) : 50 + 4 * (i - 8) + q);
    SB();
#pragma unroll
    for (int i = 0; i < 18; ++i) {
      *(LAS v4u*)(L + A_XT + (i < 8 ? cmin(20 + 4 * i + q, 49) : 50 + 4 * (i - 8) + q) * 1024 + lane * 16) = t[i];
      if (i == 7) LDS_ARRIVE(cx + 1);
    }
    LDS_ARRIVE(cx + 2);
    __builtin_amdgcn_s_setprio(0);
    STAMP(X, layer * 32 + 1);
    if (layer == 0) {
#pragma unroll 1
      for (int k = 0; k < 12; ++k) {
        const int task = rt_task(g, k >> 1);
        if (task >= 0) rt_put(X, task, (k & 1) * 4 + q, rt_get(X, k >> 1, (k & 1) * 4 + q));
        __builtin_amdgcn_s_sleep(RTSLEEP);
      }
      LDS_ARRIVE((LAS unsigned*)(L + L_CNT + 52));
    }
    if (WOK >= 0) lds_wait(cx + 3, four);
#pragma unroll
    for (int i = 0; i < 23; ++i) wt[i] = ld128<SC1>(rs, lane * 16, OFF_WO + (j * 90 + (i < 22 ? i * 4 + q : 88 + (q & 1))) * 1024);
    STAMP(X, layer * 32 + 2);
  } else {
    __builtin_amdgcn_s_setprio(3);
    {
      v4u t[5];
#pragma unroll
      for (int i = 0; i < 5; ++i) t[i] = xn_load(X, layer, 4 * i + w);
      SB();
      if (layer == 0) wq_prefetch(X, wq);
#pragma unroll
      for (int i = 0; i < 5; ++i) *(LAS v4u*)(L + A_XT + (4 * i + w) * 1024 + lane * 16) = t[i];
      LDS_ARRIVE(cx);
    }
    __builtin_amdgcn_s_setprio(APRIO);
    STAMP(X, layer * 32 + 1);
    lds_wait(cx, four);
    STAMP(X, layer * 32 + 2);
    f32x4 qc[5], kc[5], vc[5], ec[3];
    v4u xf[5];
    const int r1 = (w & 1) ? 3 : 1, r3 = (w & 1) ? 1 : 3;
    const int eoff = w < 2 ? A_EQK + lane * 8 : A_EV + (((lane & 15) >> 2) * 16 + 4 * (lane >> 4)) * 8 + (lane & 3) * 2, estr = w < 2 ? 1 : 4;
    LAS unsigned char* x0 = L + A_XT + lane * 16;
    LAS unsigned char* x1 = x0 + r1 * 1024;
    LAS unsigned char* x3 = x0 + r3 * 1024;
    LAS unsigned char* y0 = x0 + 12 * 5120;
    LAS unsigned char* y1 = x1 + 12 * 5120;
    LAS unsigned char* y3 = x3 + 12 * 5120;
#define XF(r, u) (*(LAS v4u*)(((u) < 12 ? ((r) == 0 ? x0 : (r) < 3 ? x1 : x3) : ((r) == 0 ? y0 : (r) < 3 ? y1 : y3)) + ((u) < 12 ? (u) : (u) - 12) * 5120 + ((r) == 2 || (r) == 4 ? 1024 : 0)))
#pragma unroll
    for (int r = 0; r < 5; ++r) xf[r] = XF(r, 0);
    SB();
    auto put = [&](int r) {
      const int rb5 = r == 0 ? 0 : r < 3 ? r1 + r - 1 : r3 + r - 3;
      if (r > 0) *(LAS v2u*)(L + A_Q + (w * 4 + rb5 - 1) * 512 + lane * 8) = pk4(qc[r]);
      *(LAS v2u*)(L + A_K + (w * 5 + rb5) * 512 + lane * 8) = pk4(kc[r]);
      *(LAS v2u*)(L + A_V + (w * 5 + rb5) * 512 + lane * 8) = pk4(vc[r]);
      if (r < 3) {
        const v2u v = pk4(ec[r]);
        LAS u16* p = (LAS u16*)(L + eoff + rb5 * 512);
        p[0] = (u16)v.x;
        p[estr] = (u16)(v.x >> 16);
        p[2 * estr] = (u16)v.y;
        p[3 * estr] = (u16)(v.y >> 16);
      }
      SB();
    };
#pragma unroll
    for (int u = 0; u < KS; ++u) {
      if (u == 3) lds_wait(cx + 1, four);
      if (u == 9) lds_wait(cx + 2, four);
      if (u == WOK) { if (lane == 0) __hip_atomic_fetch_add(cx + 3, 1u, RLX_WG); SB(); }
      if (u == KS - 1) lds_wait((LAS unsigned*)(L + L_CNT + 52), 4);
#pragma unroll
      for (int r = 0; r < 5; ++r) {
        M16(kc[r], wq[u % RQ][1], xf[r]);
        if (r < 4 && u + RQ - 1 < KS) { wq[(u + RQ - 1) % RQ][r] = wq_load(X, r, u + RQ - 1); SB(); }
        M16(vc[r], xf[r], wq[u % RQ][2]);
        if (r > 0) M16(qc[r], wq[u % RQ][0], xf[r]);
        if (r < 3) M16(ec[r], wq[u % RQ][3], xf[r]);
        if (u + 1 < KS) xf[r] = XF(r, u + 1);
        SB();
        if (u == KS - 1 && r >= 2) put(r - 2);
      }
    }
#undef XF
    put(3);
    MFMA_DONE();
    put(4);
    __builtin_amdgcn_s_setprio(0);
  }
  STAMP(X, layer * 32 + 3);
  BAR();
  STAMP(X, layer * 32 + 4);
  {
    const int c = lane & 15, gq = lane >> 4, rb = w & 3;
    typedef short s16x4 __attribute__((ext_vector_type(4)));
    auto mk = [](v2u a, v2u b, f32x4 cc) {
      return __builtin_amdgcn_mfma_f32_16x16x16bf16_1k(__builtin_bit_cast(s16x4, a), __builtin_bit_cast(s16x4, b), cc, 0, 0, 0);
    };
    LAS unsigned char* lp = L + rb * 512 + lane * 8;
    const v2u e0 = *(LAS v2u*)(lp + A_EQK), e1 = *(LAS v2u*)(lp + A_EQK + 512);
    v2u qm[2], k0[2], k1[2], v0[2], v1[2];
#pragma unroll
    for (int hh = 0; hh < 2; ++hh) {
      const int h = 2 * HI + hh;
      qm[hh] = *(LAS v2u*)(lp + A_Q + h * 4 * 512);
      k0[hh] = *(LAS v2u*)(lp + A_K + h * 5 * 512);
      k1[hh] = *(LAS v2u*)(lp + A_K + (h * 5 + 1) * 512);
    }
#pragma unroll
    for (int hh = 0; hh < 2; ++hh) {
      const int h = 2 * HI + hh;
      v0[hh] = *(LAS v2u*)(lp + A_V + h * 5 * 512);
      v1[hh] = *(LAS v2u*)(lp + A_V + (h * 5 + 1) * 512);
    }
    const v2u x0 = *(LAS v2u*)(lp + A_EV), x1 = *(LAS v2u*)(lp + A_EV + 512);
    SB();
    const bool first = (g == 0 && rb == 0);
    const bool par = c & 1;
    bool own[2], ok[2];
#pragma unroll
    for (int e = 0; e < 2; ++e) {
      own[e] = 4 * gq + 2 * e <= (c & 14);
      ok[e] = own[e] || !first;
    }
    const f32x4 z = {0, 0, 0, 0};
    f32x4 s0[2], s1[2];
#pragma unroll
    for (int hh = 0; hh < 2; ++hh) {
      s0[hh] = mk(k0[hh], qm[hh], z);
      s1[hh] = mk(k1[hh], qm[hh], z);
    }
#pragma unroll
    for (int hh = 0; hh < 2; ++hh) {
      const v2u qe = {0u, gq == 2 * HI + hh ? e1.x : 0u};
      s0[hh] = mk(e0, qe, s0[hh]);
      s1[hh] = mk(e1, qe, s1[hh]);
    }
    float sv[2][2], m[2], p[2][2], inv[2];
#pragma unroll
    for (int hh = 0; hh < 2; ++hh) {
      const float a0 = s0[hh].x, a1 = s0[hh].y, a2 = s0[hh].z, a3 = s0[hh].w, b0 = s1[hh].x, b1 = s1[hh].y, b2 = s1[hh].z, b3 = s1[hh].w;
      const float l0 = own[0] ? b0 : a0, h0 = own[0] ? b1 : a1, l1 = own[1] ? b2 : a2, h1 = own[1] ? b3 : a3;
      sv[hh][0] = ok[0] ? (par ? h0 : l0) : -3.0e38f;
      sv[hh][1] = ok[1] ? (par ? h1 : l1) : -3.0e38f;
      m[hh] = fmx(sv[hh][0], sv[hh][1]);
    }
    max1632x2(m[0], m[1]);
    constexpr float SC = 0.23570226039551584f * 1.4426950408889634f;
#pragma unroll
    for (int hh = 0; hh < 2; ++hh) {
      p[hh][0] = __builtin_amdgcn_exp2f((sv[hh][0] - m[hh]) * SC);
      p[hh][1] = __builtin_amdgcn_exp2f((sv[hh][1] - m[hh]) * SC);
      inv[hh] = p[hh][0] + p[hh][1];
    }
    sum1632x2(inv[0], inv[1]);
    f32x4 om[2], ox[2];
#pragma unroll
    for (int hh = 0; hh < 2; ++hh) {
      const float r = __builtin_amdgcn_rcpf(inv[hh]);
      const float q0 = p[hh][0] * r, q1 = p[hh][1] * r;
      const unsigned d0 = pk2(par ? 0.f : q0, par ? q0 : 0.f), d1 = pk2(par ? 0.f : q1, par ? q1 : 0.f);
      const v2u pp = {own[0] ? 0u : d0, own[1] ? 0u : d1}, po = {own[0] ? d0 : 0u, own[1] ? d1 : 0u};
      om[hh] = mk(v1[hh], po, mk(v0[hh], pp, z));
      ox[hh] = mk(x1, po, mk(x0, pp, z));
    }
    const int ob = OFF_O + (4 * g + rb) * KS * 1024;
    const v2u a = pk4(om[0]), b = pk4(om[1]);
    st128<SC1>(v4u{a.x, a.y, b.x, b.y}, rs, lane * 16, ob + (2 * j + HI) * 1024);
    if (gq == HI) st64<SC1>(v2u{pk2(ox[0].x, ox[0].y), pk2(ox[1].z, ox[1].w)}, rs, c * 8, ob + 16 * 1024 + (2 * j + HI) * 128);
  }
  STAMP(X, layer * 32 + 5);
  if (HI) {
#pragma unroll
    for (int i = 0; i < 23; ++i) *(LAS v4u*)(L + B_W + (i < 22 ? i * 4 + w - 4 : 88 + (w & 1)) * 1024 + lane * 16) = wt[i];
  }
  DRAIN();
  BAR();
  if (HI && w == 7 && lane < 2 - layer) signal(X.ws, lane ? flag_off(F_W, 0, j) + g * 8 : flag_off(F_A, layer, g) + j * 8, X.tag);
  STAMP(X, layer * 32 + 6);
}

template <int HI>
__device__ __forceinline__ void stage_b(const Ctx& X, int layer) {
  LAS unsigned char* L = X.L;
  const rsrc_t rs = X.rs;
  const int g = X.g, j = X.j, w = X.w, lane = X.lane;
  constexpr int kh = HI, NK = HI ? 2 : 3;
  const int rb = w & 3, c = lane & 15, gq = lane >> 4;
  const bool poller = HI && w == 7;
  const int xb = OFF_X2 + (4 * g + rb) * KS * 1024;
  const rsrc_t rx = __builtin_amdgcn_make_buffer_rsrc((void*)X.x, 0, S * D * 2, 0x00020000);
  int xo[NK];
  v2u res[NK];
#pragma unroll
  for (int i = 0; i < NK; ++i) {
    const int t = kh * 3 + i, d0 = 72 * j + 16 * t + 4 * gq;
    xo[i] = (d0 >> 5) * 1024 + (((d0 >> 3) & 3) * 16 + c) * 16 + ((d0 >> 2) & 1) * 8;
    res[i] = v2u{0, 0};
    if (t < 4 || gq < 2) {
      if (layer) res[i] = ld64<SC1>(rs, xo[i], xb);
      else res[i] = ld64<0>(rx, (c * D + d0) * 2, (4 * g + rb) * 16 * D * 2);
    }
  }
  SB();
  v4u wf[6][5];
#pragma unroll
  for (int u = 0; u < 6; ++u)
#pragma unroll
    for (int t = 0; t < 5; ++t) wf[u][t] = *(LAS v4u*)(L + B_W + ((kh * 9 + u) * 5 + t) * 1024 + lane * 16);
  SB();
  if (poller) {
    poll(X.ws, flag_off(F_A, layer, g) + (lane & 7) * 8, X.tag);
  }
  STAMP(X, layer * 32 + 7);
  BAR();
  STAMP(X, layer * 32 + 8);
  v4u of[9];
  const int ob = OFF_O + (4 * g + rb) * KS * 1024;
#pragma unroll
  for (int u = 0; u < (kh ? 7 : 9); ++u) of[u] = ld128<SC1>(rs, lane * 16 + (u & 3) * 1024, ob + kh * 9 * 1024 + (u >> 2) * 4096);
  if (kh) {
#pragma unroll
    for (int u = 7; u < 9; ++u) {
      const v2u p = ld64<SC1>(rs, gq * 256 + c * 8 + (u - 7) * 1024, ob + 16 * 1024);
      const v2u q = ld64<SC1>(rs, gq * 256 + c * 8 + (u - 7) * 1024 + 128, ob + 16 * 1024);
      of[u] = v4u{p.x, p.y, q.x, q.y};
    }
  }
  SB();
  f32x4 acc[5];
#pragma unroll
  for (int t = 0; t < 5; ++t) acc[t] = f32x4{0, 0, 0, 0};
#pragma unroll
  for (int u = 0; u < 9; ++u) {
#pragma unroll
    for (int t = 0; t < 5; ++t) {
      acc[t] = mfma16(wf[u % 6][t], of[u], acc[t]);
      if (u < 3) wf[u][t] = *(LAS v4u*)(L + B_W + ((kh * 9 + u + 6) * 5 + t) * 1024 + lane * 16);
    }
    SB();
  }
#pragma unroll
  for (int t = (kh ? 0 : 3); t < (kh ? 3 : 5); ++t) *(LAS f32x4*)(L + B_RED + (rb * 5 + t) * 1024 + lane * 16) = acc[t];
  STAMP(X, layer * 32 + 9);
  BAR();
  const int x1b = OFF_X1 + (4 * g + rb) * KS * 1024;
  float rsum = 0.f, rsq2 = 0.f;
#pragma unroll
  for (int i = 0; i < NK; ++i) {
    const int t = kh * 3 + i;
    const f32x4 o = *(LAS f32x4*)(L + B_RED + (rb * 5 + t) * 1024 + lane * 16);
    f32x4 v = o + acc[t];
    v.x += bflo(res[i].x); v.y += bfhi(res[i].x); v.z += bflo(res[i].y); v.w += bfhi(res[i].y);
    const v2u y = pk4(v);
    if (t < 4 || gq < 2) st64<SC1>(y, rs, xo[i], x1b);
    rsum = dot2(y.x, ONES, dot2(y.y, ONES, rsum));
    rsq2 = dot2(y.x, y.x, dot2(y.y, y.y, rsq2));
  }
  sum1632x2(rsum, rsq2);
  if (lane < 16) st64<SC1>(v2u{asu(rsum), asu(rsq2)}, rs, lane * 8, OFF_RS + ((4 * g + rb) * 16 + 2 * j + kh) * 128);
  STAMP(X, layer * 32 + 10);
  DRAIN();
  BAR();
  STAMP(X, layer * 32 + 11);
  if (poller) {
    if (lane == 0) signal(X.ws, flag_off(F_B, layer, g) + j * 8, X.tag);
    poll(X.ws, (lane >= 32 && layer == 0) ? flag_off(F_W, 0, j) + (lane & 31) * 8 : flag_off(F_B, layer, g) + (lane & 7) * 8, X.tag);
  }
  BAR();
  STAMP(X, layer * 32 + 12);
}

template <int NT, int BASE, int TOTAL, int LEAD, int R, int NB, typename HOOK>
__device__ __forceinline__ void c_gemm(LAS unsigned char* Xl, rsrc_t rs, int run, v4u (&a)[R], f32x4 (&acc)[NT][4], const f32x4 (&c0)[NB], int lane, HOOK hook) {
  constexpr int XB = NT > 2 ? 2 : 3;
  v4u xf[XB][4];
  LAS unsigned char* xp = Xl + lane * 16;
  LAS unsigned char* xq = xp + 2 * KS * 1024;
#define XF(r, u) (*(LAS v4u*)(((r) < 2 ? xp : xq) + (((r) & 1) * KS + (u)) * 1024))
#pragma unroll
  for (int b = 0; b < XB - 1; ++b)
#pragma unroll
    for (int r = 0; r < 4; ++r) xf[b][r] = XF(r, b);
  SB();
#pragma unroll
  for (int u = 0; u < KS; ++u) {
    hook(u);
#pragma unroll
    for (int t = 0; t < NT; ++t) {
      const int idx = BASE + u * NT + t;
#pragma unroll
      for (int r = 3; r >= 0; --r) {
        if (u == 0 && BASE == 0) m16c(acc[t][r], a[idx % R], xf[u % XB][r], c0[t % NB]);
        else if (u == 0) m16z(acc[t][r], a[idx % R], xf[u % XB][r]);
        else m16(acc[t][r], a[idx % R], xf[u % XB][r]);
        if (r >= 2 && t < 2 && u + XB - 1 < KS) { xf[(u + XB - 1) % XB][2 * t + 3 - r] = XF(2 * t + 3 - r, u + XB - 1); SB(); }
      }
      if (idx + LEAD < TOTAL) a[(idx + LEAD) % R] = ld128<SC1>(rs, lane * 16 + ((idx + LEAD) & 3) * 1024, run + ((idx + LEAD) >> 2) * 4096);
      SB();
    }
  }
#undef XF
  MFMA_DONE();
}

template <int HI>
__device__ __forceinline__ void stage_c(const Ctx& X, int layer, int run) {
  v4u a[ring<HI>()];
  constexpr int NT = 5 - HI, R = ring<HI>();
  LAS unsigned char* L = X.L;
  const rsrc_t rs = X.rs;
  const int g = X.g, j = X.j, w = X.w, lane = X.lane;
  const int c = lane & 15, gq = lane >> 4, rb = w & 3;
  const int T0 = HI ? 20 + 4 * (w - 4) : 5 * w;
  LAS unsigned* c_lo = (LAS unsigned*)(L + L_CNT + 20);
  LAS unsigned* c_hi = (LAS unsigned*)(L + L_CNT + 24);
  LAS unsigned* sig = (LAS unsigned*)(L + L_CNT + 32 + rb * 4);
  const unsigned four = 4 * (layer + 1);
  f32x4 bias[NT];
  {
    constexpr int U0 = HI ? XLO : 0, NU = HI ? KS - XLO : XLO;
    __builtin_amdgcn_s_setprio(HI ? 2 : 3);
    const int xb = OFF_X1 + ((4 * g + rb) * KS + U0) * 1024;
    v2u ps[4];
#pragma unroll
    for (int k = 0; k < 4; ++k) ps[k] = ld64<SC1>(rs, gq * 512 + k * 128 + c * 8, OFF_RS + (4 * g + rb) * 2048);
    v4u v[NU];
#pragma unroll
    for (int u = 0; u < NU; ++u) v[u] = ld128<SC1>(rs, lane * 16 + (u & 3) * 1024, xb + (u >> 2) * 4096);
    SB();
    if (!HI) ring_fill<0, CPF>(X, run, a);
#pragma unroll
    for (int t = 0; t < NT; ++t) bias[t] = *(LAS f32x4*)(L + L_CB + ((T0 + t) * 16 + 4 * gq) * 4);
    float s = (asf(ps[0].x) + asf(ps[1].x)) + (asf(ps[2].x) + asf(ps[3].x)), q = (asf(ps[0].y) + asf(ps[1].y)) + (asf(ps[2].y) + asf(ps[3].y));
    sum1632x2(s, q);
    const float mean = s * (1.f / D);
    const float var = fmx(q * (1.f / D) - mean * mean, 0.f);
    const float rstd = rsq(var + 1e-5f), nm = -mean * rstd;
#pragma unroll
    for (int u = 0; u < NU; ++u) {
      v4u o;
#pragma unroll
      for (int p = 0; p < 4; ++p) o[p] = pk2(__builtin_fmaf(bflo(v[u][p]), rstd, nm), __builtin_fmaf(bfhi(v[u][p]), rstd, nm));
      *(LAS v4u*)(L + C_X + (rb * KS + U0 + u) * 1024 + lane * 16) = o;
    }
    LDS_ARRIVE(HI ? c_hi : c_lo);
    STAMP(X, layer * 32 + 16);
    ring_fill<HI ? 0 : CPF, CPRE>(X, run, a);
    __builtin_amdgcn_s_setprio(0);
    lds_wait(c_lo, four);
    if (HI) {
      lds_wait(c_hi, four);
      if (SIGK >= 0) lds_wait(sig, layer + 1);
    }
    STAMP(X, layer * 32 + 17);
  }
  {
  f32x4 acc[NT][4];
  c_gemm<NT, 0, 2 * KS * NT, CPRE>(L + C_X, rs, run, a, acc, bias, lane, [&](int u) {
    if (!HI && u == XLO - 1) lds_wait(c_hi, four);
    if (!HI && u == SIGK) { __hip_atomic_store(sig, (unsigned)(layer + 1), RLX_WG); SB(); }
  });
  STAMP(X, layer * 32 + 19);
  v4u warm[WARM ? WARM : 1];
  if (WARM && HI && layer == 0) {
#pragma unroll
    for (int k = 0; k < WARM; ++k) {
      const int f = cmin(((g % (32 / RA)) * 4 + rb) * WARM + k, 341);
      warm[k] = ld128<SC1>(rs, lane * 16, f < 252 ? OFF_WQ + (j * 252 + f) * 1024 : OFF_WO + (j * 90 + f - 252) * 1024);
    }
#pragma unroll
    for (int k = 0; k < WARM; ++k) asm volatile("" ::"v"(warm[k]));
  }
  {
#pragma unroll
    for (int t = 0; t < NT; ++t) {
      const int T = T0 + t;
#pragma unroll
      for (int r = 0; r < 4; ++r) {
        float o[4];
#pragma unroll
        for (int e = 0; e < 4; ++e) {
          const float v = acc[t][r][e];
          const float p = __builtin_fmaf(v * v, -0.10294324f, -2.3022082f);
          const float ex = __builtin_amdgcn_exp2f(v * p);
          o[e] = v * __builtin_amdgcn_rcpf(1.0f + ex);
        }
        *(LAS v2u*)(L + C_H + (r * KS + (T >> 1)) * 1024 + ((2 * (T & 1) + (gq >> 1)) * 16 + c) * 16 + (gq & 1) * 8) = v2u{pk2(o[0], o[1]), pk2(o[2], o[3])};
#pragma unroll
        for (int k = CPRE; k < R; ++k)
          if ((k - CPRE) * (4 * NT) / (R > CPRE ? R - CPRE : 1) == t * 4 + r) {
            const int l = KS * NT + k;
            a[l % R] = ld128<SC1>(rs, lane * 16 + (l & 3) * 1024, run + (l >> 2) * 4096);
          }
        SB();
      }
    }
  }
  }
  STAMP(X, layer * 32 + 20);
  if (HI) {
    BAR();
  } else {
    LDS_ARRIVE((LAS unsigned*)(L + L_CNT + 8));
    lds_wait((LAS unsigned*)(L + L_CNT + 8), four);
  }
  STAMP(X, layer * 32 + 21);
  auto put = [&](auto& acc, auto np, int T) {
    constexpr int NP = decltype(np)::value;
#pragma unroll
    for (int r = 0; r < 4; ++r) {
      const int cn = 2 * r + (c >> 3), in = (c & 7) * 32 + gq * 8;
      if (cn != j) {
#pragma unroll
        for (int t = 0; t < NP; ++t) st64<SC1>(pk4(acc[t][r]), rs, (cn * 8 + ((j - cn - 1) & 7)) * 9216 + in + t * 256, OFF_P + g * 64 * 9216 + T * 256);
      } else {
#pragma unroll
        for (int t = 0; t < NP; ++t) *(LAS v2u*)(L + C_OWN + (T + t) * 256 + in) = pk4(acc[t][r]);
      }
    }
    SB();
  };
  auto publish = [&](int part) {
    SB();
    asm volatile("s_waitcnt vmcnt(0) lgkmcnt(0)" ::: "memory");
    if (lane == 0) {
      const unsigned old = __hip_atomic_fetch_add((LAS unsigned*)(L + L_CNT + (part == 2 ? 56 : part * 4)), 1u, RLX_WG);
      if ((old & 3u) == 3u) signal(X.ws, flag_off(F_C1 + part, layer, g) + j * 8, X.tag);
    }
    SB();
  };
  if (!HI) {
    f32x4 acc[5][4];
    c_gemm<5, KS * NT, 2 * KS * NT, R>(L + C_H, rs, run, a, acc, bias, lane, [&](int u) {
      if (u == 9) BAR();
    });
    STAMP(X, layer * 32 + 22);
    put(acc, IC<5>(), 5 * w);
    STAMP(X, layer * 32 + 23);
    publish(0);
  } else {
    f32x4 acc[4][4];
    c_gemm<4, KS * NT, 2 * KS * NT, R>(L + C_H, rs, run, a, acc, bias, lane, [&](int u) {});
    STAMP(X, layer * 32 + 22);
    put(acc, IC<4>(), 20 + 4 * rb);
    STAMP(X, layer * 32 + 23);
    publish(1);
  }
  STAMP(X, layer * 32 + 24);
}

template <int HI, int LAYER, typename PRE>
__device__ __forceinline__ void stage_d(const Ctx& X, PRE pre) {
  constexpr int layer = LAYER;
  LAS unsigned char* L = X.L;
  const rsrc_t rs = X.rs;
  const int g = X.g, j = X.j, w = X.w, lane = X.lane;
  const int pb = OFF_P + (g * 8 + j) * 8 * 9216;
  const int RB = 4 * g + (j >> 1);
  const int tok = (lane >> 1) & 7, hf = lane & 1;
  constexpr int I0 = HI ? 2 : 0;
  int fo[3], pp[3];
  v4u xr[3], bv[3], o[3];
  pp[0] = w * 64 + lane;
  pp[1] = 256 + w * 16 + (lane & 15);
  pp[2] = 320 + w * 32 + (lane & 31);
  constexpr int NL[3] = {64, 16, 32};
#pragma unroll
  for (int i = I0; i < 3; ++i) {
    const int T = pp[i] >> 4;
    fo[i] = (T >> 1) * 1024 + ((2 * (T & 1) + hf) * 16 + (j & 1) * 8 + tok) * 16;
    xr[i] = ld128<SC1>(rs, fo[i], OFF_X1 + RB * KS * 1024);
    bv[i] = ld128<0>(X.rb2, (T * 16 + hf * 8) * 2, 0);
  }
  SB();
  float s = 0.f, q = 0.f;
  v4u pv[3][8], own[3];
  auto fetch = [&](int i) {
    const int per = NL[i] / 8, grp = lane / NL[i];
#pragma unroll
    for (int k = 0; k < 8; ++k)
      if (k < per) {
        if (k < per - 1) pv[i][k] = ld128<SC1>(rs, pp[i] * 16 + grp * per * 9216, pb + k * 9216);
        else if (per < 8) pv[i][k] = ld128<SC1>(rs, pp[i] * 16 + (grp * per + (lane < 64 - NL[i] ? k : k - 1)) * 9216, pb);
      }
    own[i] = *(LAS v4u*)(L + C_OWN + pp[i] * 16);
    SB();
  };
  auto finish = [&](int i) {
    const int per = NL[i] / 8;
#pragma unroll
    for (int e = 0; e < 4; ++e) {
      pv[i][per - 1][e] = (per == 8 || lane >= 64 - NL[i]) ? own[i][e] : pv[i][per - 1][e];
      float l2 = 0.f, h2 = 0.f;
#pragma unroll
      for (int k = 0; k < 8; ++k)
        if (k < per) { l2 += bflo(pv[i][k][e]); h2 += bfhi(pv[i][k][e]); }
      if (NL[i] == 32) { l2 = sum32(l2); h2 = sum32(h2); }
      if (NL[i] == 16) sum1632x2(l2, h2);
      o[i][e] = pk2(bflo(bv[i][e]) + bflo(xr[i][e]) + l2, bfhi(bv[i][e]) + bfhi(xr[i][e]) + h2);
    }
    if (lane < NL[i]) {
      if (layer == 0) st128<SC1>(o[i], rs, fo[i], OFF_X2 + RB * KS * 1024);
      else st128<OUTAUX>(o[i], X.rout, ((g * 64 + j * 8 + tok) * D + (pp[i] >> 4) * 16 + hf * 8) * 2, 0);
      if (layer == 0) {
#pragma unroll
        for (int e = 0; e < 4; ++e) {
          const float lo = bflo(o[i][e]), hi = bfhi(o[i][e]);
          s += lo + hi;
          q = __builtin_fmaf(lo, lo, __builtin_fmaf(hi, hi, q));
        }
      }
    }
    SB();
  };
  if (!HI) {
    poll(X.ws, flag_off(F_C1, layer, g) + (lane & 7) * 8, X.tag);
    STAMP(X, layer * 32 + 25);
    fetch(0);
    fetch(1);
    finish(0);
    finish(1);
    pre();
  }
  STAMP(X, layer * 32 + 26);
  if (HI && w == 7) poll(X.ws, flag_off(F_C2, layer, g) + (lane & 7) * 8, X.tag);
  BAR();
  STAMP(X, layer * 32 + 27);
  fetch(2);
  finish(2);
  STAMP(X, layer * 32 + 28);
  if (layer == 0) {
    s += asf(__builtin_amdgcn_mov_dpp(asu(s), 0xB1, 0xF, 0xF, true));
    q += asf(__builtin_amdgcn_mov_dpp(asu(q), 0xB1, 0xF, 0xF, true));
    sum1632x2(s, q);
    if (lane < 16 && hf == 0) *(LAS f32x2*)(L + D_ST + (tok * 8 + w) * 8) = f32x2{s, q};
    BAR();
    float ts = 0.f, tq = 0.f;
#pragma unroll
    for (int k = 0; k < 4; ++k) {
      const f32x4 v = *(LAS f32x4*)(L + D_ST + tok * 64 + k * 16);
      ts += v.x + v.z;
      tq += v.y + v.w;
    }
    const float mean = ts * (1.f / D);
    const float var = fmx(tq * (1.f / D) - mean * mean, 0.f);
    const float rstd = rsq(var + 1e-5f), nm = -mean * rstd;
#pragma unroll
    for (int i = I0; i < 3; ++i)
      if (lane < NL[i]) {
        v4u y;
#pragma unroll
        for (int e = 0; e < 4; ++e) y[e] = pk2(__builtin_fmaf(bflo(o[i][e]), rstd, nm), __builtin_fmaf(bfhi(o[i][e]), rstd, nm));
        st128<SC1>(y, rs, fo[i], OFF_XN + XBUF + RB * KS * 1024);
      }
    SB();
  }
}

template <int HI>
__device__ __forceinline__ void body(Ctx& X) {
  const int run0 = OFF_WC + (X.j * 1296 + (HI ? 720 + (X.w - 4) * 144 : X.w * 180)) * 1024;
  v4u wq[RQ][4];
  asm volatile("; role %0" ::"i"(HI) : "memory");
#pragma unroll
  for (int u = 0; u < RQ; ++u)
#pragma unroll
    for (int b = 0; b < 4; ++b) wq[u][b] = v4u{0, 0, 0, 0};
#pragma unroll 1
  for (int layer = 0;; ++layer) {
    stage_a<HI>(fresh(X), layer, wq);
    stage_b<HI>(fresh(X), layer);
    int run = run0;
    asm volatile("" : "+s"(run));
    stage_c<HI>(fresh(X), layer, run);
    if (layer == 1) break;
    if (!HI && !LOLATE) wq_prefetch(fresh(X), wq);
    stage_d<HI, 0>(fresh(X), [&] { if (!HI && LOLATE) wq_prefetch(fresh(X), wq); });
    DRAIN();
    STAMP(X, 29);
    BAR();
    if (HI && X.w == 7) {
      if (X.lane == 0) signal(X.ws, flag_off(F_D, 1, X.g) + X.j * 8, X.tag);
      if (BIDX == 0 && X.lane == 1) signal(X.ws, 0, X.tag);
      poll(X.ws, (X.lane >= 32 && X.g > 0) ? flag_off(F_D, 1, X.g - 1) + (6 + (X.lane & 1)) * 8 : flag_off(F_D, 1, X.g) + (X.lane & 7) * 8, X.tag);
    }
    BAR();
    STAMP(X, 32);
  }
  stage_d<HI, 1>(fresh(X), [] {});
  STAMP(X, 32 + 29);
}

}

extern "C" __global__ void __launch_bounds__(NTHR, 2) ilk_main(unsigned char* ws, const u16* x, const u16* wqkv, const u16* wo, const u16* w1, const u16* w2,
                                                  const u16* b1, const u16* b2, u16* out) {
  extern __shared__ __attribute__((aligned(16))) unsigned char smem[];
  Ctx X;
  X.ws = ws;
  X.x = x;
  X.wqkv = wqkv;
  X.wo = wo;
  X.w1 = w1;
  X.w2 = w2;
  X.b1 = b1;
  X.L = (LAS unsigned char*)smem;
  X.rs = __builtin_amdgcn_make_buffer_rsrc((void*)ws, 0, WS_BYTES, 0x00020000);
  X.rb2 = __builtin_amdgcn_make_buffer_rsrc((void*)b2, 0, D * 2, 0x00020000);
  X.rout = __builtin_amdgcn_make_buffer_rsrc((void*)out, 0, S * D * 2, 0x00020000);
  X.tag = 0;
  X.lane = TIDX & 63;
  X.w = __builtin_amdgcn_readfirstlane(TIDX >> 6);
  const int xc = BIDX & 7, i = BIDX >> 3;
  X.g = (xc % RA) * (32 / RA) + i / RA;
  X.j = (xc / RA) * RA + i % RA;
  STAMP(X, 0);
  {
    const u64 e = __hip_atomic_load((gu64*)X.ws, RLX_AGENT);
    if (TIDX < 32) *(LAS unsigned*)(X.L + L_CNT + TIDX * 4) = 0u;
    BAR();
    unsigned bw = 0;
    if (TIDX < 288) bw = *(const GAS unsigned*)(X.b1 + X.j * 576 + TIDX * 2);
    if (X.w >= 3) __builtin_amdgcn_s_setprio(3);
    if (__builtin_expect(X.w > 3, 1)) qo_tile(X);
    asm volatile("" ::: "memory");
    if (__builtin_expect(X.w == 3, 1)) x0_rows(X);
    asm volatile("" ::: "memory");
    if (X.w >= 3) DRAIN();
    if (__builtin_expect(X.w < 3, 1)) {
      {
        u64 pc;
        asm volatile("s_getpc_b64 s[20:21]\n\ts_add_u32 s20, s20, ilk_main@rel32@lo+4\n\ts_addc_u32 s21, s21, ilk_main@rel32@hi+12\n\ts_mov_b64 %0, s[20:21]"
                     : "=s"(pc) : : "scc", "s20", "s21");
        const v4u t = *((const GAS v4u*)pc + (cmin(i * 3 + X.w, CODE_KB - 1) * 64 + X.lane));
        asm volatile("" ::"v"(t));
      }
      __builtin_amdgcn_s_sleep(PSLEEP);
      const int t0 = rt_task(X.g, X.w), t1 = rt_task(X.g, X.w + 3);
      v4u r0[8], r1[8];
      rt_load(X, t0, r0);
#pragma unroll
      for (int k = 0; k < 8; ++k) r1[k] = v4u{0, 0, 0, 0};
      if (t1 >= 0) rt_load(X, t1, r1);
      rt_turn(X, t0, X.w, r0);
      if (t1 >= 0) rt_turn(X, t1, X.w + 3, r1);
    }
    if (TIDX < 288) *(LAS f32x2*)(X.L + L_CB + TIDX * 8) = f32x2{bflo(bw), bfhi(bw)};
    const unsigned elo = __builtin_amdgcn_readfirstlane((unsigned)e), ehi = __builtin_amdgcn_readfirstlane((unsigned)(e >> 32));
    X.tag = (((u64)ehi << 32) | elo) + 0x9E3779B97F4A7C15ull;
    STAMP(X, 30);
    if (X.w >= 3) {
      unsigned old = 0;
      if (X.lane == 0) old = __hip_atomic_fetch_add((LAS unsigned*)(X.L + L_CNT + 12), 1u, RLX_WG);
      if (__builtin_amdgcn_readfirstlane(old) == 4u) {
        if (X.lane < 2) signal(X.ws, X.lane ? flag_off(F_Q, 0, X.j) + X.g * 8 : flag_off(F_D, 0, X.g) + X.j * 8, X.tag);
        poll(X.ws, X.lane >= 32 ? flag_off(F_Q, 0, X.j) + (X.lane & 31) * 8
                                : (X.lane >= 16 && X.g > 0) ? flag_off(F_D, 0, X.g - 1) + (6 + (X.lane & 1)) * 8 : flag_off(F_D, 0, X.g) + (X.lane & 7) * 8, X.tag);
      }
      __builtin_amdgcn_s_setprio(0);
    }
    BAR();
    STAMP(X, 31);
  }
  if (X.w < 4) body<0>(X);
  else body<1>(X);
}
#if defined(__HIP_DEVICE_COMPILE__)
#pragma clang attribute pop
#endif

extern "C" void kernel_launch(void* const* d_in, const int* in_sizes, int n_in, void* d_out, int out_size, void* d_ws, size_t ws_size,
                              hipStream_t stream) {
  (void)hipFuncSetAttribute((const void*)ilk_main, hipFuncAttributeMaxDynamicSharedMemorySize, LDS_BYTES);
  ilk_main<<<256, NTHR, LDS_BYTES, stream>>>((unsigned char*)d_ws, (const u16*)d_in[0], (const u16*)d_in[1], (const u16*)d_in[2], (const u16*)d_in[4],
                                           (const u16*)d_in[6], (const u16*)d_in[5], (const u16*)d_in[7], (u16*)d_out);
}
```

```cpp
#include <hip/hip_runtime.h>
#include <hip/hip_bf16.h>

#if defined(__HIP_DEVICE_COMPILE__)
#pragma clang attribute push(__attribute__((target("no-packed-fp32-ops"))), apply_to = function)
#endif
namespace {
typedef unsigned short u16;
typedef unsigned v4u __attribute__((ext_vector_type(4)));
typedef unsigned v2u __attribute__((ext_vector_type(2)));
typedef float f32x4 __attribute__((ext_vector_type(4)));
typedef float f32x2 __attribute__((ext_vector_type(2)));
typedef __bf16 bf16x2_t __attribute__((ext_vector_type(2)));
typedef __bf16 bf16x8_t __attribute__((ext_vector_type(8)));
#define GAS __attribute__((address_space(1)))
#define LAS __attribute__((address_space(3)))
typedef __amdgpu_buffer_rsrc_t rsrc_t;

constexpr int S = 2048, D = 576, F = 4608;
constexpr int KS = 18;
constexpr int NTHR = 512;
constexpr int XBUF = 128 * KS * 1024;
constexpr int OFF_CTRL = 0;
constexpr int OFF_CS = 4 * 65536;
constexpr int OFF_RS = OFF_CS + 3 * 65536;
constexpr int OFF_WQ = OFF_RS + 128 * 2048;
constexpr int OFF_WO = OFF_WQ + 8 * 14 * KS * 1024;
constexpr int OFF_WC = OFF_WO + 8 * 90 * 1024;
constexpr int OFF_XN = OFF_WC + 8 * 1296 * 1024;
constexpr int OFF_O = OFF_XN + 2 * XBUF;
constexpr int OFF_X1 = OFF_O + XBUF;
constexpr int OFF_X2 = OFF_X1 + XBUF;
constexpr int OFF_P = OFF_X2 + XBUF;
constexpr int WS_BYTES = OFF_P + 8 * XBUF;

__device__ __forceinline__ f32x4 mfma16(v4u a, v4u b, f32x4 c) {
  return __builtin_amdgcn_mfma_f32_16x16x32_bf16(__builtin_bit_cast(bf16x8_t, a), __builtin_bit_cast(bf16x8_t, b), c, 0, 0, 0);
}
__device__ __forceinline__ void m16(f32x4& c, v4u a, v4u b) { asm volatile("v_mfma_f32_16x16x32_bf16 %0, %1, %2, %0" : "+v"(c) : "v"(a), "v"(b)); }
__device__ __forceinline__ void m16z(f32x4& c, v4u a, v4u b) { asm volatile("v_mfma_f32_16x16x32_bf16 %0, %1, %2, 0" : "=&v"(c) : "v"(a), "v"(b)); }
__device__ __forceinline__ void m16c(f32x4& d, v4u a, v4u b, f32x4 c) { asm volatile("v_mfma_f32_16x16x32_bf16 %0, %1, %2, %3" : "=&v"(d) : "v"(a), "v"(b), "v"(c)); }
#define M16(c, a, b) do { if (u == 0) m16z(c, a, b); else m16(c, a, b); } while (0)
__device__ __forceinline__ unsigned pk2(float lo, float hi) {
  f32x2 v = {lo, hi};
  return __builtin_bit_cast(unsigned, __builtin_convertvector(v, bf16x2_t));
}
__device__ __forceinline__ float asf(unsigned u) { return __builtin_bit_cast(float, u); }
__device__ __forceinline__ unsigned asu(float f) { return __builtin_bit_cast(unsigned, f); }
__device__ __forceinline__ float bflo(unsigned u) { return asf(u << 16); }
__device__ __forceinline__ float bfhi(unsigned u) { return asf(u & 0xffff0000u); }
__device__ __forceinline__ v2u pk4(f32x4 v) { return v2u{pk2(v.x, v.y), pk2(v.z, v.w)}; }
__device__ __forceinline__ float dot2(unsigned a, unsigned b, float c) {
  return __builtin_amdgcn_fdot2_f32_bf16(__builtin_bit_cast(bf16x2_t, a), __builtin_bit_cast(bf16x2_t, b), c, false);
}
constexpr unsigned ONES = 0x3f803f80u;

template <int AUX> __device__ __forceinline__ v4u ld128(rsrc_t rs, int voff, int soff) { return __builtin_amdgcn_raw_buffer_load_b128(rs, voff, soff, AUX); }
template <int AUX> __device__ __forceinline__ v2u ld64(rsrc_t rs, int voff, int soff) { return __builtin_amdgcn_raw_buffer_load_b64(rs, voff, soff, AUX); }
template <int AUX> __device__ __forceinline__ void st128(v4u v, rsrc_t rs, int voff, int soff) { __builtin_amdgcn_raw_buffer_store_b128(v, rs, voff + soff, 0, AUX); }
template <int AUX> __device__ __forceinline__ void st64(v2u v, rsrc_t rs, int voff, int soff) { __builtin_amdgcn_raw_buffer_store_b64(v, rs, voff, soff, AUX); }
template <int AUX> __device__ __forceinline__ void st32(unsigned v, rsrc_t rs, int voff, int soff) { __builtin_amdgcn_raw_buffer_store_b32(v, rs, voff, soff, AUX); }
#define SB() __builtin_amdgcn_sched_barrier(0)
#define MFMA_DONE() do { SB(); asm volatile("s_nop 7\n\ts_nop 1" ::: "memory"); SB(); } while (0)
#define TIDX ((int)__builtin_amdgcn_workitem_id_x())
#define BIDX ((int)__builtin_amdgcn_workgroup_id_x())
#define SYNC() do { __builtin_amdgcn_fence(__ATOMIC_RELEASE, "workgroup"); __builtin_amdgcn_s_barrier(); __builtin_amdgcn_fence(__ATOMIC_ACQUIRE, "workgroup"); } while (0)
__device__ __forceinline__ float fmx(float a, float b) { return __builtin_fmaxf(a, b); }
__device__ __forceinline__ float rsq(float a) { return __builtin_amdgcn_rsqf(a); }

__device__ __forceinline__ float sum32(float v) {
  const auto b = __builtin_amdgcn_permlane32_swap(asu(v), asu(v), false, false);
  const unsigned b0 = b[0], b1 = b[1];
  return asf(b0) + asf(b1);
}
__device__ __forceinline__ float sum1632(float v) {
  const auto a = __builtin_amdgcn_permlane16_swap(asu(v), asu(v), false, false);
  const unsigned a0 = a[0], a1 = a[1];
  return sum32(asf(a0) + asf(a1));
}
__device__ __forceinline__ float max1632(float v) {
  const auto a = __builtin_amdgcn_permlane16_swap(asu(v), asu(v), false, false);
  const unsigned a0 = a[0], a1 = a[1];
  v = fmx(asf(a0), asf(a1));
  const auto b = __builtin_amdgcn_permlane32_swap(asu(v), asu(v), false, false);
  const unsigned b0 = b[0], b1 = b[1];
  return fmx(asf(b0), asf(b1));
}

template <typename OP> __device__ __forceinline__ void red1632x2(float& a, float& b, OP op) {
  const auto s = __builtin_amdgcn_permlane16_swap(asu(a), asu(b), false, false);
  const unsigned s0 = s[0], s1 = s[1];
  const float t = op(asf(s0), asf(s1));
  const auto u = __builtin_amdgcn_permlane32_swap(asu(t), asu(t), false, false);
  const unsigned u0 = u[0], u1 = u[1];
  const float v = op(asf(u0), asf(u1));
  const auto x = __builtin_amdgcn_permlane16_swap(asu(v), asu(v), false, false);
  const unsigned x0 = x[0], x1 = x[1];
  a = asf(x0);
  b = asf(x1);
}
__device__ __forceinline__ void max1632x2(float& a, float& b) { red1632x2(a, b, [](float x, float y) { return fmx(x, y); }); }
__device__ __forceinline__ void sum1632x2(float& a, float& b) { red1632x2(a, b, [](float x, float y) { return x + y; }); }

__device__ __forceinline__ void tr8x8(const v4u (&r)[8], v4u (&o)[8]) {
#pragma unroll
  for (int c = 0; c < 4; ++c) {
#pragma unroll
    for (int p = 0; p < 4; ++p) {
      o[2 * c][p] = __builtin_amdgcn_perm(r[2 * p + 1][c], r[2 * p][c], 0x05040100u);
      o[2 * c + 1][p] = __builtin_amdgcn_perm(r[2 * p + 1][c], r[2 * p][c], 0x07060302u);
    }
  }
}
#define RA 2
#define RING_LO 12
#define RING_HI 32
#define QRING_LO 14
#define QRING 4
#define HPRE 12
#define PSLEEP 1
#define BPF 6
#define RAWAUX 2
#define CODE_KB 61
#define RTU0 3
#define RTS 2
#define LOPRE 3
#define LOLATE 0
#define CLEAD 12
#define XLO 6
#define CPF 5
#define OUTAUX 0
#define RTSLEEP 4
#define WOK 15
#define WARM 0
#define APRIO 3
#define PUBK 10
#define WOFIRST 0
#define SIGK 12
template <int HI> constexpr int ring() { return HI ? RING_HI : RING_LO; }
constexpr int RQ = QRING;
constexpr int CPRE = CLEAD;
constexpr int cmin(int a, int b) { return a < b ? a : b; }
template <int N> struct IC { static constexpr int value = N; };
constexpr int SC1 = 16;
constexpr int A_XT = 0;
constexpr int A_Q = 5 * KS * 1024;
constexpr int A_K = A_Q + 16 * 512;
constexpr int A_V = A_K + 20 * 512;
constexpr int A_EQK = A_V + 20 * 512;
constexpr int A_EV = A_EQK + 5 * 512;
constexpr int B_W = 0, B_RED = 124 * 1024;
constexpr int C_X = 0, C_OWN = 0, C_H = 4 * KS * 1024, C_ST = 8 * KS * 1024;
constexpr int L_CB = C_ST + 1024;
constexpr int D_ST = L_CB + 576 * 8;
constexpr int L_CNT = D_ST + 512;
constexpr int L_STAMP = L_CNT + 128;
#define STAMPCLK() __builtin_amdgcn_s_memrealtime()
constexpr int LDS_BYTES = L_STAMP;
#define STAMP(X_, slot) do {} while (0)
enum { F_A = 0, F_B = 1, F_C1 = 2, F_C2 = 3, F_C3 = 4, F_D = 5, F_Q = 6, F_W = 7 };
typedef unsigned long long u64;
typedef GAS u64 gu64;
__device__ __forceinline__ int flag_off(int kind, int layer, int g) { return 4096 + ((kind * 2 + layer) * 32 + g) * 256; }
#define RLX_AGENT __ATOMIC_RELAXED, __HIP_MEMORY_SCOPE_AGENT
#define RLX_WG __ATOMIC_RELAXED, __HIP_MEMORY_SCOPE_WORKGROUP
#define BAR() do { SB(); asm volatile("s_waitcnt lgkmcnt(0)\n\ts_barrier" ::: "memory"); SB(); } while (0)
#define DRAIN() do { SB(); asm volatile("s_waitcnt vmcnt(0)" ::: "memory"); SB(); } while (0)
__device__ __forceinline__ void poll(unsigned char* ws, int off, u64 tag) {
  gu64* p = (gu64*)(ws + off);
#pragma unroll 1
  for (unsigned spins = 0; spins < (1u << 22); ++spins) {
    const u64 v = __hip_atomic_load(p, RLX_AGENT);
    if (__builtin_amdgcn_ballot_w64(v != tag) == 0) break;
    __builtin_amdgcn_s_sleep(1);
  }
  __builtin_amdgcn_fence(__ATOMIC_ACQUIRE, "wavefront");
  asm volatile("" ::: "memory");
}
__device__ __forceinline__ void signal(unsigned char* ws, int off, u64 tag) { __hip_atomic_store((gu64*)(ws + off), tag, RLX_AGENT); }
__device__ __forceinline__ void lds_wait(LAS unsigned* p, unsigned want) {
#pragma unroll 1
  for (unsigned spins = 0; spins < (1u << 24); ++spins) {
    const unsigned v = __builtin_amdgcn_readfirstlane(__hip_atomic_load(p, RLX_WG));
    if (v >= want) break;
    __builtin_amdgcn_s_sleep(1);
  }
  asm volatile("" ::: "memory");
  SB();
}

#define LDS_ARRIVE(p) do { SB(); asm volatile("s_waitcnt lgkmcnt(0)" ::: "memory"); if (lane == 0) __hip_atomic_fetch_add((p), 1u, RLX_WG); SB(); } while (0)

struct Ctx {
  unsigned char* ws;
  const u16* x;
  const u16* wqkv;
  const u16* wo;
  const u16* w1;
  const u16* w2;
  const u16* b1;
  LAS unsigned char* L;
  rsrc_t rs, rb2, rout;
  u64 tag;
  int g, j, w, lane;
};
__device__ __forceinline__ Ctx fresh(const Ctx& X) {
  Ctx Y = X;
  asm volatile("" : "+v"(Y.lane));
  asm volatile("" : "+s"(Y.g), "+s"(Y.j), "+s"(Y.w));
  return Y;
}

__device__ __forceinline__ int rt_task(int g, int q) { return q < 5 ? g * 5 + q : (g < 2 ? 160 + g : -1); }
__device__ __forceinline__ void rt_load(const Ctx& X, int task, v4u (&r)[8]) {
  const bool is1 = task < 81;
  const int tt = is1 ? task : task - 81, chunk = tt / 9, strip = tt - 9 * chunk;
  const u16* src = is1 ? X.w1 + (size_t)chunk * 64 * F + X.j * 576 + strip * 64 : X.w2 + (size_t)(X.j * 576 + chunk * 64) * D + strip * 64;
  const int ld = is1 ? F : D;
  const rsrc_t rw = __builtin_amdgcn_make_buffer_rsrc((void*)src, 0, (63 * ld + 64) * 2, 0x00020000);
  const int vo = ((X.lane >> 3) * 8 * ld + (X.lane & 7) * 8) * 2;
#pragma unroll
  for (int jj = 0; jj < 8; ++jj) r[jj] = ld128<RAWAUX>(rw, vo, jj * ld * 2);
  SB();
}
__device__ __forceinline__ void rt_turn(const Ctx& X, int task, int q, const v4u (&r)[8]) {
  const int lane = X.lane, kg = lane >> 3, nb = lane & 7;
  LAS unsigned char* sm = X.L + A_Q + q * 8192;
  v4u o[8];
  tr8x8(r, o);
  const int base = (((kg >> 2) * 4 + (nb >> 1)) * 64 + (kg & 3) * 16 + (nb & 1) * 8) * 16;
#pragma unroll
  for (int c = 0; c < 8; ++c) *(LAS v4u*)(sm + base + ((c ^ nb) * 16)) = o[c];
  SB();
}
__device__ __forceinline__ v4u rt_get(const Ctx& X, int q, int f) {
  const int lane = X.lane;
  return *(LAS v4u*)(X.L + A_Q + q * 8192 + (f * 64 + (lane & ~7) + ((lane & 7) ^ ((f & 3) * 2 + ((lane >> 3) & 1)))) * 16);
}
__device__ __forceinline__ void rt_put(const Ctx& X, int task, int f, v4u v) {
  const bool is1 = task < 81;
  const int tt = is1 ? task : task - 81, chunk = (tt * 57) >> 9, strip = tt - 9 * chunk;
  const int T = strip * 4 + (f & 3), lo = strip < 5;
  const int ks = chunk * 2 + (f >> 2);
  int o;
  if (lo) o = ((T * 13) >> 6) * 175 + T + ((is1 ? 0 : KS) + ks) * 5;
  else o = strip * 144 + (f & 3) + ((is1 ? 0 : KS) + ks) * 4;
  const int fr = X.j * 1296 + o;
  st128<SC1>(v, X.rs, X.lane * 16, OFF_WC + fr * 1024);
}

__device__ __forceinline__ void qo_tile(const Ctx& X) {
  constexpr int NQ = 2;
  const rsrc_t rq_ = __builtin_amdgcn_make_buffer_rsrc((void*)X.wqkv, 0, D * 3 * D * 2, 0x00020000);
  const int j = X.j;
  unsigned e[NQ][8];
  int dst[NQ];
  bool zero[NQ];
#pragma unroll
  for (int i = 0; i < NQ; ++i) {
    const int q = i * 256 + (X.w - 4) * 64 + X.lane, P = X.g * 504 + (q < 504 ? q : 503);
    const int kg = ((P >> 4) * 4682) >> 16, blk = (P >> 4) - kg * 14, u = kg >> 2, gq = kg & 3, m = P & 15;
    const int ty = blk < 12 ? blk >> 2 : blk == 12 ? (m >> 1) & 1 : 2;
    const int hd = blk < 12 ? blk & 3 : blk == 12 ? m >> 2 : (m >> 1) & 3;
    const int col = ty * D + (4 * j + hd) * 18 + (blk < 12 ? m : 16 + (m & 1));
    zero[i] = blk == 13 && m >= 8;
    dst[i] = OFF_WQ + (((j * 14 + blk) * KS + u) * 64 + gq * 16 + m) * 16;
    const int vo = ((u * 32 + gq * 8) * 3 * D + col) * 2;
#pragma unroll
    for (int jj = 0; jj < 8; ++jj) e[i][jj] = __builtin_amdgcn_raw_buffer_load_b16(rq_, vo, jj * 3 * D * 2, 0);
  }
  const rsrc_t ro_ = __builtin_amdgcn_make_buffer_rsrc((void*)X.wo, 0, D * D * 2, 0x00020000);
  const int t = (X.w - 4) * 64 + X.lane;
  const int P = X.g * 180 + (t < 180 ? t : 179);
  const int kg = ((P >> 4) * 13108) >> 16, tl = (P >> 4) - kg * 5, u = kg >> 2, gq = kg & 3, m = P & 15;
  const bool zo = tl * 16 + m >= 72;
  const int col = j * 72 + (zo ? 0 : tl * 16 + m);
  const int r0 = u < 16 ? 36 * u + 4 * gq : 72 * (4 * (u - 16) + gq) + 16;
  unsigned eo[8];
#pragma unroll
  for (int jj = 0; jj < 8; ++jj) {
    const int row = r0 + (u < 16 ? (jj >> 2) * 18 + (jj & 3) : (jj >> 1) * 18 + (jj & 1));
    eo[jj] = __builtin_amdgcn_raw_buffer_load_b16(ro_, (row * D + col) * 2, 0, 0);
  }
  SB();
#pragma unroll
  for (int i = 0; i < NQ; ++i) {
    v4u o;
#pragma unroll
    for (int p = 0; p < 4; ++p) o[p] = zero[i] ? 0u : (e[i][2 * p] | (e[i][2 * p + 1] << 16));
    st128<SC1>(o, X.rs, dst[i], 0);
  }
  v4u o;
#pragma unroll
  for (int p = 0; p < 4; ++p) o[p] = zo ? 0u : (eo[2 * p] | (eo[2 * p + 1] << 16));
  if (t < 180) st128<SC1>(o, X.rs, OFF_WO + ((j * 90 + u * 5 + tl) * 64 + gq * 16 + m) * 16, 0);
  SB();
}

__device__ __forceinline__ void x0_rows(const Ctx& X) {
  const rsrc_t rx = __builtin_amdgcn_make_buffer_rsrc((void*)X.x, 0, S * D * 2, 0x00020000);
  const int tok = X.lane & 7, q8 = X.lane >> 3;
  v4u v[9];
#pragma unroll
  for (int i = 0; i < 9; ++i) v[i] = ld128<0>(rx, (tok * D + q8 * 8 + i * 64) * 2, (X.g * 64 + X.j * 8) * D * 2);
  SB();
  float s = 0.f, q = 0.f;
#pragma unroll
  for (int i = 0; i < 9; ++i)
#pragma unroll
    for (int p = 0; p < 4; ++p) { s = dot2(v[i][p], ONES, s); q = dot2(v[i][p], v[i][p], q); }
  s += asf(__builtin_amdgcn_mov_dpp(asu(s), 0x128, 0xF, 0xF, true));
  q += asf(__builtin_amdgcn_mov_dpp(asu(q), 0x128, 0xF, 0xF, true));
  sum1632x2(s, q);
  const float mean = s * (1.f / D);
  const float var = fmx(q * (1.f / D) - mean * mean, 0.f);
  const float rstd = rsq(var + 1e-5f), nm = -mean * rstd;
#pragma unroll
  for (int i = 0; i < 9; ++i) {
    v4u o;
#pragma unroll
    for (int p = 0; p < 4; ++p) o[p] = pk2(__builtin_fmaf(bflo(v[i][p]), rstd, nm), __builtin_fmaf(bfhi(v[i][p]), rstd, nm));
    st128<SC1>(o, X.rs, (q8 * 16 + tok) * 16 + i * 2048, OFF_XN + (4 * X.g + (X.j >> 1)) * KS * 1024 + (X.j & 1) * 128);
  }
  SB();
}

__device__ __forceinline__ v4u wq_load(const Ctx& X, int b, int u) {
  const int blk = b < 3 ? b * 4 + X.w : 12 + (X.w >> 1);
  return ld128<SC1>(X.rs, X.lane * 16 + (u & 3) * 1024, OFF_WQ + (X.j * 14 + blk) * KS * 1024 + (u >> 2) * 4096);
}
__device__ __forceinline__ void wq_prefetch(const Ctx& X, v4u (&wq)[RQ][4]) {
#pragma unroll
  for (int u = 0; u < RQ - 1; ++u)
#pragma unroll
    for (int b = 0; b < 4; ++b) wq[u][b] = wq_load(X, b, u);
  SB();
}
__device__ __forceinline__ v4u xn_load(const Ctx& X, int layer, int f) {
  const int u = (f * 205) >> 10, rb5 = f - 5 * u;
  return ld128<SC1>(X.rs, X.lane * 16, OFF_XN + layer * XBUF + ((4 * X.g - 1 + (X.g == 0 && rb5 == 0 ? 1 : rb5)) * KS + u) * 1024);
}

template <int LO, int HIX, int R>
__device__ __forceinline__ void ring_fill(const Ctx& X, int run, v4u (&a)[R]) {
#pragma unroll
  for (int i = LO; i < HIX; ++i) a[i] = ld128<SC1>(X.rs, X.lane * 16 + (i & 3) * 1024, run + (i >> 2) * 4096);
  SB();
}
template <int LO, int HIX, int R>
__device__ __forceinline__ void ring_zero(v4u (&a)[R]) {
#pragma unroll
  for (int i = LO; i < HIX; ++i) a[i] = v4u{0, 0, 0, 0};
}

template <int HI>
__device__ __forceinline__ void stage_a(const Ctx& X, int layer, v4u (&wq)[RQ][4]) {
  LAS unsigned char* L = X.L;
  const rsrc_t rs = X.rs;
  const int g = X.g, j = X.j, w = X.w, lane = X.lane;
  LAS unsigned* cx = (LAS unsigned*)(L + L_CNT + 64);
  const unsigned four = 4 * (layer + 1);
  v4u wt[23];
  if (HI) {
    const int q = w - 4;
    __builtin_amdgcn_s_setprio(2);
    v4u t[18];
#pragma unroll
    for (int i = 0; i < 18; ++i) t[i] = xn_load(X, layer, i < 8 ? cmin(20 + 4 * i + q, 49) : 50 + 4 * (i - 8) + q);
    SB();
#pragma unroll
    for (int i = 0; i < 18; ++i) {
      *(LAS v4u*)(L + A_XT + (i < 8 ? cmin(20 + 4 * i + q, 49) : 50 + 4 * (i - 8) + q) * 1024 + lane * 16) = t[i];
      if (i == 7) LDS_ARRIVE(cx + 1);
    }
    LDS_ARRIVE(cx + 2);
    __builtin_amdgcn_s_setprio(0);
    STAMP(X, layer * 32 + 1);
    if (layer == 0) {
#pragma unroll 1
      for (int k = 0; k < 12; ++k) {
        const int task = rt_task(g, k >> 1);
        if (task >= 0) rt_put(X, task, (k & 1) * 4 + q, rt_get(X, k >> 1, (k & 1) * 4 + q));
        __builtin_amdgcn_s_sleep(RTSLEEP);
      }
      LDS_ARRIVE((LAS unsigned*)(L + L_CNT + 52));
    }
    if (WOK >= 0) lds_wait(cx + 3, four);
#pragma unroll
    for (int i = 0; i < 23; ++i) wt[i] = ld128<SC1>(rs, lane * 16, OFF_WO + (j * 90 + (i < 22 ? i * 4 + q : 88 + (q & 1))) * 1024);
    STAMP(X, layer * 32 + 2);
  } else {
    __builtin_amdgcn_s_setprio(3);
    {
      v4u t[5];
#pragma unroll
      for (int i = 0; i < 5; ++i) t[i] = xn_load(X, layer, 4 * i + w);
      SB();
      if (layer == 0) wq_prefetch(X, wq);
#pragma unroll
      for (int i = 0; i < 5; ++i) *(LAS v4u*)(L + A_XT + (4 * i + w) * 1024 + lane * 16) = t[i];
      LDS_ARRIVE(cx);
    }
    __builtin_amdgcn_s_setprio(APRIO);
    STAMP(X, layer * 32 + 1);
    lds_wait(cx, four);
    STAMP(X, layer * 32 + 2);
    f32x4 qc[5], kc[5], vc[5], ec[3];
    v4u xf[5];
    const int r1 = (w & 1) ? 3 : 1, r3 = (w & 1) ? 1 : 3;
    const int eoff = w < 2 ? A_EQK + lane * 8 : A_EV + (((lane & 15) >> 2) * 16 + 4 * (lane >> 4)) * 8 + (lane & 3) * 2, estr = w < 2 ? 1 : 4;
    LAS unsigned char* x0 = L + A_XT + lane * 16;
    LAS unsigned char* x1 = x0 + r1 * 1024;
    LAS unsigned char* x3 = x0 + r3 * 1024;
    LAS unsigned char* y0 = x0 + 12 * 5120;
    LAS unsigned char* y1 = x1 + 12 * 5120;
    LAS unsigned char* y3 = x3 + 12 * 5120;
#define XF(r, u) (*(LAS v4u*)(((u) < 12 ? ((r) == 0 ? x0 : (r) < 3 ? x1 : x3) : ((r) == 0 ? y0 : (r) < 3 ? y1 : y3)) + ((u) < 12 ? (u) : (u) - 12) * 5120 + ((r) == 2 || (r) == 4 ? 1024 : 0)))
#pragma unroll
    for (int r = 0; r < 5; ++r) xf[r] = XF(r, 0);
    SB();
    auto put = [&](int r) {
      const int rb5 = r == 0 ? 0 : r < 3 ? r1 + r - 1 : r3 + r - 3;
      if (r > 0) *(LAS v2u*)(L + A_Q + (w * 4 + rb5 - 1) * 512 + lane * 8) = pk4(qc[r]);
      *(LAS v2u*)(L + A_K + (w * 5 + rb5) * 512 + lane * 8) = pk4(kc[r]);
      *(LAS v2u*)(L + A_V + (w * 5 + rb5) * 512 + lane * 8) = pk4(vc[r]);
      if (r < 3) {
        const v2u v = pk4(ec[r]);
        LAS u16* p = (LAS u16*)(L + eoff + rb5 * 512);
        p[0] = (u16)v.x;
        p[estr] = (u16)(v.x >> 16);
        p[2 * estr] = (u16)v.y;
        p[3 * estr] = (u16)(v.y >> 16);
      }
      SB();
    };
#pragma unroll
    for (int u = 0; u < KS; ++u) {
      if (u == 3) lds_wait(cx + 1, four);
      if (u == 9) lds_wait(cx + 2, four);
      if (u == WOK) { if (lane == 0) __hip_atomic_fetch_add(cx + 3, 1u, RLX_WG); SB(); }
      if (u == KS - 1) lds_wait((LAS unsigned*)(L + L_CNT + 52), 4);
#pragma unroll
      for (int r = 0; r < 5; ++r) {
        M16(kc[r], wq[u % RQ][1], xf[r]);
        if (r < 4 && u + RQ - 1 < KS) { wq[(u + RQ - 1) % RQ][r] = wq_load(X, r, u + RQ - 1); SB(); }
        M16(vc[r], xf[r], wq[u % RQ][2]);
        if (r > 0) M16(qc[r], wq[u % RQ][0], xf[r]);
        if (r < 3) M16(ec[r], wq[u % RQ][3], xf[r]);
        if (u + 1 < KS) xf[r] = XF(r, u + 1);
        SB();
        if (u == KS - 1 && r >= 2) put(r - 2);
      }
    }
#undef XF
    put(3);
    MFMA_DONE();
    put(4);
    __builtin_amdgcn_s_setprio(0);
  }
  STAMP(X, layer * 32 + 3);
  BAR();
  STAMP(X, layer * 32 + 4);
  {
    const int c = lane & 15, gq = lane >> 4, rb = w & 3;
    typedef short s16x4 __attribute__((ext_vector_type(4)));
    auto mk = [](v2u a, v2u b, f32x4 cc) {
      return __builtin_amdgcn_mfma_f32_16x16x16bf16_1k(__builtin_bit_cast(s16x4, a), __builtin_bit_cast(s16x4, b), cc, 0, 0, 0);
    };
    LAS unsigned char* lp = L + rb * 512 + lane * 8;
    const v2u e0 = *(LAS v2u*)(lp + A_EQK), e1 = *(LAS v2u*)(lp + A_EQK + 512);
    v2u qm[2], k0[2], k1[2], v0[2], v1[2];
#pragma unroll
    for (int hh = 0; hh < 2; ++hh) {
      const int h = 2 * HI + hh;
      qm[hh] = *(LAS v2u*)(lp + A_Q + h * 4 * 512);
      k0[hh] = *(LAS v2u*)(lp + A_K + h * 5 * 512);
      k1[hh] = *(LAS v2u*)(lp + A_K + (h * 5 + 1) * 512);
    }
#pragma unroll
    for (int hh = 0; hh < 2; ++hh) {
      const int h = 2 * HI + hh;
      v0[hh] = *(LAS v2u*)(lp + A_V + h * 5 * 512);
      v1[hh] = *(LAS v2u*)(lp + A_V + (h * 5 + 1) * 512);
    }
    const v2u x0 = *(LAS v2u*)(lp + A_EV), x1 = *(LAS v2u*)(lp + A_EV + 512);
    SB();
    const bool first = (g == 0 && rb == 0);
    const bool par = c & 1;
    bool own[2], ok[2];
#pragma unroll
    for (int e = 0; e < 2; ++e) {
      own[e] = 4 * gq + 2 * e <= (c & 14);
      ok[e] = own[e] || !first;
    }
    const f32x4 z = {0, 0, 0, 0};
    f32x4 s0[2], s1[2];
#pragma unroll
    for (int hh = 0; hh < 2; ++hh) {
      s0[hh] = mk(k0[hh], qm[hh], z);
      s1[hh] = mk(k1[hh], qm[hh], z);
    }
#pragma unroll
    for (int hh = 0; hh < 2; ++hh) {
      const v2u qe = {0u, gq == 2 * HI + hh ? e1.x : 0u};
      s0[hh] = mk(e0, qe, s0[hh]);
      s1[hh] = mk(e1, qe, s1[hh]);
    }
    float sv[2][2], m[2], p[2][2], inv[2];
#pragma unroll
    for (int hh = 0; hh < 2; ++hh) {
      const float a0 = s0[hh].x, a1 = s0[hh].y, a2 = s0[hh].z, a3 = s0[hh].w, b0 = s1[hh].x, b1 = s1[hh].y, b2 = s1[hh].z, b3 = s1[hh].w;
      const float l0 = own[0] ? b0 : a0, h0 = own[0] ? b1 : a1, l1 = own[1] ? b2 : a2, h1 = own[1] ? b3 : a3;
      sv[hh][0] = ok[0] ? (par ? h0 : l0) : -3.0e38f;
      sv[hh][1] = ok[1] ? (par ? h1 : l1) : -3.0e38f;
      m[hh] = fmx(sv[hh][0], sv[hh][1]);
    }
    max1632x2(m[0], m[1]);
    constexpr float SC = 0.23570226039551584f * 1.4426950408889634f;
#pragma unroll
    for (int hh = 0; hh < 2; ++hh) {
      p[hh][0] = __builtin_amdgcn_exp2f((sv[hh][0] - m[hh]) * SC);
      p[hh][1] = __builtin_amdgcn_exp2f((sv[hh][1] - m[hh]) * SC);
      inv[hh] = p[hh][0] + p[hh][1];
    }
    sum1632x2(inv[0], inv[1]);
    f32x4 om[2], ox[2];
#pragma unroll
    for (int hh = 0; hh < 2; ++hh) {
      const float r = __builtin_amdgcn_rcpf(inv[hh]);
      const float q0 = p[hh][0] * r, q1 = p[hh][1] * r;
      const unsigned d0 = pk2(par ? 0.f : q0, par ? q0 : 0.f), d1 = pk2(par ? 0.f : q1, par ? q1 : 0.f);
      const v2u pp = {own[0] ? 0u : d0, own[1] ? 0u : d1}, po = {own[0] ? d0 : 0u, own[1] ? d1 : 0u};
      om[hh] = mk(v1[hh], po, mk(v0[hh], pp, z));
      ox[hh] = mk(x1, po, mk(x0, pp, z));
    }
    const int ob = OFF_O + (4 * g + rb) * KS * 1024;
    const v2u a = pk4(om[0]), b = pk4(om[1]);
    st128<SC1>(v4u{a.x, a.y, b.x, b.y}, rs, lane * 16, ob + (2 * j + HI) * 1024);
    if (gq == HI) st64<SC1>(v2u{pk2(ox[0].x, ox[0].y), pk2(ox[1].z, ox[1].w)}, rs, c * 8, ob + 16 * 1024 + (2 * j + HI) * 128);
  }
  STAMP(X, layer * 32 + 5);
  if (HI) {
#pragma unroll
    for (int i = 0; i < 23; ++i) *(LAS v4u*)(L + B_W + (i < 22 ? i * 4 + w - 4 : 88 + (w & 1)) * 1024 + lane * 16) = wt[i];
  }
  DRAIN();
  BAR();
  if (HI && w == 7 && lane < 2 - layer) signal(X.ws, lane ? flag_off(F_W, 0, j) + g * 8 : flag_off(F_A, layer, g) + j * 8, X.tag);
  STAMP(X, layer * 32 + 6);
}

template <int HI>
__device__ __forceinline__ void stage_b(const Ctx& X, int layer) {
  LAS unsigned char* L = X.L;
  const rsrc_t rs = X.rs;
  const int g = X.g, j = X.j, w = X.w, lane = X.lane;
  constexpr int kh = HI, NK = HI ? 2 : 3;
  const int rb = w & 3, c = lane & 15, gq = lane >> 4;
  const bool poller = HI && w == 7;
  const int xb = OFF_X2 + (4 * g + rb) * KS * 1024;
  const rsrc_t rx = __builtin_amdgcn_make_buffer_rsrc((void*)X.x, 0, S * D * 2, 0x00020000);
  int xo[NK];
  v2u res[NK];
#pragma unroll
  for (int i = 0; i < NK; ++i) {
    const int t = kh * 3 + i, d0 = 72 * j + 16 * t + 4 * gq;
    xo[i] = (d0 >> 5) * 1024 + (((d0 >> 3) & 3) * 16 + c) * 16 + ((d0 >> 2) & 1) * 8;
    res[i] = v2u{0, 0};
    if (t < 4 || gq < 2) {
      if (layer) res[i] = ld64<SC1>(rs, xo[i], xb);
      else res[i] = ld64<0>(rx, (c * D + d0) * 2, (4 * g + rb) * 16 * D * 2);
    }
  }
  SB();
  v4u wf[6][5];
#pragma unroll
  for (int u = 0; u < 6; ++u)
#pragma unroll
    for (int t = 0; t < 5; ++t) wf[u][t] = *(LAS v4u*)(L + B_W + ((kh * 9 + u) * 5 + t) * 1024 + lane * 16);
  SB();
  if (poller) {
    poll(X.ws, flag_off(F_A, layer, g) + (lane & 7) * 8, X.tag);
  }
  STAMP(X, layer * 32 + 7);
  BAR();
  STAMP(X, layer * 32 + 8);
  v4u of[9];
  const int ob = OFF_O + (4 * g + rb) * KS * 1024;
#pragma unroll
  for (int u = 0; u < (kh ? 7 : 9); ++u) of[u] = ld128<SC1>(rs, lane * 16 + (u & 3) * 1024, ob + kh * 9 * 1024 + (u >> 2) * 4096);
  if (kh) {
#pragma unroll
    for (int u = 7; u < 9; ++u) {
      const v2u p = ld64<SC1>(rs, gq * 256 + c * 8 + (u - 7) * 1024, ob + 16 * 1024);
      const v2u q = ld64<SC1>(rs, gq * 256 + c * 8 + (u - 7) * 1024 + 128, ob + 16 * 1024);
      of[u] = v4u{p.x, p.y, q.x, q.y};
    }
  }
  SB();
  f32x4 acc[5];
#pragma unroll
  for (int t = 0; t < 5; ++t) acc[t] = f32x4{0, 0, 0, 0};
#pragma unroll
  for (int u = 0; u < 9; ++u) {
#pragma unroll
    for (int t = 0; t < 5; ++t) {
      acc[t] = mfma16(wf[u % 6][t], of[u], acc[t]);
      if (u < 3) wf[u][t] = *(LAS v4u*)(L + B_W + ((kh * 9 + u + 6) * 5 + t) * 1024 + lane * 16);
    }
    SB();
  }
#pragma unroll
  for (int t = (kh ? 0 : 3); t < (kh ? 3 : 5); ++t) *(LAS f32x4*)(L + B_RED + (rb * 5 + t) * 1024 + lane * 16) = acc[t];
  STAMP(X, layer * 32 + 9);
  BAR();
  const int x1b = OFF_X1 + (4 * g + rb) * KS * 1024;
  float rsum = 0.f, rsq2 = 0.f;
#pragma unroll
  for (int i = 0; i < NK; ++i) {
    const int t = kh * 3 + i;
    const f32x4 o = *(LAS f32x4*)(L + B_RED + (rb * 5 + t) * 1024 + lane * 16);
    f32x4 v = o + acc[t];
    v.x += bflo(res[i].x); v.y += bfhi(res[i].x); v.z += bflo(res[i].y); v.w += bfhi(res[i].y);
    const v2u y = pk4(v);
    if (t < 4 || gq < 2) st64<SC1>(y, rs, xo[i], x1b);
    rsum = dot2(y.x, ONES, dot2(y.y, ONES, rsum));
    rsq2 = dot2(y.x, y.x, dot2(y.y, y.y, rsq2));
  }
  sum1632x2(rsum, rsq2);
  if (lane < 16) st64<SC1>(v2u{asu(rsum), asu(rsq2)}, rs, lane * 8, OFF_RS + ((4 * g + rb) * 16 + 2 * j + kh) * 128);
  STAMP(X, layer * 32 + 10);
  DRAIN();
  BAR();
  STAMP(X, layer * 32 + 11);
  if (poller) {
    if (lane == 0) signal(X.ws, flag_off(F_B, layer, g) + j * 8, X.tag);
    poll(X.ws, (lane >= 32 && layer == 0) ? flag_off(F_W, 0, j) + (lane & 31) * 8 : flag_off(F_B, layer, g) + (lane & 7) * 8, X.tag);
  }
  BAR();
  STAMP(X, layer * 32 + 12);
}

template <int NT, int BASE, int TOTAL, int LEAD, int R, int NB, typename HOOK>
__device__ __forceinline__ void c_gemm(LAS unsigned char* Xl, rsrc_t rs, int run, v4u (&a)[R], f32x4 (&acc)[NT][4], const f32x4 (&c0)[NB], int lane, HOOK hook) {
  constexpr int XB = NT > 2 ? 2 : 3;
  v4u xf[XB][4];
  LAS unsigned char* xp = Xl + lane * 16;
  LAS unsigned char* xq = xp + 2 * KS * 1024;
#define XF(r, u) (*(LAS v4u*)(((r) < 2 ? xp : xq) + (((r) & 1) * KS + (u)) * 1024))
#pragma unroll
  for (int b = 0; b < XB - 1; ++b)
#pragma unroll
    for (int r = 0; r < 4; ++r) xf[b][r] = XF(r, b);
  SB();
#pragma unroll
  for (int u = 0; u < KS; ++u) {
    hook(u);
#pragma unroll
    for (int t = 0; t < NT; ++t) {
      const int idx = BASE + u * NT + t;
#pragma unroll
      for (int r = 3; r >= 0; --r) {
        if (u == 0 && BASE == 0) m16c(acc[t][r], a[idx % R], xf[u % XB][r], c0[t % NB]);
        else if (u == 0) m16z(acc[t][r], a[idx % R], xf[u % XB][r]);
        else m16(acc[t][r], a[idx % R], xf[u % XB][r]);
        if (r >= 2 && t < 2 && u + XB - 1 < KS) { xf[(u + XB - 1) % XB][2 * t + 3 - r] = XF(2 * t + 3 - r, u + XB - 1); SB(); }
      }
      if (idx + LEAD < TOTAL) a[(idx + LEAD) % R] = ld128<SC1>(rs, lane * 16 + ((idx + LEAD) & 3) * 1024, run + ((idx + LEAD) >> 2) * 4096);
      SB();
    }
  }
#undef XF
  MFMA_DONE();
}

template <int HI>
__device__ __forceinline__ void stage_c(const Ctx& X, int layer, int run) {
  v4u a[ring<HI>()];
  constexpr int NT = 5 - HI, R = ring<HI>();
  LAS unsigned char* L = X.L;
  const rsrc_t rs = X.rs;
  const int g = X.g, j = X.j, w = X.w, lane = X.lane;
  const int c = lane & 15, gq = lane >> 4, rb = w & 3;
  const int T0 = HI ? 20 + 4 * (w - 4) : 5 * w;
  LAS unsigned* c_lo = (LAS unsigned*)(L + L_CNT + 20);
  LAS unsigned* c_hi = (LAS unsigned*)(L + L_CNT + 24);
  LAS unsigned* sig = (LAS unsigned*)(L + L_CNT + 32 + rb * 4);
  const unsigned four = 4 * (layer + 1);
  f32x4 bias[NT];
  {
    constexpr int U0 = HI ? XLO : 0, NU = HI ? KS - XLO : XLO;
    __builtin_amdgcn_s_setprio(HI ? 2 : 3);
    const int xb = OFF_X1 + ((4 * g + rb) * KS + U0) * 1024;
    v2u ps[4];
#pragma unroll
    for (int k = 0; k < 4; ++k) ps[k] = ld64<SC1>(rs, gq * 512 + k * 128 + c * 8, OFF_RS + (4 * g + rb) * 2048);
    v4u v[NU];
#pragma unroll
    for (int u = 0; u < NU; ++u) v[u] = ld128<SC1>(rs, lane * 16 + (u & 3) * 1024, xb + (u >> 2) * 4096);
    SB();
    if (!HI) ring_fill<0, CPF>(X, run, a);
#pragma unroll
    for (int t = 0; t < NT; ++t) bias[t] = *(LAS f32x4*)(L + L_CB + ((T0 + t) * 16 + 4 * gq) * 4);
    float s = (asf(ps[0].x) + asf(ps[1].x)) + (asf(ps[2].x) + asf(ps[3].x)), q = (asf(ps[0].y) + asf(ps[1].y)) + (asf(ps[2].y) + asf(ps[3].y));
    sum1632x2(s, q);
    const float mean = s * (1.f / D);
    const float var = fmx(q * (1.f / D) - mean * mean, 0.f);
    const float rstd = rsq(var + 1e-5f), nm = -mean * rstd;
#pragma unroll
    for (int u = 0; u < NU; ++u) {
      v4u o;
#pragma unroll
      for (int p = 0; p < 4; ++p) o[p] = pk2(__builtin_fmaf(bflo(v[u][p]), rstd, nm), __builtin_fmaf(bfhi(v[u][p]), rstd, nm));
      *(LAS v4u*)(L + C_X + (rb * KS + U0 + u) * 1024 + lane * 16) = o;
    }
    LDS_ARRIVE(HI ? c_hi : c_lo);
    STAMP(X, layer * 32 + 16);
    ring_fill<HI ? 0 : CPF, CPRE>(X, run, a);
    __builtin_amdgcn_s_setprio(0);
    lds_wait(c_lo, four);
    if (HI) {
      lds_wait(c_hi, four);
      if (SIGK >= 0) lds_wait(sig, layer + 1);
    }
    STAMP(X, layer * 32 + 17);
  }
  {
  f32x4 acc[NT][4];
  c_gemm<NT, 0, 2 * KS * NT, CPRE>(L + C_X, rs, run, a, acc, bias, lane, [&](int u) {
    if (!HI && u == XLO - 1) lds_wait(c_hi, four);
    if (!HI && u == SIGK) { __hip_atomic_store(sig, (unsigned)(layer + 1), RLX_WG); SB(); }
  });
  STAMP(X, layer * 32 + 19);
  v4u warm[WARM ? WARM : 1];
  if (WARM && HI && layer == 0) {
#pragma unroll
    for (int k = 0; k < WARM; ++k) {
      const int f = cmin(((g % (32 / RA)) * 4 + rb) * WARM + k, 341);
      warm[k] = ld128<SC1>(rs, lane * 16, f < 252 ? OFF_WQ + (j * 252 + f) * 1024 : OFF_WO + (j * 90 + f - 252) * 1024);
    }
#pragma unroll
    for (int k = 0; k < WARM; ++k) asm volatile("" ::"v"(warm[k]));
  }
  {
#pragma unroll
    for (int t = 0; t < NT; ++t) {
      const int T = T0 + t;
#pragma unroll
      for (int r = 0; r < 4; ++r) {
        float o[4];
#pragma unroll
        for (int e = 0; e < 4; ++e) {
          const float v = acc[t][r][e];
          const float p = __builtin_fmaf(v * v, -0.10294324f, -2.3022082f);
          const float ex = __builtin_amdgcn_exp2f(v * p);
          o[e] = v * __builtin_amdgcn_rcpf(1.0f + ex);
        }
        *(LAS v2u*)(L + C_H + (r * KS + (T >> 1)) * 1024 + ((2 * (T & 1) + (gq >> 1)) * 16 + c) * 16 + (gq & 1) * 8) = v2u{pk2(o[0], o[1]), pk2(o[2], o[3])};
#pragma unroll
        for (int k = CPRE; k < R; ++k)
          if ((k - CPRE) * (4 * NT) / (R > CPRE ? R - CPRE : 1) == t * 4 + r) {
            const int l = KS * NT + k;
            a[l % R] = ld128<SC1>(rs, lane * 16 + (l & 3) * 1024, run + (l >> 2) * 4096);
          }
        SB();
      }
    }
  }
  }
  STAMP(X, layer * 32 + 20);
  if (HI) {
    BAR();
  } else {
    LDS_ARRIVE((LAS unsigned*)(L + L_CNT + 8));
    lds_wait((LAS unsigned*)(L + L_CNT + 8), four);
  }
  STAMP(X, layer * 32 + 21);
  auto put = [&](auto& acc, auto np, int T) {
    constexpr int NP = decltype(np)::value;
#pragma unroll
    for (int r = 0; r < 4; ++r) {
      const int cn = 2 * r + (c >> 3), in = (c & 7) * 32 + gq * 8;
      if (cn != j) {
#pragma unroll
        for (int t = 0; t < NP; ++t) st64<SC1>(pk4(acc[t][r]), rs, (cn * 8 + ((j - cn - 1) & 7)) * 9216 + in + t * 256, OFF_P + g * 64 * 9216 + T * 256);
      } else {
#pragma unroll
        for (int t = 0; t < NP; ++t) *(LAS v2u*)(L + C_OWN + (T + t) * 256 + in) = pk4(acc[t][r]);
      }
    }
    SB();
  };
  auto publish = [&](int part) {
    SB();
    asm volatile("s_waitcnt vmcnt(0) lgkmcnt(0)" ::: "memory");
    if (lane == 0) {
      const unsigned old = __hip_atomic_fetch_add((LAS unsigned*)(L + L_CNT + (part == 2 ? 56 : part * 4)), 1u, RLX_WG);
      if ((old & 3u) == 3u) signal(X.ws, flag_off(F_C1 + part, layer, g) + j * 8, X.tag);
    }
    SB();
  };
  if (!HI) {
    f32x4 acc[5][4];
    c_gemm<5, KS * NT, 2 * KS * NT, R>(L + C_H, rs, run, a, acc, bias, lane, [&](int u) {
      if (u == 9) BAR();
    });
    STAMP(X, layer * 32 + 22);
    put(acc, IC<5>(), 5 * w);
    STAMP(X, layer * 32 + 23);
    publish(0);
  } else {
    f32x4 acc[4][4];
    c_gemm<4, KS * NT, 2 * KS * NT, R>(L + C_H, rs, run, a, acc, bias, lane, [&](int u) {});
    STAMP(X, layer * 32 + 22);
    put(acc, IC<4>(), 20 + 4 * rb);
    STAMP(X, layer * 32 + 23);
    publish(1);
  }
  STAMP(X, layer * 32 + 24);
}

template <int HI, int LAYER, typename PRE>
__device__ __forceinline__ void stage_d(const Ctx& X, PRE pre) {
  constexpr int layer = LAYER;
  LAS unsigned char* L = X.L;
  const rsrc_t rs = X.rs;
  const int g = X.g, j = X.j, w = X.w, lane = X.lane;
  const int pb = OFF_P + (g * 8 + j) * 8 * 9216;
  const int RB = 4 * g + (j >> 1);
  const int tok = (lane >> 1) & 7, hf = lane & 1;
  constexpr int I0 = HI ? 2 : 0;
  int fo[3], pp[3];
  v4u xr[3], bv[3], o[3];
  pp[0] = w * 64 + lane;
  pp[1] = 256 + w * 16 + (lane & 15);
  pp[2] = 320 + w * 32 + (lane & 31);
  constexpr int NL[3] = {64, 16, 32};
#pragma unroll
  for (int i = I0; i < 3; ++i) {
    const int T = pp[i] >> 4;
    fo[i] = (T >> 1) * 1024 + ((2 * (T & 1) + hf) * 16 + (j & 1) * 8 + tok) * 16;
    xr[i] = ld128<SC1>(rs, fo[i], OFF_X1 + RB * KS * 1024);
    bv[i] = ld128<0>(X.rb2, (T * 16 + hf * 8) * 2, 0);
  }
  SB();
  float s = 0.f, q = 0.f;
  v4u pv[3][8], own[3];
  auto fetch = [&](int i) {
    const int per = NL[i] / 8, grp = lane / NL[i];
#pragma unroll
    for (int k = 0; k < 8; ++k)
      if (k < per) {
        if (k < per - 1) pv[i][k] = ld128<SC1>(rs, pp[i] * 16 + grp * per * 9216, pb + k * 9216);
        else if (per < 8) pv[i][k] = ld128<SC1>(rs, pp[i] * 16 + (grp * per + (lane < 64 - NL[i] ? k : k - 1)) * 9216, pb);
      }
    own[i] = *(LAS v4u*)(L + C_OWN + pp[i] * 16);
    SB();
  };
  auto finish = [&](int i) {
    const int per = NL[i] / 8;
#pragma unroll
    for (int e = 0; e < 4; ++e) {
      pv[i][per - 1][e] = (per == 8 || lane >= 64 - NL[i]) ? own[i][e] : pv[i][per - 1][e];
      float l2 = 0.f, h2 = 0.f;
#pragma unroll
      for (int k = 0; k < 8; ++k)
        if (k < per) { l2 += bflo(pv[i][k][e]); h2 += bfhi(pv[i][k][e]); }
      if (NL[i] == 32) { l2 = sum32(l2); h2 = sum32(h2); }
      if (NL[i] == 16) sum1632x2(l2, h2);
      o[i][e] = pk2(bflo(bv[i][e]) + bflo(xr[i][e]) + l2, bfhi(bv[i][e]) + bfhi(xr[i][e]) + h2);
    }
    if (lane < NL[i]) {
      if (layer == 0) st128<SC1>(o[i], rs, fo[i], OFF_X2 + RB * KS * 1024);
      else st128<OUTAUX>(o[i], X.rout, ((g * 64 + j * 8 + tok) * D + (pp[i] >> 4) * 16 + hf * 8) * 2, 0);
      if (layer == 0) {
#pragma unroll
        for (int e = 0; e < 4; ++e) {
          const float lo = bflo(o[i][e]), hi = bfhi(o[i][e]);
          s += lo + hi;
          q = __builtin_fmaf(lo, lo, __builtin_fmaf(hi, hi, q));
        }
      }
    }
    SB();
  };
  if (!HI) {
    poll(X.ws, flag_off(F_C1, layer, g) + (lane & 7) * 8, X.tag);
    STAMP(X, layer * 32 + 25);
    fetch(0);
    fetch(1);
    finish(0);
    finish(1);
    pre();
  }
  STAMP(X, layer * 32 + 26);
  if (HI && w == 7) poll(X.ws, flag_off(F_C2, layer, g) + (lane & 7) * 8, X.tag);
  BAR();
  STAMP(X, layer * 32 + 27);
  fetch(2);
  finish(2);
  STAMP(X, layer * 32 + 28);
  if (layer == 0) {
    s += asf(__builtin_amdgcn_mov_dpp(asu(s), 0xB1, 0xF, 0xF, true));
    q += asf(__builtin_amdgcn_mov_dpp(asu(q), 0xB1, 0xF, 0xF, true));
    sum1632x2(s, q);
    if (lane < 16 && hf == 0) *(LAS f32x2*)(L + D_ST + (tok * 8 + w) * 8) = f32x2{s, q};
    BAR();
    float ts = 0.f, tq = 0.f;
#pragma unroll
    for (int k = 0; k < 4; ++k) {
      const f32x4 v = *(LAS f32x4*)(L + D_ST + tok * 64 + k * 16);
      ts += v.x + v.z;
      tq += v.y + v.w;
    }
    const float mean = ts * (1.f / D);
    const float var = fmx(tq * (1.f / D) - mean * mean, 0.f);
    const float rstd = rsq(var + 1e-5f), nm = -mean * rstd;
#pragma unroll
    for (int i = I0; i < 3; ++i)
      if (lane < NL[i]) {
        v4u y;
#pragma unroll
        for (int e = 0; e < 4; ++e) y[e] = pk2(__builtin_fmaf(bflo(o[i][e]), rstd, nm), __builtin_fmaf(bfhi(o[i][e]), rstd, nm));
        st128<SC1>(y, rs, fo[i], OFF_XN + XBUF + RB * KS * 1024);
      }
    SB();
  }
}

template <int HI>
__device__ __forceinline__ void body(Ctx& X) {
  const int run0 = OFF_WC + (X.j * 1296 + (HI ? 720 + (X.w - 4) * 144 : X.w * 180)) * 1024;
  v4u wq[RQ][4];
  asm volatile("; role %0" ::"i"(HI) : "memory");
#pragma unroll
  for (int u = 0; u < RQ; ++u)
#pragma unroll
    for (int b = 0; b < 4; ++b) wq[u][b] = v4u{0, 0, 0, 0};
#pragma unroll 1
  for (int layer = 0;; ++layer) {
    stage_a<HI>(fresh(X), layer, wq);
    stage_b<HI>(fresh(X), layer);
    int run = run0;
    asm volatile("" : "+s"(run));
    stage_c<HI>(fresh(X), layer, run);
    if (layer == 1) break;
    if (!HI && !LOLATE) wq_prefetch(fresh(X), wq);
    stage_d<HI, 0>(fresh(X), [&] { if (!HI && LOLATE) wq_prefetch(fresh(X), wq); });
    DRAIN();
    STAMP(X, 29);
    BAR();
    if (HI && X.w == 7) {
      if (X.lane == 0) signal(X.ws, flag_off(F_D, 1, X.g) + X.j * 8, X.tag);
      if (BIDX == 0 && X.lane == 1) signal(X.ws, 0, X.tag);
      poll(X.ws, (X.lane >= 32 && X.g > 0) ? flag_off(F_D, 1, X.g - 1) + (6 + (X.lane & 1)) * 8 : flag_off(F_D, 1, X.g) + (X.lane & 7) * 8, X.tag);
    }
    BAR();
    STAMP(X, 32);
  }
  stage_d<HI, 1>(fresh(X), [] {});
  STAMP(X, 32 + 29);
}

}

extern "C" __global__ void __launch_bounds__(NTHR, 2) ilk_main(unsigned char* ws, const u16* x, const u16* wqkv, const u16* wo, const u16* w1, const u16* w2,
                                                  const u16* b1, const u16* b2, u16* out) {
  extern __shared__ __attribute__((aligned(16))) unsigned char smem[];
  Ctx X;
  X.ws = ws;
  X.x = x;
  X.wqkv = wqkv;
  X.wo = wo;
  X.w1 = w1;
  X.w2 = w2;
  X.b1 = b1;
  X.L = (LAS unsigned char*)smem;
  X.rs = __builtin_amdgcn_make_buffer_rsrc((void*)ws, 0, WS_BYTES, 0x00020000);
  X.rb2 = __builtin_amdgcn_make_buffer_rsrc((void*)b2, 0, D * 2, 0x00020000);
  X.rout = __builtin_amdgcn_make_buffer_rsrc((void*)out, 0, S * D * 2, 0x00020000);
  X.tag = 0;
  X.lane = TIDX & 63;
  X.w = __builtin_amdgcn_readfirstlane(TIDX >> 6);
  const int xc = BIDX & 7, i = BIDX >> 3;
  X.g = (xc % RA) * (32 / RA) + i / RA;
  X.j = (xc / RA) * RA + i % RA;
  STAMP(X, 0);
  {
    u64 pc;
    asm volatile("s_getpc_b64 s[20:21]\n\ts_add_u32 s20, s20, ilk_main@rel32@lo+4\n\ts_addc_u32 s21, s21, ilk_main@rel32@hi+12\n\ts_mov_b64 %0, s[20:21]"
                 : "=s"(pc) : : "scc", "s20", "s21");
    const v4u c0 = *((const GAS v4u*)pc + TIDX);
    SB();
    const u64 e = __hip_atomic_load((gu64*)X.ws, RLX_AGENT);
    if (TIDX < 32) *(LAS unsigned*)(X.L + L_CNT + TIDX * 4) = 0u;
    BAR();
    unsigned bw = 0;
    if (TIDX < 288) bw = *(const GAS unsigned*)(X.b1 + X.j * 576 + TIDX * 2);
    if (X.w >= 3) __builtin_amdgcn_s_setprio(3);
    if (__builtin_expect(X.w > 3, 1)) qo_tile(X);
    asm volatile("" ::: "memory");
    if (__builtin_expect(X.w == 3, 1)) x0_rows(X);
    asm volatile("" ::: "memory");
    if (X.w >= 3) DRAIN();
    if (__builtin_expect(X.w < 3, 1)) {
      {
        const v4u t = *((const GAS v4u*)pc + ((8 + cmin(i * 3 + X.w, CODE_KB - 9)) * 64 + X.lane));
        asm volatile("" ::"v"(t));
      }
      __builtin_amdgcn_s_sleep(PSLEEP);
      const int t0 = rt_task(X.g, X.w), t1 = rt_task(X.g, X.w + 3);
      v4u r0[8], r1[8];
      rt_load(X, t0, r0);
#pragma unroll
      for (int k = 0; k < 8; ++k) r1[k] = v4u{0, 0, 0, 0};
      if (t1 >= 0) rt_load(X, t1, r1);
      rt_turn(X, t0, X.w, r0);
      if (t1 >= 0) rt_turn(X, t1, X.w + 3, r1);
    }
    if (TIDX < 288) *(LAS f32x2*)(X.L + L_CB + TIDX * 8) = f32x2{bflo(bw), bfhi(bw)};
    asm volatile("" ::"v"(c0));
    const unsigned elo = __builtin_amdgcn_readfirstlane((unsigned)e), ehi = __builtin_amdgcn_readfirstlane((unsigned)(e >> 32));
    X.tag = (((u64)ehi << 32) | elo) + 0x9E3779B97F4A7C15ull;
    STAMP(X, 30);
    if (X.w >= 3) {
      unsigned old = 0;
      if (X.lane == 0) old = __hip_atomic_fetch_add((LAS unsigned*)(X.L + L_CNT + 12), 1u, RLX_WG);
      if (__builtin_amdgcn_readfirstlane(old) == 4u) {
        if (X.lane < 2) signal(X.ws, X.lane ? flag_off(F_Q, 0, X.j) + X.g * 8 : flag_off(F_D, 0, X.g) + X.j * 8, X.tag);
        poll(X.ws, X.lane >= 32 ? flag_off(F_Q, 0, X.j) + (X.lane & 31) * 8
                                : (X.lane >= 16 && X.g > 0) ? flag_off(F_D, 0, X.g - 1) + (6 + (X.lane & 1)) * 8 : flag_off(F_D, 0, X.g) + (X.lane & 7) * 8, X.tag);
      }
      __builtin_amdgcn_s_setprio(0);
    }
    BAR();
    STAMP(X, 31);
  }
  if (X.w < 4) body<0>(X);
  else body<1>(X);
}
#if defined(__HIP_DEVICE_COMPILE__)
#pragma clang attribute pop
#endif

extern "C" void kernel_launch(void* const* d_in, const int* in_sizes, int n_in, void* d_out, int out_size, void* d_ws, size_t ws_size,
                              hipStream_t stream) {
  (void)hipFuncSetAttribute((const void*)ilk_main, hipFuncAttributeMaxDynamicSharedMemorySize, LDS_BYTES);
  ilk_main<<<256, NTHR, LDS_BYTES, stream>>>((unsigned char*)d_ws, (const u16*)d_in[0], (const u16*)d_in[1], (const u16*)d_in[2], (const u16*)d_in[4],
                                           (const u16*)d_in[6], (const u16*)d_in[5], (const u16*)d_in[7], (u16*)d_out);
}
```

```cpp
#include <hip/hip_runtime.h>
#include <hip/hip_bf16.h>

#if defined(__HIP_DEVICE_COMPILE__)
#pragma clang attribute push(__attribute__((target("no-packed-fp32-ops"))), apply_to = function)
#endif
namespace {
typedef unsigned short u16;
typedef unsigned v4u __attribute__((ext_vector_type(4)));
typedef unsigned v2u __attribute__((ext_vector_type(2)));
typedef float f32x4 __attribute__((ext_vector_type(4)));
typedef float f32x2 __attribute__((ext_vector_type(2)));
typedef __bf16 bf16x2_t __attribute__((ext_vector_type(2)));
typedef __bf16 bf16x8_t __attribute__((ext_vector_type(8)));
#define GAS __attribute__((address_space(1)))
#define LAS __attribute__((address_space(3)))
typedef __amdgpu_buffer_rsrc_t rsrc_t;

constexpr int S = 2048, D = 576, F = 4608;
constexpr int KS = 18;
constexpr int NTHR = 512;
constexpr int XBUF = 128 * KS * 1024;
constexpr int OFF_RS = 2 * 65536;
constexpr int OFF_WQ = OFF_RS + 128 * 2048;
constexpr int OFF_WO = OFF_WQ + 8 * 14 * KS * 1024;
constexpr int OFF_WC = OFF_WO + 8 * 90 * 1024;
constexpr int OFF_XN = OFF_WC + 8 * 1296 * 1024;
constexpr int OFF_O = OFF_XN + 2 * XBUF;
constexpr int OFF_X1 = OFF_O + XBUF;
constexpr int OFF_X2 = OFF_X1 + XBUF;
constexpr int OFF_P = OFF_X2 + XBUF;
constexpr int WS_BYTES = OFF_P + 8 * XBUF;

__device__ __forceinline__ f32x4 mfma16(v4u a, v4u b, f32x4 c) {
  return __builtin_amdgcn_mfma_f32_16x16x32_bf16(__builtin_bit_cast(bf16x8_t, a), __builtin_bit_cast(bf16x8_t, b), c, 0, 0, 0);
}
__device__ __forceinline__ void m16(f32x4& c, v4u a, v4u b) { asm volatile("v_mfma_f32_16x16x32_bf16 %0, %1, %2, %0" : "+v"(c) : "v"(a), "v"(b)); }
__device__ __forceinline__ void m16z(f32x4& c, v4u a, v4u b) { asm volatile("v_mfma_f32_16x16x32_bf16 %0, %1, %2, 0" : "=&v"(c) : "v"(a), "v"(b)); }
__device__ __forceinline__ void m16c(f32x4& d, v4u a, v4u b, f32x4 c) { asm volatile("v_mfma_f32_16x16x32_bf16 %0, %1, %2, %3" : "=&v"(d) : "v"(a), "v"(b), "v"(c)); }
#define M16(c, a, b) do { if (u == 0) m16z(c, a, b); else m16(c, a, b); } while (0)
__device__ __forceinline__ unsigned pk2(float lo, float hi) {
  f32x2 v = {lo, hi};
  return __builtin_bit_cast(unsigned, __builtin_convertvector(v, bf16x2_t));
}
__device__ __forceinline__ float asf(unsigned u) { return __builtin_bit_cast(float, u); }
__device__ __forceinline__ unsigned asu(float f) { return __builtin_bit_cast(unsigned, f); }
__device__ __forceinline__ float bflo(unsigned u) { return asf(u << 16); }
__device__ __forceinline__ float bfhi(unsigned u) { return asf(u & 0xffff0000u); }
__device__ __forceinline__ v2u pk4(f32x4 v) { return v2u{pk2(v.x, v.y), pk2(v.z, v.w)}; }
__device__ __forceinline__ float dot2(unsigned a, unsigned b, float c) {
  return __builtin_amdgcn_fdot2_f32_bf16(__builtin_bit_cast(bf16x2_t, a), __builtin_bit_cast(bf16x2_t, b), c, false);
}
constexpr unsigned ONES = 0x3f803f80u;

template <int AUX> __device__ __forceinline__ v4u ld128(rsrc_t rs, int voff, int soff) { return __builtin_amdgcn_raw_buffer_load_b128(rs, voff, soff, AUX); }
template <int AUX> __device__ __forceinline__ v2u ld64(rsrc_t rs, int voff, int soff) { return __builtin_amdgcn_raw_buffer_load_b64(rs, voff, soff, AUX); }
template <int AUX> __device__ __forceinline__ void st128(v4u v, rsrc_t rs, int voff, int soff) { __builtin_amdgcn_raw_buffer_store_b128(v, rs, voff + soff, 0, AUX); }
template <int AUX> __device__ __forceinline__ void st64(v2u v, rsrc_t rs, int voff, int soff) { __builtin_amdgcn_raw_buffer_store_b64(v, rs, voff, soff, AUX); }
#define SB() __builtin_amdgcn_sched_barrier(0)
#define MFMA_DONE() do { SB(); asm volatile("s_nop 7\n\ts_nop 1" ::: "memory"); SB(); } while (0)
#define TIDX ((int)__builtin_amdgcn_workitem_id_x())
#define BIDX ((int)__builtin_amdgcn_workgroup_id_x())
__device__ __forceinline__ float fmx(float a, float b) { return __builtin_fmaxf(a, b); }
__device__ __forceinline__ float rsq(float a) { return __builtin_amdgcn_rsqf(a); }

__device__ __forceinline__ float sum32(float v) {
  const auto b = __builtin_amdgcn_permlane32_swap(asu(v), asu(v), false, false);
  const unsigned b0 = b[0], b1 = b[1];
  return asf(b0) + asf(b1);
}

template <typename OP> __device__ __forceinline__ void red1632x2(float& a, float& b, OP op) {
  const auto s = __builtin_amdgcn_permlane16_swap(asu(a), asu(b), false, false);
  const unsigned s0 = s[0], s1 = s[1];
  const float t = op(asf(s0), asf(s1));
  const auto u = __builtin_amdgcn_permlane32_swap(asu(t), asu(t), false, false);
  const unsigned u0 = u[0], u1 = u[1];
  const float v = op(asf(u0), asf(u1));
  const auto x = __builtin_amdgcn_permlane16_swap(asu(v), asu(v), false, false);
  const unsigned x0 = x[0], x1 = x[1];
  a = asf(x0);
  b = asf(x1);
}
__device__ __forceinline__ void max1632x2(float& a, float& b) { red1632x2(a, b, [](float x, float y) { return fmx(x, y); }); }
__device__ __forceinline__ void sum1632x2(float& a, float& b) { red1632x2(a, b, [](float x, float y) { return x + y; }); }

__device__ __forceinline__ void tr8x8(const v4u (&r)[8], v4u (&o)[8]) {
#pragma unroll
  for (int c = 0; c < 4; ++c) {
#pragma unroll
    for (int p = 0; p < 4; ++p) {
      o[2 * c][p] = __builtin_amdgcn_perm(r[2 * p + 1][c], r[2 * p][c], 0x05040100u);
      o[2 * c + 1][p] = __builtin_amdgcn_perm(r[2 * p + 1][c], r[2 * p][c], 0x07060302u);
    }
  }
}
constexpr int RA = 2;
constexpr int RQ = 4;
constexpr int CPRE = 12;
constexpr int CPF = 5;
constexpr int XLO = 6;
constexpr int SIGK = 12;
constexpr int WOK = 15;
constexpr int NT_HINT = 2;
#define CODE_KB 60
template <int HI> constexpr int ring() { return HI ? 32 : 12; }
constexpr int cmin(int a, int b) { return a < b ? a : b; }
template <int N> struct IC { static constexpr int value = N; };
constexpr int SC1 = 16;
constexpr int A_XT = 0;
constexpr int A_Q = 5 * KS * 1024;
constexpr int A_K = A_Q + 16 * 512;
constexpr int A_V = A_K + 20 * 512;
constexpr int A_EQK = A_V + 20 * 512;
constexpr int A_EV = A_EQK + 5 * 512;
constexpr int B_W = 0, B_RED = 124 * 1024;
constexpr int C_X = 0, C_OWN = 0, C_H = 4 * KS * 1024;
constexpr int L_CB = 8 * KS * 1024;
constexpr int D_ST = L_CB + 576 * 4;
constexpr int L_CNT = D_ST + 512;
constexpr int LDS_BYTES = L_CNT + 128;
enum { F_A = 0, F_B = 1, F_C1 = 2, F_C2 = 3, F_D = 4, F_Q = 5, F_W = 6 };
typedef unsigned long long u64;
typedef GAS u64 gu64;
__device__ __forceinline__ int flag_off(int kind, int layer, int g) { return 4096 + ((kind * 2 + layer) * 32 + g) * 256; }
#define RLX_AGENT __ATOMIC_RELAXED, __HIP_MEMORY_SCOPE_AGENT
#define RLX_WG __ATOMIC_RELAXED, __HIP_MEMORY_SCOPE_WORKGROUP
#define BAR() do { SB(); asm volatile("s_waitcnt lgkmcnt(0)\n\ts_barrier" ::: "memory"); SB(); } while (0)
#define DRAIN() do { SB(); asm volatile("s_waitcnt vmcnt(0)" ::: "memory"); SB(); } while (0)
__device__ __forceinline__ void poll(unsigned char* ws, int off, u64 tag) {
  gu64* p = (gu64*)(ws + off);
#pragma unroll 1
  for (unsigned spins = 0; spins < (1u << 19); ++spins) {
    const u64 v = __hip_atomic_load(p, RLX_AGENT);
    if (__builtin_amdgcn_ballot_w64(v != tag) == 0) break;
    __builtin_amdgcn_s_sleep(1);
  }
  __builtin_amdgcn_fence(__ATOMIC_ACQUIRE, "wavefront");
  asm volatile("" ::: "memory");
}
__device__ __forceinline__ void signal(unsigned char* ws, int off, u64 tag) { __hip_atomic_store((gu64*)(ws + off), tag, RLX_AGENT); }
__device__ __forceinline__ void lds_wait(LAS unsigned* p, unsigned want) {
#pragma unroll 1
  for (unsigned spins = 0; spins < (1u << 23); ++spins) {
    const unsigned v = __builtin_amdgcn_readfirstlane(__hip_atomic_load(p, RLX_WG));
    if (v >= want) break;
    __builtin_amdgcn_s_sleep(1);
  }
  asm volatile("" ::: "memory");
  SB();
}

#define LDS_ARRIVE(p) do { SB(); asm volatile("s_waitcnt lgkmcnt(0)" ::: "memory"); if (lane == 0) __hip_atomic_fetch_add((p), 1u, RLX_WG); SB(); } while (0)

struct Ctx {
  unsigned char* ws;
  const u16* x;
  const u16* wqkv;
  const u16* wo;
  const u16* w1;
  const u16* w2;
  const u16* b1;
  LAS unsigned char* L;
  rsrc_t rs, rb2, rout;
  u64 tag;
  int g, j, w, lane;
};
__device__ __forceinline__ Ctx fresh(const Ctx& X) {
  Ctx Y = X;
  asm volatile("" : "+v"(Y.lane));
  asm volatile("" : "+s"(Y.g), "+s"(Y.j), "+s"(Y.w));
  return Y;
}

__device__ __forceinline__ int rt_task(int g, int q) { return q < 5 ? g * 5 + q : (g < 2 ? 160 + g : -1); }
__device__ __forceinline__ void rt_load(const Ctx& X, int task, v4u (&r)[8]) {
  const bool is1 = task < 81;
  const int tt = is1 ? task : task - 81, chunk = tt / 9, strip = tt - 9 * chunk;
  const u16* src = is1 ? X.w1 + (size_t)chunk * 64 * F + X.j * 576 + strip * 64 : X.w2 + (size_t)(X.j * 576 + chunk * 64) * D + strip * 64;
  const int ld = is1 ? F : D;
  const rsrc_t rw = __builtin_amdgcn_make_buffer_rsrc((void*)src, 0, (63 * ld + 64) * 2, 0x00020000);
  const int vo = ((X.lane >> 3) * 8 * ld + (X.lane & 7) * 8) * 2;
#pragma unroll
  for (int jj = 0; jj < 8; ++jj) r[jj] = ld128<NT_HINT>(rw, vo, jj * ld * 2);
  SB();
}
__device__ __forceinline__ void rt_turn(const Ctx& X, int q, const v4u (&r)[8]) {
  const int lane = X.lane, kg = lane >> 3, nb = lane & 7;
  LAS unsigned char* sm = X.L + A_Q + q * 8192;
  v4u o[8];
  tr8x8(r, o);
  const int base = (((kg >> 2) * 4 + (nb >> 1)) * 64 + (kg & 3) * 16 + (nb & 1) * 8) * 16;
#pragma unroll
  for (int c = 0; c < 8; ++c) *(LAS v4u*)(sm + base + ((c ^ nb) * 16)) = o[c];
  SB();
}
__device__ __forceinline__ v4u rt_get(const Ctx& X, int q, int f) {
  const int lane = X.lane;
  return *(LAS v4u*)(X.L + A_Q + q * 8192 + (f * 64 + (lane & ~7) + ((lane & 7) ^ ((f & 3) * 2 + ((lane >> 3) & 1)))) * 16);
}
__device__ __forceinline__ void rt_put(const Ctx& X, int task, int f, v4u v) {
  const bool is1 = task < 81;
  const int tt = is1 ? task : task - 81, chunk = (tt * 57) >> 9, strip = tt - 9 * chunk;
  const int T = strip * 4 + (f & 3), lo = strip < 5;
  const int ks = chunk * 2 + (f >> 2);
  int o;
  if (lo) o = ((T * 13) >> 6) * 175 + T + ((is1 ? 0 : KS) + ks) * 5;
  else o = strip * 144 + (f & 3) + ((is1 ? 0 : KS) + ks) * 4;
  const int fr = X.j * 1296 + o;
  st128<SC1>(v, X.rs, X.lane * 16, OFF_WC + fr * 1024);
}

__device__ __forceinline__ void qo_tile(const Ctx& X) {
  constexpr int NQ = 2;
  const rsrc_t rq_ = __builtin_amdgcn_make_buffer_rsrc((void*)X.wqkv, 0, D * 3 * D * 2, 0x00020000);
  const int j = X.j;
  unsigned e[NQ][8];
  int dst[NQ];
  bool zero[NQ];
#pragma unroll
  for (int i = 0; i < NQ; ++i) {
    const int q = i * 256 + (X.w - 4) * 64 + X.lane, P = X.g * 504 + (q < 504 ? q : 503);
    const int kg = ((P >> 4) * 4682) >> 16, blk = (P >> 4) - kg * 14, u = kg >> 2, gq = kg & 3, m = P & 15;
    const int ty = blk < 12 ? blk >> 2 : blk == 12 ? (m >> 1) & 1 : 2;
    const int hd = blk < 12 ? blk & 3 : blk == 12 ? m >> 2 : (m >> 1) & 3;
    const int col = ty * D + (4 * j + hd) * 18 + (blk < 12 ? m : 16 + (m & 1));
    zero[i] = blk == 13 && m >= 8;
    dst[i] = OFF_WQ + (((j * 14 + blk) * KS + u) * 64 + gq * 16 + m) * 16;
    const int vo = ((u * 32 + gq * 8) * 3 * D + col) * 2;
#pragma unroll
    for (int jj = 0; jj < 8; ++jj) e[i][jj] = __builtin_amdgcn_raw_buffer_load_b16(rq_, vo, jj * 3 * D * 2, 0);
  }
  const rsrc_t ro_ = __builtin_amdgcn_make_buffer_rsrc((void*)X.wo, 0, D * D * 2, 0x00020000);
  const int t = (X.w - 4) * 64 + X.lane;
  const int P = X.g * 180 + (t < 180 ? t : 179);
  const int kg = ((P >> 4) * 13108) >> 16, tl = (P >> 4) - kg * 5, u = kg >> 2, gq = kg & 3, m = P & 15;
  const bool zo = tl * 16 + m >= 72;
  const int col = j * 72 + (zo ? 0 : tl * 16 + m);
  const int r0 = u < 16 ? 36 * u + 4 * gq : 72 * (4 * (u - 16) + gq) + 16;
  unsigned eo[8];
#pragma unroll
  for (int jj = 0; jj < 8; ++jj) {
    const int row = r0 + (u < 16 ? (jj >> 2) * 18 + (jj & 3) : (jj >> 1) * 18 + (jj & 1));
    eo[jj] = __builtin_amdgcn_raw_buffer_load_b16(ro_, (row * D + col) * 2, 0, 0);
  }
  SB();
#pragma unroll
  for (int i = 0; i < NQ; ++i) {
    v4u o;
#pragma unroll
    for (int p = 0; p < 4; ++p) o[p] = zero[i] ? 0u : (e[i][2 * p] | (e[i][2 * p + 1] << 16));
    st128<SC1>(o, X.rs, dst[i], 0);
  }
  v4u o;
#pragma unroll
  for (int p = 0; p < 4; ++p) o[p] = zo ? 0u : (eo[2 * p] | (eo[2 * p + 1] << 16));
  if (t < 180) st128<SC1>(o, X.rs, OFF_WO + ((j * 90 + u * 5 + tl) * 64 + gq * 16 + m) * 16, 0);
  SB();
}

__device__ __forceinline__ void x0_rows(const Ctx& X) {
  const rsrc_t rx = __builtin_amdgcn_make_buffer_rsrc((void*)X.x, 0, S * D * 2, 0x00020000);
  const int tok = X.lane & 7, q8 = X.lane >> 3;
  v4u v[9];
#pragma unroll
  for (int i = 0; i < 9; ++i) v[i] = ld128<0>(rx, (tok * D + q8 * 8 + i * 64) * 2, (X.g * 64 + X.j * 8) * D * 2);
  SB();
  float s = 0.f, q = 0.f;
#pragma unroll
  for (int i = 0; i < 9; ++i)
#pragma unroll
    for (int p = 0; p < 4; ++p) { s = dot2(v[i][p], ONES, s); q = dot2(v[i][p], v[i][p], q); }
  s += asf(__builtin_amdgcn_mov_dpp(asu(s), 0x128, 0xF, 0xF, true));
  q += asf(__builtin_amdgcn_mov_dpp(asu(q), 0x128, 0xF, 0xF, true));
  sum1632x2(s, q);
  const float mean = s * (1.f / D);
  const float var = fmx(q * (1.f / D) - mean * mean, 0.f);
  const float rstd = rsq(var + 1e-5f), nm = -mean * rstd;
#pragma unroll
  for (int i = 0; i < 9; ++i) {
    v4u o;
#pragma unroll
    for (int p = 0; p < 4; ++p) o[p] = pk2(__builtin_fmaf(bflo(v[i][p]), rstd, nm), __builtin_fmaf(bfhi(v[i][p]), rstd, nm));
    st128<SC1>(o, X.rs, (q8 * 16 + tok) * 16 + i * 2048, OFF_XN + (4 * X.g + (X.j >> 1)) * KS * 1024 + (X.j & 1) * 128);
  }
  SB();
}

__device__ __forceinline__ v4u wq_load(const Ctx& X, int b, int u) {
  const int blk = b < 3 ? b * 4 + X.w : 12 + (X.w >> 1);
  return ld128<SC1>(X.rs, X.lane * 16 + (u & 3) * 1024, OFF_WQ + (X.j * 14 + blk) * KS * 1024 + (u >> 2) * 4096);
}
__device__ __forceinline__ void wq_prefetch(const Ctx& X, v4u (&wq)[RQ][4]) {
#pragma unroll
  for (int u = 0; u < RQ - 1; ++u)
#pragma unroll
    for (int b = 0; b < 4; ++b) wq[u][b] = wq_load(X, b, u);
  SB();
}
__device__ __forceinline__ v4u xn_load(const Ctx& X, int layer, int f) {
  const int u = (f * 205) >> 10, rb5 = f - 5 * u;
  return ld128<SC1>(X.rs, X.lane * 16, OFF_XN + layer * XBUF + ((4 * X.g - 1 + (X.g == 0 && rb5 == 0 ? 1 : rb5)) * KS + u) * 1024);
}

template <int LO, int HIX, int R>
__device__ __forceinline__ void ring_fill(const Ctx& X, int run, v4u (&a)[R]) {
#pragma unroll
  for (int i = LO; i < HIX; ++i) a[i] = ld128<SC1>(X.rs, X.lane * 16 + (i & 3) * 1024, run + (i >> 2) * 4096);
  SB();
}

template <int HI>
__device__ __forceinline__ void stage_a(const Ctx& X, int layer, v4u (&wq)[RQ][4]) {
  LAS unsigned char* L = X.L;
  const rsrc_t rs = X.rs;
  const int g = X.g, j = X.j, w = X.w, lane = X.lane;
  LAS unsigned* cx = (LAS unsigned*)(L + L_CNT + 64);
  const unsigned four = 4 * (layer + 1);
  v4u wt[23];
  if (HI) {
    const int q = w - 4;
    __builtin_amdgcn_s_setprio(2);
    v4u t[18];
#pragma unroll
    for (int i = 0; i < 18; ++i) t[i] = xn_load(X, layer, i < 8 ? cmin(20 + 4 * i + q, 49) : 50 + 4 * (i - 8) + q);
    SB();
#pragma unroll
    for (int i = 0; i < 18; ++i) {
      *(LAS v4u*)(L + A_XT + (i < 8 ? cmin(20 + 4 * i + q, 49) : 50 + 4 * (i - 8) + q) * 1024 + lane * 16) = t[i];
      if (i == 7) LDS_ARRIVE(cx + 1);
    }
    LDS_ARRIVE(cx + 2);
    __builtin_amdgcn_s_setprio(0);
    if (layer == 0) {
#pragma unroll 1
      for (int k = 0; k < 12; ++k) {
        const int task = rt_task(g, k >> 1);
        if (task >= 0) rt_put(X, task, (k & 1) * 4 + q, rt_get(X, k >> 1, (k & 1) * 4 + q));
        __builtin_amdgcn_s_sleep(4);
      }
      LDS_ARRIVE((LAS unsigned*)(L + L_CNT + 52));
    }
    lds_wait(cx + 3, four);
#pragma unroll
    for (int i = 0; i < 23; ++i) wt[i] = ld128<SC1>(rs, lane * 16, OFF_WO + (j * 90 + (i < 22 ? i * 4 + q : 88 + (q & 1))) * 1024);
  } else {
    __builtin_amdgcn_s_setprio(3);
    {
      v4u t[5];
#pragma unroll
      for (int i = 0; i < 5; ++i) t[i] = xn_load(X, layer, 4 * i + w);
      SB();
      if (layer == 0) wq_prefetch(X, wq);
#pragma unroll
      for (int i = 0; i < 5; ++i) *(LAS v4u*)(L + A_XT + (4 * i + w) * 1024 + lane * 16) = t[i];
      LDS_ARRIVE(cx);
    }
    __builtin_amdgcn_s_setprio(3);
    lds_wait(cx, four);
    f32x4 qc[5], kc[5], vc[5], ec[3];
    v4u xf[5];
    const int r1 = (w & 1) ? 3 : 1, r3 = (w & 1) ? 1 : 3;
    const int eoff = w < 2 ? A_EQK + lane * 8 : A_EV + (((lane & 15) >> 2) * 16 + 4 * (lane >> 4)) * 8 + (lane & 3) * 2, estr = w < 2 ? 1 : 4;
    LAS unsigned char* x0 = L + A_XT + lane * 16;
    LAS unsigned char* x1 = x0 + r1 * 1024;
    LAS unsigned char* x3 = x0 + r3 * 1024;
    LAS unsigned char* y0 = x0 + 12 * 5120;
    LAS unsigned char* y1 = x1 + 12 * 5120;
    LAS unsigned char* y3 = x3 + 12 * 5120;
#define XF(r, u) (*(LAS v4u*)(((u) < 12 ? ((r) == 0 ? x0 : (r) < 3 ? x1 : x3) : ((r) == 0 ? y0 : (r) < 3 ? y1 : y3)) + ((u) < 12 ? (u) : (u) - 12) * 5120 + ((r) == 2 || (r) == 4 ? 1024 : 0)))
#pragma unroll
    for (int r = 0; r < 5; ++r) xf[r] = XF(r, 0);
    SB();
    auto put = [&](int r) {
      const int rb5 = r == 0 ? 0 : r < 3 ? r1 + r - 1 : r3 + r - 3;
      if (r > 0) *(LAS v2u*)(L + A_Q + (w * 4 + rb5 - 1) * 512 + lane * 8) = pk4(qc[r]);
      *(LAS v2u*)(L + A_K + (w * 5 + rb5) * 512 + lane * 8) = pk4(kc[r]);
      *(LAS v2u*)(L + A_V + (w * 5 + rb5) * 512 + lane * 8) = pk4(vc[r]);
      if (r < 3) {
        const v2u v = pk4(ec[r]);
        LAS u16* p = (LAS u16*)(L + eoff + rb5 * 512);
        p[0] = (u16)v.x;
        p[estr] = (u16)(v.x >> 16);
        p[2 * estr] = (u16)v.y;
        p[3 * estr] = (u16)(v.y >> 16);
      }
      SB();
    };
#pragma unroll
    for (int u = 0; u < KS; ++u) {
      if (u == 3) lds_wait(cx + 1, four);
      if (u == 9) lds_wait(cx + 2, four);
      if (u == WOK) { if (lane == 0) __hip_atomic_fetch_add(cx + 3, 1u, RLX_WG); SB(); }
      if (u == KS - 1) lds_wait((LAS unsigned*)(L + L_CNT + 52), 4);
#pragma unroll
      for (int r = 0; r < 5; ++r) {
        M16(kc[r], wq[u % RQ][1], xf[r]);
        if (r < 4 && u + RQ - 1 < KS) { wq[(u + RQ - 1) % RQ][r] = wq_load(X, r, u + RQ - 1); SB(); }
        M16(vc[r], xf[r], wq[u % RQ][2]);
        if (r > 0) M16(qc[r], wq[u % RQ][0], xf[r]);
        if (r < 3) M16(ec[r], wq[u % RQ][3], xf[r]);
        if (u + 1 < KS) xf[r] = XF(r, u + 1);
        SB();
        if (u == KS - 1 && r >= 2) put(r - 2);
      }
    }
#undef XF
    put(3);
    MFMA_DONE();
    put(4);
    __builtin_amdgcn_s_setprio(0);
  }
  BAR();
  {
    const int c = lane & 15, gq = lane >> 4, rb = w & 3;
    typedef short s16x4 __attribute__((ext_vector_type(4)));
    auto mk = [](v2u a, v2u b, f32x4 cc) {
      return __builtin_amdgcn_mfma_f32_16x16x16bf16_1k(__builtin_bit_cast(s16x4, a), __builtin_bit_cast(s16x4, b), cc, 0, 0, 0);
    };
    LAS unsigned char* lp = L + rb * 512 + lane * 8;
    const v2u e0 = *(LAS v2u*)(lp + A_EQK), e1 = *(LAS v2u*)(lp + A_EQK + 512);
    v2u qm[2], k0[2], k1[2], v0[2], v1[2];
#pragma unroll
    for (int hh = 0; hh < 2; ++hh) {
      const int h = 2 * HI + hh;
      qm[hh] = *(LAS v2u*)(lp + A_Q + h * 4 * 512);
      k0[hh] = *(LAS v2u*)(lp + A_K + h * 5 * 512);
      k1[hh] = *(LAS v2u*)(lp + A_K + (h * 5 + 1) * 512);
    }
#pragma unroll
    for (int hh = 0; hh < 2; ++hh) {
      const int h = 2 * HI + hh;
      v0[hh] = *(LAS v2u*)(lp + A_V + h * 5 * 512);
      v1[hh] = *(LAS v2u*)(lp + A_V + (h * 5 + 1) * 512);
    }
    const v2u x0 = *(LAS v2u*)(lp + A_EV), x1 = *(LAS v2u*)(lp + A_EV + 512);
    SB();
    const bool first = (g == 0 && rb == 0);
    const bool par = c & 1;
    bool own[2], ok[2];
#pragma unroll
    for (int e = 0; e < 2; ++e) {
      own[e] = 4 * gq + 2 * e <= (c & 14);
      ok[e] = own[e] || !first;
    }
    const f32x4 z = {0, 0, 0, 0};
    f32x4 s0[2], s1[2];
#pragma unroll
    for (int hh = 0; hh < 2; ++hh) {
      s0[hh] = mk(k0[hh], qm[hh], z);
      s1[hh] = mk(k1[hh], qm[hh], z);
    }
#pragma unroll
    for (int hh = 0; hh < 2; ++hh) {
      const v2u qe = {0u, gq == 2 * HI + hh ? e1.x : 0u};
      s0[hh] = mk(e0, qe, s0[hh]);
      s1[hh] = mk(e1, qe, s1[hh]);
    }
    float sv[2][2], m[2], p[2][2], inv[2];
#pragma unroll
    for (int hh = 0; hh < 2; ++hh) {
      const float a0 = s0[hh].x, a1 = s0[hh].y, a2 = s0[hh].z, a3 = s0[hh].w, b0 = s1[hh].x, b1 = s1[hh].y, b2 = s1[hh].z, b3 = s1[hh].w;
      const float l0 = own[0] ? b0 : a0, h0 = own[0] ? b1 : a1, l1 = own[1] ? b2 : a2, h1 = own[1] ? b3 : a3;
      sv[hh][0] = ok[0] ? (par ? h0 : l0) : -3.0e38f;
      sv[hh][1] = ok[1] ? (par ? h1 : l1) : -3.0e38f;
      m[hh] = fmx(sv[hh][0], sv[hh][1]);
    }
    max1632x2(m[0], m[1]);
    constexpr float SC = 0.23570226039551584f * 1.4426950408889634f;
#pragma unroll
    for (int hh = 0; hh < 2; ++hh) {
      p[hh][0] = __builtin_amdgcn_exp2f((sv[hh][0] - m[hh]) * SC);
      p[hh][1] = __builtin_amdgcn_exp2f((sv[hh][1] - m[hh]) * SC);
      inv[hh] = p[hh][0] + p[hh][1];
    }
    sum1632x2(inv[0], inv[1]);
    f32x4 om[2], ox[2];
#pragma unroll
    for (int hh = 0; hh < 2; ++hh) {
      const float r = __builtin_amdgcn_rcpf(inv[hh]);
      const float q0 = p[hh][0] * r, q1 = p[hh][1] * r;
      const unsigned d0 = pk2(par ? 0.f : q0, par ? q0 : 0.f), d1 = pk2(par ? 0.f : q1, par ? q1 : 0.f);
      const v2u pp = {own[0] ? 0u : d0, own[1] ? 0u : d1}, po = {own[0] ? d0 : 0u, own[1] ? d1 : 0u};
      om[hh] = mk(v1[hh], po, mk(v0[hh], pp, z));
      ox[hh] = mk(x1, po, mk(x0, pp, z));
    }
    const int ob = OFF_O + (4 * g + rb) * KS * 1024;
    const v2u a = pk4(om[0]), b = pk4(om[1]);
    st128<SC1>(v4u{a.x, a.y, b.x, b.y}, rs, lane * 16, ob + (2 * j + HI) * 1024);
    if (gq == HI) st64<SC1>(v2u{pk2(ox[0].x, ox[0].y), pk2(ox[1].z, ox[1].w)}, rs, c * 8, ob + 16 * 1024 + (2 * j + HI) * 128);
  }
  if (HI) {
#pragma unroll
    for (int i = 0; i < 23; ++i) *(LAS v4u*)(L + B_W + (i < 22 ? i * 4 + w - 4 : 88 + (w & 1)) * 1024 + lane * 16) = wt[i];
  }
  DRAIN();
  BAR();
  if (HI && w == 7 && lane < 2 - layer) signal(X.ws, lane ? flag_off(F_W, 0, j) + g * 8 : flag_off(F_A, layer, g) + j * 8, X.tag);
}

template <int HI>
__device__ __forceinline__ void stage_b(const Ctx& X, int layer) {
  LAS unsigned char* L = X.L;
  const rsrc_t rs = X.rs;
  const int g = X.g, j = X.j, w = X.w, lane = X.lane;
  constexpr int kh = HI, NK = HI ? 2 : 3;
  const int rb = w & 3, c = lane & 15, gq = lane >> 4;
  const bool poller = HI && w == 7;
  const int xb = OFF_X2 + (4 * g + rb) * KS * 1024;
  const rsrc_t rx = __builtin_amdgcn_make_buffer_rsrc((void*)X.x, 0, S * D * 2, 0x00020000);
  int xo[NK];
  v2u res[NK];
#pragma unroll
  for (int i = 0; i < NK; ++i) {
    const int t = kh * 3 + i, d0 = 72 * j + 16 * t + 4 * gq;
    xo[i] = (d0 >> 5) * 1024 + (((d0 >> 3) & 3) * 16 + c) * 16 + ((d0 >> 2) & 1) * 8;
    res[i] = v2u{0, 0};
    if (t < 4 || gq < 2) {
      if (layer) res[i] = ld64<SC1>(rs, xo[i], xb);
      else res[i] = ld64<0>(rx, (c * D + d0) * 2, (4 * g + rb) * 16 * D * 2);
    }
  }
  SB();
  v4u wf[6][5];
#pragma unroll
  for (int u = 0; u < 6; ++u)
#pragma unroll
    for (int t = 0; t < 5; ++t) wf[u][t] = *(LAS v4u*)(L + B_W + ((kh * 9 + u) * 5 + t) * 1024 + lane * 16);
  SB();
  if (poller) {
    poll(X.ws, flag_off(F_A, layer, g) + (lane & 7) * 8, X.tag);
  }
  BAR();
  v4u of[9];
  const int ob = OFF_O + (4 * g + rb) * KS * 1024;
#pragma unroll
  for (int u = 0; u < (kh ? 7 : 9); ++u) of[u] = ld128<SC1>(rs, lane * 16 + (u & 3) * 1024, ob + kh * 9 * 1024 + (u >> 2) * 4096);
  if (kh) {
#pragma unroll
    for (int u = 7; u < 9; ++u) {
      const v2u p = ld64<SC1>(rs, gq * 256 + c * 8 + (u - 7) * 1024, ob + 16 * 1024);
      const v2u q = ld64<SC1>(rs, gq * 256 + c * 8 + (u - 7) * 1024 + 128, ob + 16 * 1024);
      of[u] = v4u{p.x, p.y, q.x, q.y};
    }
  }
  SB();
  f32x4 acc[5];
#pragma unroll
  for (int t = 0; t < 5; ++t) acc[t] = f32x4{0, 0, 0, 0};
#pragma unroll
  for (int u = 0; u < 9; ++u) {
#pragma unroll
    for (int t = 0; t < 5; ++t) {
      acc[t] = mfma16(wf[u % 6][t], of[u], acc[t]);
      if (u < 3) wf[u][t] = *(LAS v4u*)(L + B_W + ((kh * 9 + u + 6) * 5 + t) * 1024 + lane * 16);
    }
    SB();
  }
#pragma unroll
  for (int t = (kh ? 0 : 3); t < (kh ? 3 : 5); ++t) *(LAS f32x4*)(L + B_RED + (rb * 5 + t) * 1024 + lane * 16) = acc[t];
  BAR();
  const int x1b = OFF_X1 + (4 * g + rb) * KS * 1024;
  float rsum = 0.f, rsq2 = 0.f;
#pragma unroll
  for (int i = 0; i < NK; ++i) {
    const int t = kh * 3 + i;
    const f32x4 o = *(LAS f32x4*)(L + B_RED + (rb * 5 + t) * 1024 + lane * 16);
    f32x4 v = o + acc[t];
    v.x += bflo(res[i].x); v.y += bfhi(res[i].x); v.z += bflo(res[i].y); v.w += bfhi(res[i].y);
    const v2u y = pk4(v);
    if (t < 4 || gq < 2) st64<SC1>(y, rs, xo[i], x1b);
    rsum = dot2(y.x, ONES, dot2(y.y, ONES, rsum));
    rsq2 = dot2(y.x, y.x, dot2(y.y, y.y, rsq2));
  }
  sum1632x2(rsum, rsq2);
  if (lane < 16) st64<SC1>(v2u{asu(rsum), asu(rsq2)}, rs, lane * 8, OFF_RS + ((4 * g + rb) * 16 + 2 * j + kh) * 128);
  DRAIN();
  BAR();
  if (poller) {
    if (lane == 0) signal(X.ws, flag_off(F_B, layer, g) + j * 8, X.tag);
    poll(X.ws, (lane >= 32 && layer == 0) ? flag_off(F_W, 0, j) + (lane & 31) * 8 : flag_off(F_B, layer, g) + (lane & 7) * 8, X.tag);
  }
  BAR();
}

template <int NT, int BASE, int TOTAL, int LEAD, int R, int NB, typename HOOK>
__device__ __forceinline__ void c_gemm(LAS unsigned char* Xl, rsrc_t rs, int run, v4u (&a)[R], f32x4 (&acc)[NT][4], const f32x4 (&c0)[NB], int lane, HOOK hook) {
  constexpr int XB = NT > 2 ? 2 : 3;
  v4u xf[XB][4];
  LAS unsigned char* xp = Xl + lane * 16;
  LAS unsigned char* xq = xp + 2 * KS * 1024;
#define XF(r, u) (*(LAS v4u*)(((r) < 2 ? xp : xq) + (((r) & 1) * KS + (u)) * 1024))
#pragma unroll
  for (int b = 0; b < XB - 1; ++b)
#pragma unroll
    for (int r = 0; r < 4; ++r) xf[b][r] = XF(r, b);
  SB();
#pragma unroll
  for (int u = 0; u < KS; ++u) {
    hook(u);
#pragma unroll
    for (int t = 0; t < NT; ++t) {
      const int idx = BASE + u * NT + t;
#pragma unroll
      for (int r = 3; r >= 0; --r) {
        if (u == 0 && BASE == 0) m16c(acc[t][r], a[idx % R], xf[u % XB][r], c0[t % NB]);
        else if (u == 0) m16z(acc[t][r], a[idx % R], xf[u % XB][r]);
        else m16(acc[t][r], a[idx % R], xf[u % XB][r]);
        if (r >= 2 && t < 2 && u + XB - 1 < KS) { xf[(u + XB - 1) % XB][2 * t + 3 - r] = XF(2 * t + 3 - r, u + XB - 1); SB(); }
      }
      if (idx + LEAD < TOTAL) a[(idx + LEAD) % R] = ld128<SC1>(rs, lane * 16 + ((idx + LEAD) & 3) * 1024, run + ((idx + LEAD) >> 2) * 4096);
      SB();
    }
  }
#undef XF
  MFMA_DONE();
}

template <int HI>
__device__ __forceinline__ void stage_c(const Ctx& X, int layer, int run) {
  v4u a[ring<HI>()];
  constexpr int NT = 5 - HI, R = ring<HI>();
  LAS unsigned char* L = X.L;
  const rsrc_t rs = X.rs;
  const int g = X.g, j = X.j, w = X.w, lane = X.lane;
  const int c = lane & 15, gq = lane >> 4, rb = w & 3;
  const int T0 = HI ? 20 + 4 * (w - 4) : 5 * w;
  LAS unsigned* c_lo = (LAS unsigned*)(L + L_CNT + 20);
  LAS unsigned* c_hi = (LAS unsigned*)(L + L_CNT + 24);
  LAS unsigned* sig = (LAS unsigned*)(L + L_CNT + 32 + rb * 4);
  const unsigned four = 4 * (layer + 1);
  f32x4 bias[NT];
  {
    constexpr int U0 = HI ? XLO : 0, NU = HI ? KS - XLO : XLO;
    __builtin_amdgcn_s_setprio(HI ? 2 : 3);
    const int xb = OFF_X1 + ((4 * g + rb) * KS + U0) * 1024;
    v2u ps[4];
#pragma unroll
    for (int k = 0; k < 4; ++k) ps[k] = ld64<SC1>(rs, gq * 512 + k * 128 + c * 8, OFF_RS + (4 * g + rb) * 2048);
    v4u v[NU];
#pragma unroll
    for (int u = 0; u < NU; ++u) v[u] = ld128<SC1>(rs, lane * 16 + (u & 3) * 1024, xb + (u >> 2) * 4096);
    SB();
    if (!HI) ring_fill<0, CPF>(X, run, a);
#pragma unroll
    for (int t = 0; t < NT; ++t) bias[t] = *(LAS f32x4*)(L + L_CB + ((T0 + t) * 16 + 4 * gq) * 4);
    float s = (asf(ps[0].x) + asf(ps[1].x)) + (asf(ps[2].x) + asf(ps[3].x)), q = (asf(ps[0].y) + asf(ps[1].y)) + (asf(ps[2].y) + asf(ps[3].y));
    sum1632x2(s, q);
    const float mean = s * (1.f / D);
    const float var = fmx(q * (1.f / D) - mean * mean, 0.f);
    const float rstd = rsq(var + 1e-5f), nm = -mean * rstd;
#pragma unroll
    for (int u = 0; u < NU; ++u) {
      v4u o;
#pragma unroll
      for (int p = 0; p < 4; ++p) o[p] = pk2(__builtin_fmaf(bflo(v[u][p]), rstd, nm), __builtin_fmaf(bfhi(v[u][p]), rstd, nm));
      *(LAS v4u*)(L + C_X + (rb * KS + U0 + u) * 1024 + lane * 16) = o;
    }
    LDS_ARRIVE(HI ? c_hi : c_lo);
    ring_fill<HI ? 0 : CPF, CPRE>(X, run, a);
    __builtin_amdgcn_s_setprio(0);
    lds_wait(c_lo, four);
    if (HI) {
      lds_wait(c_hi, four);
      lds_wait(sig, layer + 1);
    }
  }
  {
  f32x4 acc[NT][4];
  c_gemm<NT, 0, 2 * KS * NT, CPRE>(L + C_X, rs, run, a, acc, bias, lane, [&](int u) {
    if (!HI && u == XLO - 1) lds_wait(c_hi, four);
    if (!HI && u == SIGK) { __hip_atomic_store(sig, (unsigned)(layer + 1), RLX_WG); SB(); }
  });
  {
#pragma unroll
    for (int t = 0; t < NT; ++t) {
      const int T = T0 + t;
#pragma unroll
      for (int r = 0; r < 4; ++r) {
        float o[4];
#pragma unroll
        for (int e = 0; e < 4; ++e) {
          const float v = acc[t][r][e];
          const float p = __builtin_fmaf(v * v, -0.10294324f, -2.3022082f);
          const float ex = __builtin_amdgcn_exp2f(v * p);
          o[e] = v * __builtin_amdgcn_rcpf(1.0f + ex);
        }
        *(LAS v2u*)(L + C_H + (r * KS + (T >> 1)) * 1024 + ((2 * (T & 1) + (gq >> 1)) * 16 + c) * 16 + (gq & 1) * 8) = v2u{pk2(o[0], o[1]), pk2(o[2], o[3])};
#pragma unroll
        for (int k = CPRE; k < R; ++k)
          if ((k - CPRE) * (4 * NT) / (R > CPRE ? R - CPRE : 1) == t * 4 + r) {
            const int l = KS * NT + k;
            a[l % R] = ld128<SC1>(rs, lane * 16 + (l & 3) * 1024, run + (l >> 2) * 4096);
          }
        SB();
      }
    }
  }
  }
  if (HI) {
    BAR();
  } else {
    LDS_ARRIVE((LAS unsigned*)(L + L_CNT + 8));
    lds_wait((LAS unsigned*)(L + L_CNT + 8), four);
  }
  auto put = [&](auto& acc, auto np, int T) {
    constexpr int NP = decltype(np)::value;
#pragma unroll
    for (int r = 0; r < 4; ++r) {
      const int cn = 2 * r + (c >> 3), in = (c & 7) * 32 + gq * 8;
      if (cn != j) {
#pragma unroll
        for (int t = 0; t < NP; ++t) st64<SC1>(pk4(acc[t][r]), rs, (cn * 8 + ((j - cn - 1) & 7)) * 9216 + in + t * 256, OFF_P + g * 64 * 9216 + T * 256);
      } else {
#pragma unroll
        for (int t = 0; t < NP; ++t) *(LAS v2u*)(L + C_OWN + (T + t) * 256 + in) = pk4(acc[t][r]);
      }
    }
    SB();
  };
  auto publish = [&](int part) {
    SB();
    asm volatile("s_waitcnt vmcnt(0) lgkmcnt(0)" ::: "memory");
    if (lane == 0) {
      const unsigned old = __hip_atomic_fetch_add((LAS unsigned*)(L + L_CNT + part * 4), 1u, RLX_WG);
      if ((old & 3u) == 3u) signal(X.ws, flag_off(F_C1 + part, layer, g) + j * 8, X.tag);
    }
    SB();
  };
  if (!HI) {
    f32x4 acc[5][4];
    c_gemm<5, KS * NT, 2 * KS * NT, R>(L + C_H, rs, run, a, acc, bias, lane, [&](int u) {
      if (u == 9) BAR();
    });
    put(acc, IC<5>(), 5 * w);
    publish(0);
  } else {
    f32x4 acc[4][4];
    c_gemm<4, KS * NT, 2 * KS * NT, R>(L + C_H, rs, run, a, acc, bias, lane, [&](int u) {});
    put(acc, IC<4>(), 20 + 4 * rb);
    publish(1);
  }
}

template <int HI, int LAYER>
__device__ __forceinline__ void stage_d(const Ctx& X) {
  constexpr int layer = LAYER;
  LAS unsigned char* L = X.L;
  const rsrc_t rs = X.rs;
  const int g = X.g, j = X.j, w = X.w, lane = X.lane;
  const int pb = OFF_P + (g * 8 + j) * 8 * 9216;
  const int RB = 4 * g + (j >> 1);
  const int tok = (lane >> 1) & 7, hf = lane & 1;
  constexpr int I0 = HI ? 2 : 0;
  int fo[3], pp[3];
  v4u xr[3], bv[3], o[3];
  pp[0] = w * 64 + lane;
  pp[1] = 256 + w * 16 + (lane & 15);
  pp[2] = 320 + w * 32 + (lane & 31);
  constexpr int NL[3] = {64, 16, 32};
#pragma unroll
  for (int i = I0; i < 3; ++i) {
    const int T = pp[i] >> 4;
    fo[i] = (T >> 1) * 1024 + ((2 * (T & 1) + hf) * 16 + (j & 1) * 8 + tok) * 16;
    xr[i] = ld128<SC1>(rs, fo[i], OFF_X1 + RB * KS * 1024);
    bv[i] = ld128<0>(X.rb2, (T * 16 + hf * 8) * 2, 0);
  }
  SB();
  float s = 0.f, q = 0.f;
  v4u pv[3][8], own[3];
  auto fetch = [&](int i) {
    const int per = NL[i] / 8, grp = lane / NL[i];
#pragma unroll
    for (int k = 0; k < 8; ++k)
      if (k < per) {
        if (k < per - 1) pv[i][k] = ld128<SC1>(rs, pp[i] * 16 + grp * per * 9216, pb + k * 9216);
        else if (per < 8) pv[i][k] = ld128<SC1>(rs, pp[i] * 16 + (grp * per + (lane < 64 - NL[i] ? k : k - 1)) * 9216, pb);
      }
    own[i] = *(LAS v4u*)(L + C_OWN + pp[i] * 16);
    SB();
  };
  auto finish = [&](int i) {
    const int per = NL[i] / 8;
#pragma unroll
    for (int e = 0; e < 4; ++e) {
      pv[i][per - 1][e] = (per == 8 || lane >= 64 - NL[i]) ? own[i][e] : pv[i][per - 1][e];
      float l2 = 0.f, h2 = 0.f;
#pragma unroll
      for (int k = 0; k < 8; ++k)
        if (k < per) { l2 += bflo(pv[i][k][e]); h2 += bfhi(pv[i][k][e]); }
      if (NL[i] == 32) { l2 = sum32(l2); h2 = sum32(h2); }
      if (NL[i] == 16) sum1632x2(l2, h2);
      o[i][e] = pk2(bflo(bv[i][e]) + bflo(xr[i][e]) + l2, bfhi(bv[i][e]) + bfhi(xr[i][e]) + h2);
    }
    if (lane < NL[i]) {
      if (layer == 0) st128<SC1>(o[i], rs, fo[i], OFF_X2 + RB * KS * 1024);
      else st128<0>(o[i], X.rout, ((g * 64 + j * 8 + tok) * D + (pp[i] >> 4) * 16 + hf * 8) * 2, 0);
      if (layer == 0) {
#pragma unroll
        for (int e = 0; e < 4; ++e) {
          const float lo = bflo(o[i][e]), hi = bfhi(o[i][e]);
          s += lo + hi;
          q = __builtin_fmaf(lo, lo, __builtin_fmaf(hi, hi, q));
        }
      }
    }
    SB();
  };
  if (!HI) {
    poll(X.ws, flag_off(F_C1, layer, g) + (lane & 7) * 8, X.tag);
    fetch(0);
    fetch(1);
    finish(0);
    finish(1);
  }
  if (HI && w == 7) poll(X.ws, flag_off(F_C2, layer, g) + (lane & 7) * 8, X.tag);
  BAR();
  fetch(2);
  finish(2);
  if (layer == 0) {
    s += asf(__builtin_amdgcn_mov_dpp(asu(s), 0xB1, 0xF, 0xF, true));
    q += asf(__builtin_amdgcn_mov_dpp(asu(q), 0xB1, 0xF, 0xF, true));
    sum1632x2(s, q);
    if (lane < 16 && hf == 0) *(LAS f32x2*)(L + D_ST + (tok * 8 + w) * 8) = f32x2{s, q};
    BAR();
    float ts = 0.f, tq = 0.f;
#pragma unroll
    for (int k = 0; k < 4; ++k) {
      const f32x4 v = *(LAS f32x4*)(L + D_ST + tok * 64 + k * 16);
      ts += v.x + v.z;
      tq += v.y + v.w;
    }
    const float mean = ts * (1.f / D);
    const float var = fmx(tq * (1.f / D) - mean * mean, 0.f);
    const float rstd = rsq(var + 1e-5f), nm = -mean * rstd;
#pragma unroll
    for (int i = I0; i < 3; ++i)
      if (lane < NL[i]) {
        v4u y;
#pragma unroll
        for (int e = 0; e < 4; ++e) y[e] = pk2(__builtin_fmaf(bflo(o[i][e]), rstd, nm), __builtin_fmaf(bfhi(o[i][e]), rstd, nm));
        st128<SC1>(y, rs, fo[i], OFF_XN + XBUF + RB * KS * 1024);
      }
    SB();
  }
}

template <int HI>
__device__ __forceinline__ void body(Ctx& X) {
  const int run0 = OFF_WC + (X.j * 1296 + (HI ? 720 + (X.w - 4) * 144 : X.w * 180)) * 1024;
  v4u wq[RQ][4];
  asm volatile("; role %0" ::"i"(HI) : "memory");
#pragma unroll
  for (int u = 0; u < RQ; ++u)
#pragma unroll
    for (int b = 0; b < 4; ++b) wq[u][b] = v4u{0, 0, 0, 0};
#pragma unroll 1
  for (int layer = 0;; ++layer) {
    stage_a<HI>(fresh(X), layer, wq);
    stage_b<HI>(fresh(X), layer);
    int run = run0;
    asm volatile("" : "+s"(run));
    stage_c<HI>(fresh(X), layer, run);
    if (layer == 1) break;
    if (!HI) wq_prefetch(fresh(X), wq);
    stage_d<HI, 0>(fresh(X));
    DRAIN();
    BAR();
    if (HI && X.w == 7) {
      if (X.lane == 0) signal(X.ws, flag_off(F_D, 1, X.g) + X.j * 8, X.tag);
      if (BIDX == 0 && X.lane == 1) signal(X.ws, 0, X.tag);
      poll(X.ws, (X.lane >= 32 && X.g > 0) ? flag_off(F_D, 1, X.g - 1) + (6 + (X.lane & 1)) * 8 : flag_off(F_D, 1, X.g) + (X.lane & 7) * 8, X.tag);
    }
    BAR();
  }
  stage_d<HI, 1>(fresh(X));
}

}

extern "C" __global__ void __launch_bounds__(NTHR, 2) ilk_main(unsigned char* ws, const u16* x, const u16* wqkv, const u16* wo, const u16* w1, const u16* w2,
                                                  const u16* b1, const u16* b2, u16* out) {
  extern __shared__ __attribute__((aligned(16))) unsigned char smem[];
  Ctx X;
  X.ws = ws;
  X.x = x;
  X.wqkv = wqkv;
  X.wo = wo;
  X.w1 = w1;
  X.w2 = w2;
  X.b1 = b1;
  X.L = (LAS unsigned char*)smem;
  X.rs = __builtin_amdgcn_make_buffer_rsrc((void*)ws, 0, WS_BYTES, 0x00020000);
  X.rb2 = __builtin_amdgcn_make_buffer_rsrc((void*)b2, 0, D * 2, 0x00020000);
  X.rout = __builtin_amdgcn_make_buffer_rsrc((void*)out, 0, S * D * 2, 0x00020000);
  X.tag = 0;
  X.lane = TIDX & 63;
  X.w = __builtin_amdgcn_readfirstlane(TIDX >> 6);
  const int xc = BIDX & 7, i = BIDX >> 3;
  X.g = (xc % RA) * (32 / RA) + i / RA;
  X.j = (xc / RA) * RA + i % RA;
  {
    u64 pc;
    asm volatile("s_getpc_b64 s[20:21]\n\ts_add_u32 s20, s20, ilk_main@rel32@lo+4\n\ts_addc_u32 s21, s21, ilk_main@rel32@hi+12\n\ts_mov_b64 %0, s[20:21]"
                 : "=s"(pc) : : "scc", "s20", "s21");
    const v4u c0 = *((const GAS v4u*)pc + TIDX);
    SB();
    const u64 e = __hip_atomic_load((gu64*)X.ws, RLX_AGENT);
    if (TIDX < 32) *(LAS unsigned*)(X.L + L_CNT + TIDX * 4) = 0u;
    BAR();
    unsigned bw = 0;
    if (TIDX < 288) bw = *(const GAS unsigned*)(X.b1 + X.j * 576 + TIDX * 2);
    if (X.w >= 3) __builtin_amdgcn_s_setprio(3);
    if (__builtin_expect(X.w > 3, 1)) qo_tile(X);
    asm volatile("" ::: "memory");
    if (__builtin_expect(X.w == 3, 1)) x0_rows(X);
    asm volatile("" ::: "memory");
    if (X.w >= 3) DRAIN();
    if (__builtin_expect(X.w < 3, 1)) {
      {
        const v4u t = *((const GAS v4u*)pc + ((8 + cmin(i * 3 + X.w, CODE_KB - 9)) * 64 + X.lane));
        asm volatile("" ::"v"(t));
      }
      __builtin_amdgcn_s_sleep(1);
      const int t0 = rt_task(X.g, X.w), t1 = rt_task(X.g, X.w + 3);
      v4u r0[8], r1[8];
      rt_load(X, t0, r0);
#pragma unroll
      for (int k = 0; k < 8; ++k) r1[k] = v4u{0, 0, 0, 0};
      if (t1 >= 0) rt_load(X, t1, r1);
      rt_turn(X, X.w, r0);
      if (t1 >= 0) rt_turn(X, X.w + 3, r1);
    }
    if (TIDX < 288) *(LAS f32x2*)(X.L + L_CB + TIDX * 8) = f32x2{bflo(bw), bfhi(bw)};
    asm volatile("" ::"v"(c0));
    const unsigned elo = __builtin_amdgcn_readfirstlane((unsigned)e), ehi = __builtin_amdgcn_readfirstlane((unsigned)(e >> 32));
    X.tag = (((u64)ehi << 32) | elo) + 0x9E3779B97F4A7C15ull;
    if (X.w >= 3) {
      unsigned old = 0;
      if (X.lane == 0) old = __hip_atomic_fetch_add((LAS unsigned*)(X.L + L_CNT + 12), 1u, RLX_WG);
      if (__builtin_amdgcn_readfirstlane(old) == 4u) {
        if (X.lane < 2) signal(X.ws, X.lane ? flag_off(F_Q, 0, X.j) + X.g * 8 : flag_off(F_D, 0, X.g) + X.j * 8, X.tag);
        poll(X.ws, X.lane >= 32 ? flag_off(F_Q, 0, X.j) + (X.lane & 31) * 8
                                : (X.lane >= 16 && X.g > 0) ? flag_off(F_D, 0, X.g - 1) + (6 + (X.lane & 1)) * 8 : flag_off(F_D, 0, X.g) + (X.lane & 7) * 8, X.tag);
      }
      __builtin_amdgcn_s_setprio(0);
    }
    BAR();
  }
  if (X.w < 4) body<0>(X);
  else body<1>(X);
}
#if defined(__HIP_DEVICE_COMPILE__)
#pragma clang attribute pop
#endif

extern "C" void kernel_launch(void* const* d_in, const int* in_sizes, int n_in, void* d_out, int out_size, void* d_ws, size_t ws_size,
                              hipStream_t stream) {
  (void)hipFuncSetAttribute((const void*)ilk_main, hipFuncAttributeMaxDynamicSharedMemorySize, LDS_BYTES);
  ilk_main<<<256, NTHR, LDS_BYTES, stream>>>((unsigned char*)d_ws, (const u16*)d_in[0], (const u16*)d_in[1], (const u16*)d_in[2], (const u16*)d_in[4],
                                           (const u16*)d_in[6], (const u16*)d_in[5], (const u16*)d_in[7], (u16*)d_out);
}
```
